```python
import torch

def _softmax(x, axis=-1):
    return torch.softmax(x, dim=axis)
def _gelu(x):
    return torch.nn.functional.gelu(x, approximate="tanh")
def _silu(x):
    return torch.nn.functional.silu(x)
def _sigmoid(x):
    return torch.sigmoid(x)
def _layernorm(x, axis=-1, eps=1e-5):
    m = x.mean(dim=axis, keepdim=True)
    v = x.var(dim=axis, keepdim=True, unbiased=False)
    return (x - m) / torch.sqrt(v + eps)
def _scatter_add(tgt, idx, src, axis=0):
    return tgt.clone().index_add_(axis, idx, src)
def _segment_sum(src, seg, n):
    out = torch.zeros((n,) + src.shape[1:], dtype=src.dtype)
    return out.index_add_(0, seg, src)
def _pad(x, pw):
    flat = tuple(v for pair in reversed(pw) for v in pair)
    return torch.nn.functional.pad(x, flat)

kernel_name = 'sequential_preLN__sliding_block__dense_activation_relu__prompt'

def setup_inputs():

    _w = (lambda a: (lambda t: t.to(torch.bfloat16) if t.is_floating_point() else t)(torch.as_tensor(a)))
    def _A_setup():
        B, S, D = 1, 2048, 640
        return {
            "x": _w((lambda *s: torch.randn(*s, dtype=torch.bfloat16) * 0.05)(B, S, D)),
            "wqkv": _w((lambda *s: torch.randn(*s, dtype=torch.bfloat16) * 0.05)(D, 3 * D)),
            "wo": _w((lambda *s: torch.randn(*s, dtype=torch.bfloat16) * 0.05)(D, D)),
        }
    _A_in = _A_setup()

    def _B_setup():
        B, S, D, Dff = 1, 2048, 640, 1728
        return {
            "x": _w((lambda *s: torch.randn(*s, dtype=torch.bfloat16) * 0.05)(B, S, D)),
            "w1": _w((lambda *s: torch.randn(*s, dtype=torch.bfloat16) * 0.05)(D, Dff)),
            "w2": _w((lambda *s: torch.randn(*s, dtype=torch.bfloat16) * 0.05)(Dff, D)),
        }
    _B_in = _B_setup()

    return {
        "A_x": _A_in["x"],
        "A_wqkv": _A_in["wqkv"],
        "A_wo": _A_in["wo"],
        "B_x": _B_in["x"],
        "B_w1": _B_in["w1"],
        "B_w2": _B_in["w2"],
    }

def reference(A_x, A_wqkv, A_wo, B_x, B_w1, B_w2):
    def _A_ref(x, wqkv, wo):
        B, S, D = x.shape
        H = 32; Dh = D // H; W = 256
        qkv = (lambda x, *d: x.permute(*d))((x @ wqkv).reshape(B, S, 3, H, Dh), 2, 0, 3, 1, 4)
        q, k, v = qkv[0], qkv[1], qkv[2]
        scores = torch.einsum("bhqd,bhkd->bhqk", q, k) * (Dh ** -0.5)
        idx = torch.arange(S)
        mask = (idx[:, None] - idx[None, :] >= 0) & (idx[:, None] - idx[None, :] < W)
        scores = torch.where(mask[None, None], scores, -1e9)
        p = _softmax(scores, axis=-1)
        o = torch.einsum("bhqk,bhkd->bhqd", p, v)
        return (lambda x, *d: x.permute(*d))(o, 0, 2, 1, 3).reshape(B, S, D) @ wo

    def _B_ref(x, w1, w2):
        h = torch.einsum("bsd,df->bsf", x, w1)
        zero = torch.zeros_like(h)
        h = torch.maximum(h, zero)
        return torch.einsum("bsf,fd->bsd", h, w2)

    x = A_x
    h = x + _A_ref(_layernorm(x, axis=-1), A_wqkv, A_wo)
    h2 = h + _B_ref(_layernorm(h, axis=-1), B_w1, B_w2)
    return h2
```

```cpp
#include <hip/hip_runtime.h>
#include <hip/hip_bf16.h>

#define GAS __attribute__((address_space(1)))
#define LAS __attribute__((address_space(3)))
#define DI __device__ __forceinline__
typedef unsigned v4u __attribute__((ext_vector_type(4)));
typedef unsigned v2u __attribute__((ext_vector_type(2)));
typedef float f32x2 __attribute__((ext_vector_type(2)));
typedef float f32x4 __attribute__((ext_vector_type(4)));
typedef float f32x16 __attribute__((ext_vector_type(16)));
typedef short bf16x8 __attribute__((ext_vector_type(8)));
typedef short bf16x4 __attribute__((ext_vector_type(4)));
typedef __bf16 bfx2 __attribute__((ext_vector_type(2)));
typedef unsigned long long u64;
typedef GAS u64 gu64;
#define RLX_AGENT __ATOMIC_RELAXED, __HIP_MEMORY_SCOPE_AGENT

namespace {
constexpr int SEQ = 2048, DM = 640, NQKV = 1920, DFF = 1728;
constexpr float LN_EPS = 1e-5f;
constexpr float QSCALE = 0.22360679774997896f * 1.4426950408889634f;

constexpr int FL_STRIDE = 128;
constexpr int WS_EPOCH = 0, WS_TMO = 256, WS_ARR = 1024;
constexpr int WS_F1 = 4096;
constexpr int WS_F2 = WS_F1 + 256 * FL_STRIDE;
constexpr int WS_F3 = WS_F2 + 256 * FL_STRIDE;
constexpr int WS_F4 = WS_F3 + 256 * FL_STRIDE;
constexpr int WS_FP = WS_F4 + 256 * FL_STRIDE;
constexpr int WS_STAMP = WS_FP + 64 * FL_STRIDE;
constexpr int WS_QKV = 1 << 20;
constexpr int WS_O = WS_QKV + 480 * 2048 * 8;
constexpr int HB = 12288;
constexpr int WS_H = WS_O + 32 * 81920;
constexpr int WS_H1 = WS_H + 256 * HB;
constexpr int WS_WOP = WS_H1 + 32 * 8 * 28672;
constexpr int WS_W1P = WS_WOP + 64 * 12800;
constexpr int WS_W2P = WS_W1P + 108 * 20 * 1024;
constexpr int WS_END = WS_W2P + 64 * 35 * 1024;

constexpr int LDS_BYTES = 160 * 1024;
constexpr unsigned SPIN_CAP = 1u << 18;
constexpr int AUX_SC1 = 16;

struct Params {
  const char* x; const char* wqkv; const char* wo; const char* w1; const char* w2;
  char* out; char* ws;
};

DI f32x4 mfma16(v4u a, v4u b, f32x4 c) {
  return __builtin_amdgcn_mfma_f32_16x16x32_bf16(__builtin_bit_cast(bf16x8, a), __builtin_bit_cast(bf16x8, b), c, 0, 0, 0);
}
DI f32x4 mfma16h(v2u a, v2u b, f32x4 c) {
  return __builtin_amdgcn_mfma_f32_16x16x16bf16_1k(__builtin_bit_cast(bf16x4, a), __builtin_bit_cast(bf16x4, b), c, 0, 0, 0);
}
DI f32x16 mfma32(v4u a, v4u b, f32x16 c) {
  return __builtin_amdgcn_mfma_f32_32x32x16_bf16(__builtin_bit_cast(bf16x8, a), __builtin_bit_cast(bf16x8, b), c, 0, 0, 0);
}
DI unsigned pk2(float lo, float hi) {
  f32x2 v = {lo, hi};
  return __builtin_bit_cast(unsigned, __builtin_convertvector(v, bfx2));
}
DI float bflo(unsigned u) { return __builtin_bit_cast(float, u << 16); }
DI float bfhi(unsigned u) { return __builtin_bit_cast(float, u & 0xffff0000u); }

DI float xsum(float v) {
  const unsigned u = __builtin_bit_cast(unsigned, v);
  const auto a = __builtin_amdgcn_permlane32_swap(u, u, false, false);
  const float s = __builtin_bit_cast(float, (unsigned)a[0]) + __builtin_bit_cast(float, (unsigned)a[1]);
  const unsigned w = __builtin_bit_cast(unsigned, s);
  const auto c = __builtin_amdgcn_permlane16_swap(w, w, false, false);
  return __builtin_bit_cast(float, (unsigned)c[0]) + __builtin_bit_cast(float, (unsigned)c[1]);
}

DI void glds16(const char* g, unsigned lds_base) {
  unsigned sv;
  asm volatile("s_mov_b32 %0, m0\n\ts_mov_b32 m0, %2\n\ts_nop 0\n\tglobal_load_lds_dwordx4 %1, off\n\ts_mov_b32 m0, %0"
               : "=&s"(sv) : "v"(g), "s"(lds_base) : "memory");
}
DI void glds16_sc1(const char* g, unsigned lds_base) {
  unsigned sv;
  asm volatile("s_mov_b32 %0, m0\n\ts_mov_b32 m0, %2\n\ts_nop 0\n\tglobal_load_lds_dwordx4 %1, off sc1\n\ts_mov_b32 m0, %0"
               : "=&s"(sv) : "v"(g), "s"(lds_base) : "memory");
}
DI void touch4(const char* g, unsigned lds_base) {
  unsigned sv;
  asm volatile("s_mov_b32 %0, m0\n\ts_mov_b32 m0, %2\n\ts_nop 0\n\tglobal_load_lds_dword %1, off sc1\n\ts_mov_b32 m0, %0"
               : "=&s"(sv) : "v"(g), "s"(lds_base) : "memory");
}
#define WAITV(N) asm volatile("s_waitcnt vmcnt(" #N ")" ::: "memory")
#define WAIT_BAR(N) asm volatile("s_waitcnt vmcnt(" #N ") lgkmcnt(0)\n\ts_barrier" ::: "memory")
#define LBAR() asm volatile("s_waitcnt lgkmcnt(0)\n\ts_barrier" ::: "memory")
#define SBAR() __builtin_amdgcn_sched_barrier(0)
#define PIN(x) asm volatile("" : "+v"(x))

typedef LAS const char* lptr;
DI v4u lds128(lptr p) { return *(const LAS v4u*)p; }
DI v2u lds64(lptr p) { return *(const volatile LAS v2u*)p; }
DI v2u ldstr(lptr p) { return __builtin_bit_cast(v2u, __builtin_amdgcn_ds_read_tr16_b64_v4i16((LAS bf16x4*)p)); }

DI v4u gld128(__amdgpu_buffer_rsrc_t rs, int off) { return __builtin_amdgcn_raw_buffer_load_b128(rs, off, 0, AUX_SC1); }
DI v2u gld64(__amdgpu_buffer_rsrc_t rs, int off) { return __builtin_amdgcn_raw_buffer_load_b64(rs, off, 0, AUX_SC1); }
DI void gst128(__amdgpu_buffer_rsrc_t rs, int off, v4u v) { __builtin_amdgcn_raw_buffer_store_b128(v, rs, off, 0, AUX_SC1); }
DI void gst64(__amdgpu_buffer_rsrc_t rs, int off, v2u v) { __builtin_amdgcn_raw_buffer_store_b64(v, rs, off, 0, AUX_SC1); }

DI void poll_flags(char* ws, int off, bool want, u64 tag) {
  gu64* f = (gu64*)(ws + off);
  for (unsigned spins = 0;;) {
    u64 v = tag;
    if (want) v = __hip_atomic_load(f, RLX_AGENT);
    if (__all(v == tag)) break;
    if (++spins > SPIN_CAP) { __hip_atomic_store((gu64*)(ws + WS_TMO), tag, RLX_AGENT); break; }
    if ((spins & 1023u) == 0u && __builtin_amdgcn_readfirstlane((unsigned)(__hip_atomic_load((gu64*)(ws + WS_TMO), RLX_AGENT) == tag))) break;
  }
  __builtin_amdgcn_fence(__ATOMIC_ACQUIRE, "wavefront");
}
DI void set_flag(char* ws, int off, u64 tag) { __hip_atomic_store((gu64*)(ws + off), tag, RLX_AGENT); }

#define STAMP(i) do {} while (0)
#define STAMP_DUMP() do {} while (0)

DI void pack_issue(const Params& p, int u, unsigned lds, int lane) {
  const char* m; int r0, r1, pitch, cg;
  if (u < 240) {
    const int ru = u / 10, w = ru / 3, part = ru - 3 * w;
    cg = u - 10 * ru; m = p.wo; pitch = 1280; r0 = 80 * w + 32 * part; r1 = part == 2 ? r0 : r0 + 16;
  } else if (u < 780) {
    const int v = u - 240, ks = v / 27;
    cg = v - 27 * ks; m = p.w1; pitch = 3456;
    if (ks < 16) { r0 = 80 * (ks >> 1) + 32 * (ks & 1); r1 = r0 + 16; } else { r0 = 160 * (ks - 16) + 64; r1 = r0 + 80; }
  } else {
    const int v = u - 780, ks = v / 10;
    cg = v - 10 * ks; m = p.w2; pitch = 1280; r0 = 32 * ks; r1 = r0 + 16;
  }
  const int lr = lane >> 3, c = (lane & 7) ^ (((lr >> 1) & 3) << 1);
  const char* s0 = m + (size_t)((r0 + lr) * pitch + cg * 128 + c * 16);
  const char* s1 = m + (size_t)((r1 + lr) * pitch + cg * 128 + c * 16);
  glds16(s0, lds); glds16(s0 + 8 * pitch, lds + 1024); glds16(s1, lds + 2048); glds16(s1 + 8 * pitch, lds + 3072);
}

DI void pack_finish(__amdgpu_buffer_rsrc_t rs, lptr L, int u, int lane) {
  const int row = 4 * (lane >> 4) + ((lane & 15) >> 2);
  const int a0 = row * 128 + (lane & 3) * 8, sw = (row >> 1) & 3;
  v2u lo[4], hi[4];
#pragma unroll
  for (int t = 0; t < 4; ++t) { lo[t] = ldstr(L + a0 + ((t ^ sw) * 32)); hi[t] = ldstr(L + a0 + ((t ^ sw) * 32) + 2048); }
  int T0, base, sj, sn; bool half = false;
  if (u < 240) {
    const int ru = u / 10, w = ru / 3, part = ru - 3 * w;
    T0 = 4 * (u - 10 * ru); base = WS_WOP + w * 12800 + part * 1024; sj = 8 * 12800; sn = 2560; half = part == 2;
  } else if (u < 780) {
    const int v = u - 240, ks = v / 27;
    T0 = 4 * (v - 27 * ks); base = WS_W1P + ks * 1024; sj = 5 * 20480; sn = 20480;
  } else {
    const int v = u - 780, ru = v / 10;
    const int w = ru < 42 ? ru / 7 : 6 + (ru - 42) / 6, ks = ru < 42 ? ru - 7 * w : ru - 42 - 6 * (w - 6);
    T0 = 4 * (v - 10 * ru); base = WS_W2P + w * 35840 + ks * 5120; sj = 8 * 35840; sn = 1024;
  }
#pragma unroll
  for (int t = 0; t < 4; ++t) {
    const int T = T0 + t, jj = T / 5, nt = T - 5 * jj;
    const int d = base + jj * sj + nt * sn;
    if (half) gst64(rs, d + lane * 8, lo[t]);
    else gst128(rs, d + lane * 16, v4u{lo[t].x, lo[t].y, hi[t].x, hi[t].y});
  }
}

__global__ void __launch_bounds__(512, 2) block_kernel(Params p) {
  extern __shared__ __attribute__((aligned(16))) char smem[];
  const int tid = threadIdx.x, lane = tid & 63, w = __builtin_amdgcn_readfirstlane(tid >> 6);
  const int tok = lane & 15, g = lane >> 4;
  const int b = blockIdx.x, xcd = b & 7, t = b >> 3;
  const unsigned lds0 = (unsigned)(size_t)smem;
  lptr L = (lptr)smem;
  const __amdgpu_buffer_rsrc_t rs = __builtin_amdgcn_make_buffer_rsrc(p.ws, 0, WS_END, 0x00020000);
  STAMP(0);
  const u64 e0 = __hip_atomic_load((gu64*)(p.ws + WS_EPOCH), RLX_AGENT);
  const v4u ONES = {0x3F803F80u, 0x3F803F80u, 0x3F803F80u, 0x3F803F80u};
  u64 tag;

  {
    const int P128 = 4 * (xcd & 3) + (t & 3), s = (xcd >> 2) * 8 + (t >> 2);
    if (s < 15) {
      LAS f32x2* st = (LAS f32x2*)(smem + 131072);
      LAS float* cs = (LAS float*)(smem + 132096);
      const int q4 = (lane & 15) >> 2, pp = lane & 3;
      const int fsw = q4 | ((g & 1) << 2);
      const int xlane = tok * 128 + ((g ^ (tok >> 1)) * 16);
      const int wlane = 16384 + (8 * g + q4) * 256 + pp * 8;
      if (w >= 4) {
        const int a = w - 4, lt = tid - 256;
        const int rot = (t * 3 + xcd) % 10;
        const char* xs; const char* wsrc;
        { const int row = lt >> 3, c = (lt & 7) ^ ((row >> 1) & 7); xs = p.x + (size_t)((P128 * 128 + row) * 1280 + c * 16); }
        { const int row = lt >> 4, f = (row & 3) | (((row >> 3) & 1) << 2), c = (lt & 15) ^ (f << 1); wsrc = p.wqkv + (size_t)(row * 3840 + s * 256 + c * 16); }
        auto stage = [&](int i) {
          int kc = i + rot; if (kc >= 10) kc -= 10;
          const unsigned d = lds0 + (i & 3) * 32768 + a * 1024;
          const char* xk = xs + kc * 128; const char* wk = wsrc + (size_t)(kc * (64 * 3840));
#pragma unroll
          for (int q = 0; q < 4; ++q) { glds16(wk + q * (16 * 3840), d + 16384 + q * 4096); glds16(xk + q * (32 * 1280), d + q * 4096); }
        };
        stage(0); stage(1); stage(2);
        f32x4 accC[2], accS[2], accG[2];
#pragma unroll
        for (int i = 0; i < 2; ++i) accC[i] = accS[i] = accG[i] = f32x4{0, 0, 0, 0};
        const int wo0 = wlane + (((2 * a) ^ fsw) * 32);
#pragma unroll
        for (int i = 0; i < 10; ++i) {
          if (i < 8) WAIT_BAR(16); else if (i == 8) WAIT_BAR(8); else WAIT_BAR(0);
          if (i + 3 < 10) stage(i + 3);
          SBAR();
          lptr S = L + (i & 3) * 32768;
          v4u xb[2][2], wa[2][2];
#pragma unroll
          for (int k2 = 0; k2 < 2; ++k2)
#pragma unroll
            for (int e = 0; e < 2; ++e) {
              xb[k2][e] = lds128(S + (2 * a + e) * 2048 + (xlane ^ (k2 * 64)));
              const v2u lo = ldstr(S + (wo0 ^ (e * 32)) + k2 * 8192), hi = ldstr(S + (wo0 ^ (e * 32)) + k2 * 8192 + 1024);
              wa[k2][e] = v4u{lo.x, lo.y, hi.x, hi.y};
            }
          SBAR();
#pragma unroll
          for (int k2 = 0; k2 < 2; ++k2)
#pragma unroll
            for (int e = 0; e < 2; ++e) {
              accC[e] = mfma16(wa[k2][e], ONES, accC[e]);
              accS[e] = mfma16(ONES, xb[k2][e], accS[e]);
              accG[e] = mfma16(xb[k2][e], xb[k2][e], accG[e]);
            }
          SBAR();
        }
        tag = (((u64)__builtin_amdgcn_readfirstlane((unsigned)(e0 >> 32)) << 32) | __builtin_amdgcn_readfirstlane((unsigned)e0)) + 0x9E3779B97F4A7C15ull;
        const int r = tok & 3;
#pragma unroll
        for (int e = 0; e < 2; ++e) {
          const float G = r == 0 ? accG[e][0] : r == 1 ? accG[e][1] : r == 2 ? accG[e][2] : accG[e][3];
          const float mean = accS[e][0] * (1.0f / DM);
          const float var = fmaxf(G * (1.0f / DM) - mean * mean, 0.f);
          if (g == (tok >> 2)) st[(2 * a + e) * 16 + tok] = f32x2{mean, 1.0f / sqrtf(var + LN_EPS)};
          if (tok == 0) *(LAS f32x4*)(cs + (2 * a + e) * 16 + 4 * g) = accC[e];
        }
        LBAR();
      } else {
        const int mh = w >> 1, nh = w & 1;
        const int xo = mh * 8192 + xlane;
        const int wo0 = wlane + (((4 * nh) ^ fsw) * 32);
        f32x4 acc[4][4];
#pragma unroll
        for (int m = 0; m < 4; ++m)
#pragma unroll
          for (int n = 0; n < 4; ++n) acc[m][n] = f32x4{0, 0, 0, 0};
        v4u xb[2][4], wa[2][4];
        auto rdfrag = [&](int i, int k2) {
          lptr S = L + (i & 3) * 32768;
#pragma unroll
          for (int n = 0; n < 4; ++n) {
            const v2u lo = ldstr(S + (wo0 ^ (n * 32)) + k2 * 8192), hi = ldstr(S + (wo0 ^ (n * 32)) + k2 * 8192 + 1024);
            wa[k2][n] = v4u{lo.x, lo.y, hi.x, hi.y};
            xb[k2][n] = lds128(S + n * 2048 + (xo ^ (k2 * 64)));
          }
          SBAR();
        };
        auto mm = [&](int k2) {
#pragma unroll
          for (int m = 0; m < 4; ++m)
#pragma unroll
            for (int n = 0; n < 4; ++n) acc[m][n] = mfma16(wa[k2][n], xb[k2][m], acc[m][n]);
          SBAR();
        };
        const int pu = 384 + (P128 * 15 + s) * 4 + w;
        const bool packer = pu < 1320;
        tag = (((u64)__builtin_amdgcn_readfirstlane((unsigned)(e0 >> 32)) << 32) | __builtin_amdgcn_readfirstlane((unsigned)e0)) + 0x9E3779B97F4A7C15ull;
#pragma unroll
        for (int i = 0; i < 10; ++i) {
          if (i == 8) WAIT_BAR(0); else LBAR();
          if (i == 8 && tid == 0) set_flag(p.ws, WS_ARR + b * 8, tag);
          STAMP(16 + i);
          rdfrag(i, 0);
          rdfrag(i, 1);
          mm(0);
          if (i == 1 && packer) pack_issue(p, pu, lds0 + 133120 + w * 4096, lane);
          if (i == 5 && packer) { WAITV(0); pack_finish(rs, L + 133120 + w * 4096, pu, lane); }
          mm(1);
        }
        STAMP(1);
        LBAR();
        const float qs = s < 5 ? QSCALE : 1.0f;
        f32x4 c4[4];
#pragma unroll
        for (int n = 0; n < 4; ++n) c4[n] = *(const LAS f32x4*)(cs + (4 * nh + n) * 16 + 4 * g);
        const bool odd = tok & 1;
#pragma unroll
        for (int m = 0; m < 4; ++m) {
          const int mt = 4 * mh + m;
          const f32x2 ms = st[mt * 16 + tok];
          const float sc = ms.y * qs;
#pragma unroll
          for (int n2 = 0; n2 < 2; ++n2) {
            unsigned pk[2][2];
#pragma unroll
            for (int e = 0; e < 2; ++e) {
              const f32x4 a4 = acc[m][2 * n2 + e], cc = c4[2 * n2 + e];
              pk[e][0] = pk2((a4[0] - ms.x * cc[0]) * sc, (a4[1] - ms.x * cc[1]) * sc);
              pk[e][1] = pk2((a4[2] - ms.x * cc[2]) * sc, (a4[3] - ms.x * cc[3]) * sc);
            }
            const unsigned s0 = odd ? pk[0][0] : pk[1][0], s1 = odd ? pk[0][1] : pk[1][1];
            const unsigned r0 = (unsigned)__builtin_amdgcn_mov_dpp((int)s0, 0xB1, 0xF, 0xF, true);
            const unsigned r1 = (unsigned)__builtin_amdgcn_mov_dpp((int)s1, 0xB1, 0xF, 0xF, true);
            const v4u v = odd ? v4u{r0, r1, pk[1][0], pk[1][1]} : v4u{pk[0][0], pk[0][1], r0, r1};
            const int quad = 32 * s + 4 * (4 * nh + 2 * n2 + (odd ? 1 : 0)) + g;
            gst128(rs, WS_QKV + (quad * 2048 + P128 * 128 + mt * 16 + (tok & 14)) * 8, v);
          }
        }
      }
      WAIT_BAR(0);
      if (tid == 0) set_flag(p.ws, WS_F1 + (P128 * 15 + s) * FL_STRIDE, tag);
    } else {
      const int pu = ((xcd - 4) * 4 + (t - 28)) * 24 + w * 3;
#pragma unroll 1
      for (int i = 0; i < 3; ++i) pack_issue(p, pu + i, lds0 + (w * 3 + i) * 4096, lane);
      tag = (((u64)__builtin_amdgcn_readfirstlane((unsigned)(e0 >> 32)) << 32) | __builtin_amdgcn_readfirstlane((unsigned)e0)) + 0x9E3779B97F4A7C15ull;
      WAITV(0);
#pragma unroll 1
      for (int i = 0; i < 3; ++i) pack_finish(rs, L + (w * 3 + i) * 4096, pu + i, lane);
      WAIT_BAR(0);
      if (tid == 0) set_flag(p.ws, WS_ARR + b * 8, tag);
    }
  }
  STAMP(2);

  const int pg = xcd * 4 + (t & 3), jg = t >> 2;
  auto touch_w2 = [&](int part) {
    const char* a = p.ws + (size_t)(WS_W2P + (t * 8 + w) * 8960 + (part * 24 + lane) * 128);
    if (lane < (part < 2 ? 24 : 22)) touch4(a, lds0 + LDS_BYTES - 256);
  };
  v4u wo[5][3];
  v2u xr[2][2];
  const int ntile = jg < 6 ? 14 : 12, tbase = jg < 7 ? 14 * jg : 96;
  const bool gemm = 2 * w < ntile;
  constexpr int W1E = 8;
  constexpr int W2L = 7;
  constexpr int W2LOFF = 8 * 11776 + 1024;
  v4u w1f[20][2];
  const int w1base = gemm ? WS_W1P + (tbase + 2 * w) * 20480 + lane * 16 : 0x7F000000;
  const int w2base = WS_W2P + (jg * 8 + w) * 35840;
  {
    constexpr int PL = 2592, QOFF = 40 * PL, ONESOFF = QOFF + 20 * 512;
    const int pn = t, j = xcd;
    const int tok0 = 64 * pn - 256;
    const int wobase = WS_WOP + (jg * 8 + w) * 12800;
    auto wo_loads = [&]() {
#pragma unroll
      for (int i = 0; i < 5; ++i) {
        wo[i][0] = gld128(rs, wobase + i * 2560 + lane * 16);
        wo[i][1] = gld128(rs, wobase + i * 2560 + 1024 + lane * 16);
        wo[i][2] = gld128(rs, wobase + i * 2560 + 2048 + lane * 8);
      }
#pragma unroll
      for (int hp = 0; hp < 2; ++hp) {
        int u = w - 2 * hp; u = u < 0 ? 0 : (u > 5 ? 5 : u);
        const int mt = 2 * hp + u / 3, part = u % 3;
        const char* src = p.x + (size_t)((64 * pg + 16 * mt + tok) * 1280 + (80 * jg + 32 * part + 4 * g) * 2);
        xr[hp][0] = *(const GAS v2u*)src;
        xr[hp][1] = *(const GAS v2u*)(src + (part < 2 ? 32 : 0));
      }
      SBAR();
    };
    if (w == 1) {
#pragma unroll 1
      for (int i = 0; i < 4; ++i) poll_flags(p.ws, WS_ARR + (i * 64 + lane) * 8, true, tag);
    } else if (w >= 2) {
      if (tid < 128 + 352) *(LAS v2u*)(smem + ONESOFF + (tid - 128) * 8) = v2u{0x00003F80u, 0u};
      if (pn < 4) {
        const int n16 = -tok0 / 2;
#pragma unroll 1
        for (int i = tid - 128; i < 40 * n16; i += 384) { const int pl = i / n16; *(LAS v4u*)(smem + pl * PL + (i - pl * n16) * 16) = v4u{0u, 0u, 0u, 0u}; }
      }
    }
    LBAR();
    if (w == 0) {
      const int R = lane / 6, rem = lane % 6, pi = rem >> 1;
      const int q0 = 160 * R + 20 * j, sl = (rem & 1) ? (q0 + 19) >> 5 : q0 >> 5;
      const int P = (pn >> 1) - pi;
      const bool want = lane < 18 && P >= 0 && (R > 0 || pi == 0);
      poll_flags(p.ws, WS_F1 + ((want ? P : 0) * 15 + (want ? sl : 0)) * FL_STRIDE, want, tag);
    } else {
      wo_loads();
    }
    LBAR();
    STAMP(3);
#pragma unroll
    for (int i = 0; i < 5; ++i) {
      const int pl = 5 * w + i;
      const int quad = (pl < 20 ? 160 + pl : 300 + pl) + 20 * j;
      const char* src = p.ws + (size_t)(WS_QKV + (quad * 2048 + tok0) * 8 + lane * 16);
#pragma unroll
      for (int c = 0; c < 3; ++c) {
        const int tk = tok0 + c * 128 + lane * 2;
        if (tk >= 0 && (c < 2 || lane < 32)) glds16_sc1(src + c * 1024, lds0 + pl * PL + c * 1024);
      }
    }
    if (w < 5) {
#pragma unroll
      for (int i = 0; i < 2; ++i) {
        const int pl = 4 * w + 2 * i + (lane >> 5);
        const char* src = p.ws + (size_t)(WS_QKV + ((20 * j + pl) * 2048 + 64 * pn) * 8 + (lane & 31) * 16);
        glds16_sc1(src, lds0 + QOFF + (4 * w + 2 * i) * 512);
      }
    }
    WAIT_BAR(0);
    STAMP(4);
    if (w == 0) wo_loads();
    auto pre = [&](int i) {
      const char* a = p.ws + (size_t)(WS_W1P + (t * 8 + w) * 8640 + (i * 34 + lane) * 128);
      if (lane < 34) touch4(a, lds0 + LDS_BYTES - 256);
    };
    const int hl = w >> 1, qh = w & 1, r32 = lane & 31, h = lane >> 5;
    const int bmin = 8 - 2 * pn - qh;
    v4u qf0, qf1;
    {
      const v2u a = lds64(L + QOFF + (5 * hl + 2 * h) * 512 + (32 * qh + r32) * 8), c = lds64(L + QOFF + (5 * hl + 2 * h + 1) * 512 + (32 * qh + r32) * 8);
      qf0 = v4u{a.x, a.y, c.x, c.y};
      const v2u d = lds64(L + QOFF + (5 * hl + 4) * 512 + (32 * qh + r32) * 8);
      qf1 = h ? v4u{0u, 0u, 0u, 0u} : v4u{d.x, d.y, 0x00003F80u, 0u};
    }
    lptr kp0 = L + (5 * hl + 2 * h) * PL + (32 * qh + r32) * 8;
    lptr kp1 = L + (5 * hl + 4) * PL + (32 * qh + r32) * 8;
    auto kfrag = [&](int blk, v4u (&kf)[2]) {
      const v2u a = lds64(kp0 + blk * 256), d = lds64(kp0 + PL + blk * 256), e = lds64(kp1 + blk * 256);
      kf[0] = v4u{a.x, a.y, d.x, d.y};
      kf[1] = v4u{e.x, e.y, blk >= bmin ? 0u : 0x0000F149u  , 0u};
    };
    auto scores = [&](const v4u (&kf)[2], f32x16 c) -> f32x16 { return mfma32(kf[1], qf1, mfma32(kf[0], qf0, c)); };
    f32x16 zero16;
#pragma unroll
    for (int r = 0; r < 16; ++r) zero16[r] = 0.f;
    float m;
    {
      v4u kf[9][2];
      kfrag(8, kf[8]);
#pragma unroll
      for (int blk = 0; blk < 8; ++blk) kfrag(blk, kf[blk]);
      SBAR();
      pre(0);
      const f32x16 s8 = scores(kf[8], zero16), s0 = scores(kf[0], zero16);
      f32x16 sn = scores(kf[1], zero16);
      SBAR();
      float m0 = -3.0e38f, m1 = -3.0e38f;
#pragma unroll
      for (int r = 0; r < 16; r += 2) {
        const int kk = (r & 3) + 8 * (r >> 2) + 4 * h;
        m0 = fmaxf(fmaxf(m0, kk <= r32 ? s8[r] : s0[r]), kk + 1 <= r32 ? s8[r + 1] : s0[r + 1]);
      }
      PIN(m0);
#pragma unroll
      for (int blk = 1; blk < 8; ++blk) {
        const f32x16 sb = sn;
        SBAR();
        if (blk < 7) sn = scores(kf[blk + 1], zero16);
        SBAR();
#pragma unroll
        for (int r = 0; r < 16; r += 4) { m0 = fmaxf(fmaxf(m0, sb[r]), sb[r + 1]); m1 = fmaxf(fmaxf(m1, sb[r + 2]), sb[r + 3]); }
        PIN(m0); PIN(m1);
      }
      m = fmaxf(m0, m1);
      const unsigned mu = __builtin_bit_cast(unsigned, m);
      const auto sw = __builtin_amdgcn_permlane32_swap(mu, mu, false, false);
      m = fmaxf(__builtin_bit_cast(float, (unsigned)sw[0]), __builtin_bit_cast(float, (unsigned)sw[1]));
    }
    STAMP(13);
    pre(1);
    asm volatile("" ::: "memory");
    f32x16 negm;
#pragma unroll
    for (int r = 0; r < 16; ++r) negm[r] = -m;
    const int blk16 = (lane >> 4) & 1, q4 = (lane & 15) >> 2, pp = lane & 3;
    const int vplane = (blk16 == 0) ? (20 + 5 * hl + pp) * PL : (pp == 0 ? (24 + 5 * hl) * PL : ONESOFF + (((20 + 5 * hl) * 32 + 160) & 255));
    lptr vp = L + vplane + (32 * qh + 4 * h + q4) * 8;
    f32x16 o = zero16;
    auto vfrag = [&](int blk, v4u (&vf)[2]) {
#pragma unroll
      for (int s2 = 0; s2 < 2; ++s2) {
        const v2u a = ldstr(vp + blk * 256 + s2 * 128), c = ldstr(vp + blk * 256 + s2 * 128 + 64);
        vf[s2] = v4u{a.x, a.y, c.x, c.y};
      }
    };
    auto pv = [&](const v4u (&vf)[2], const unsigned (&pk)[8]) {
      o = mfma32(vf[0], v4u{pk[0], pk[1], pk[2], pk[3]}, o);
      o = mfma32(vf[1], v4u{pk[4], pk[5], pk[6], pk[7]}, o);
    };
    {
      v4u k8[2], k0[2], kn[2][2], v8[2], v0[2];
      kfrag(8, k8); kfrag(0, k0); kfrag(1, kn[1]); kfrag(2, kn[0]);
      vfrag(8, v8); vfrag(0, v0);
      SBAR();
      const f32x16 sa = scores(k8, negm), sb = scores(k0, negm);
      f32x16 sn = scores(kn[1], negm);
      SBAR();
      unsigned p8[8], p0[8];
#pragma unroll
      for (int r = 0; r < 16; r += 2) {
        const int kk = (r & 3) + 8 * (r >> 2) + 4 * h;
        const float e0v = __builtin_amdgcn_exp2f(kk <= r32 ? sa[r] : sb[r]), e1v = __builtin_amdgcn_exp2f(kk + 1 <= r32 ? sa[r + 1] : sb[r + 1]);
        const unsigned pkd = pk2(e0v, e1v);
        const unsigned msk = kk + 1 <= r32 ? 0xffffffffu : (kk <= r32 ? 0x0000ffffu : 0u);
        p8[r >> 1] = pkd & msk; p0[r >> 1] = pkd & ~msk;
        PIN(p8[r >> 1]); PIN(p0[r >> 1]);
      }
      SBAR();
      pv(v8, p8);
      pv(v0, p0);
#pragma unroll
      for (int blk = 1; blk < 8; ++blk) {
        const f32x16 sc = sn;
        v4u vf[2];
        SBAR();
        if (blk < 7) sn = scores(kn[(blk + 1) & 1], negm);
        vfrag(blk, vf);
        if (blk < 6) kfrag(blk + 2, kn[blk & 1]);
        SBAR();
        if (blk == 3) touch_w2(0);
        unsigned pk[8];
#pragma unroll
        for (int r = 0; r < 16; r += 2) { pk[r >> 1] = pk2(__builtin_amdgcn_exp2f(sc[r]), __builtin_amdgcn_exp2f(sc[r + 1])); PIN(pk[r >> 1]); }
        SBAR();
        pv(vf, pk);
      }
    }
    STAMP(14);
    {
      const float den = __shfl(o[8], r32 + 32);
      const float inv = 1.0f / den;
      const int mt = 2 * qh + (r32 >> 4), tk = r32 & 15;
      const int obase = WS_O + ((pn * 8 + j) * 4 + mt) * 2560;
#pragma unroll
      for (int c = 0; c < 3; ++c) {
        const int Qd = 5 * hl + (c < 2 ? 2 * c + h : 4), nt = Qd >> 2, gg = Qd & 3;
        const int off = nt < 4 ? (nt >> 1) * 1024 + (tk + 16 * gg) * 16 + (nt & 1) * 8 : 2048 + (tk + 16 * gg) * 8;
        const v2u v = {pk2(o[4 * c] * inv, o[4 * c + 1] * inv), pk2(o[4 * c + 2] * inv, o[4 * c + 3] * inv)};
        if (c < 2 || h == 0) gst64(rs, obase + off, v);
      }
    }
    WAIT_BAR(0);
    if (tid == 0) set_flag(p.ws, WS_F2 + (pn * 8 + j) * FL_STRIDE, tag);
    if (w != 7) touch_w2(1);
  }
  const int pn = pg, j = jg;
  STAMP(5);

  v4u hres[2];
  hres[0] = hres[1] = v4u{0u, 0u, 0u, 0u};
  auto red_write = [&](const f32x4 (&acc)[2][5]) {
#pragma unroll
    for (int ml = 0; ml < 2; ++ml)
#pragma unroll
      for (int nt = 0; nt < 5; ++nt) *(LAS f32x4*)(smem + ((w * 10 + ml * 5 + nt) * 64 + lane) * 16) = acc[ml][nt];
  };
  auto red_sum = [&](int u, f32x4& s0, f32x4& s1) {
    const int ml = u / 3, part = u % 3;
    const int t0 = ml * 5 + 2 * part, t1 = part < 2 ? t0 + 1 : t0;
    f32x4 r0[4], r1[4];
#pragma unroll
    for (int ww = 0; ww < 4; ++ww) { r0[ww] = *(const LAS f32x4*)(smem + ((ww * 10 + t0) * 64 + lane) * 16); r1[ww] = *(const LAS f32x4*)(smem + ((ww * 10 + t1) * 64 + lane) * 16); }
    s0 = (r0[0] + r0[1]) + (r0[2] + r0[3]); s1 = (r1[0] + r1[1]) + (r1[2] + r1[3]);
#pragma unroll
    for (int ww = 0; ww < 4; ++ww) { r0[ww] = *(const LAS f32x4*)(smem + (((ww + 4) * 10 + t0) * 64 + lane) * 16); r1[ww] = *(const LAS f32x4*)(smem + (((ww + 4) * 10 + t1) * 64 + lane) * 16); }
    s0 += (r0[0] + r0[1]) + (r0[2] + r0[3]); s1 += (r1[0] + r1[1]) + (r1[2] + r1[3]);
  };
  {
#pragma unroll
    for (int ks = 0; ks < W1E; ++ks) { w1f[ks][0] = gld128(rs, w1base + ks * 1024); w1f[ks][1] = gld128(rs, w1base + (20 + ks) * 1024); }
    SBAR();
    if (w == 7) { poll_flags(p.ws, WS_F2 + (pn * 8 + (lane & 7)) * FL_STRIDE, lane < 8, tag); touch_w2(1); }
    LBAR();
    STAMP(6);
    v4u ba[4][3];
    {
      const int base = WS_O + (pn * 8 + w) * 10240;
#pragma unroll
      for (int mt = 0; mt < 4; ++mt) {
        ba[mt][0] = gld128(rs, base + mt * 2560 + lane * 16);
        ba[mt][1] = gld128(rs, base + mt * 2560 + 1024 + lane * 16);
        const v2u c = gld64(rs, base + mt * 2560 + 2048 + lane * 8);
        ba[mt][2] = v4u{c.x, c.y, 0u, 0u};
      }
      SBAR();
    }
    f32x4 acc[2][2][5];
#pragma unroll
    for (int hp = 0; hp < 2; ++hp) {
#pragma unroll
      for (int ml = 0; ml < 2; ++ml)
#pragma unroll
        for (int nt = 0; nt < 5; ++nt) acc[hp][ml][nt] = mfma16(wo[nt][0], ba[2 * hp + ml][0], f32x4{0, 0, 0, 0});
#pragma unroll
      for (int k = 1; k < 3; ++k)
#pragma unroll
        for (int ml = 0; ml < 2; ++ml)
#pragma unroll
          for (int nt = 0; nt < 5; ++nt) acc[hp][ml][nt] = mfma16(wo[nt][k], ba[2 * hp + ml][k], acc[hp][ml][nt]);
      if (hp == 0) red_write(acc[0]);
    }
    STAMP(26);
#pragma unroll
    for (int hp = 0; hp < 2; ++hp) {
      if (hp == 1) { LBAR(); red_write(acc[1]); }
      LBAR();
      if (hp == 1) STAMP(27);
      const int u = w - 2 * hp;
      if (u >= 0 && u < 6) {
        const int part = u % 3, mt = 2 * hp + u / 3;
        f32x4 s0, s1;
        red_sum(u, s0, s1);
        v4u hv;
        hv.x = pk2(s0[0] + bflo(xr[hp][0].x), s0[1] + bfhi(xr[hp][0].x));
        hv.y = pk2(s0[2] + bflo(xr[hp][0].y), s0[3] + bfhi(xr[hp][0].y));
        hv.z = pk2(s1[0] + bflo(xr[hp][1].x), s1[1] + bfhi(xr[hp][1].x));
        hv.w = pk2(s1[2] + bflo(xr[hp][1].y), s1[3] + bfhi(xr[hp][1].y));
        hres[hp] = hv;
        const int base = WS_H + (pn * 8 + j) * HB;
        if (part < 2) gst128(rs, base + mt * 2560 + part * 1024 + lane * 16, hv);
        else { gst64(rs, base + mt * 2560 + 2048 + lane * 8, v2u{hv.x, hv.y}); hv.z = 0u; hv.w = 0u; }
        float su = 0.f, sq = 0.f;
#pragma unroll
        for (int i = 0; i < 4; ++i) { const float a = bflo(hv[i]), c = bfhi(hv[i]); su += a + c; sq = fmaf(a, a, fmaf(c, c, sq)); }
        su = xsum(su); sq = xsum(sq);
        if (g == 0) gst64(rs, base + 10240 + ((mt * 3 + part) * 16 + tok) * 8, v2u{__builtin_bit_cast(unsigned, su), __builtin_bit_cast(unsigned, sq)});
      }
    }
    WAIT_BAR(0);
    if (tid == 0) set_flag(p.ws, WS_F3 + (pn * 8 + j) * FL_STRIDE, tag);
    if (w != 7) touch_w2(2);
#pragma unroll
    for (int ks = W1E; ks < 20; ++ks) { w1f[ks][0] = gld128(rs, w1base + ks * 1024); w1f[ks][1] = gld128(rs, w1base + (20 + ks) * 1024); }
    SBAR();
  }
  STAMP(7);

  v4u w2f[35];
  int kso[7];
  {
    const int nks = w < 6 ? 7 : 6, r0 = 0;
#pragma unroll
    for (int ks = 0; ks < 7; ++ks) { int a = ks + r0; if (a >= nks) a -= nks; kso[ks] = (ks == 6 && nks == 6) ? 0 : a; }
  }
  auto w2off = [&](int f) { return w2base + kso[f / 5] * 5120 + (f % 5) * 1024; };
  {
    constexpr int HL = 11776;
    LAS f32x2* st = (LAS f32x2*)(smem + 8 * HL);
    f32x4 acc[4][2], accC[2];
    accC[0] = accC[1] = f32x4{0, 0, 0, 0};
#pragma unroll
    for (int ks = 0; ks < 20; ++ks) { accC[0] = mfma16(w1f[ks][0], ONES, accC[0]); accC[1] = mfma16(w1f[ks][1], ONES, accC[1]); }
    SBAR();
    if (w == 7) { poll_flags(p.ws, WS_F3 + (pn * 8 + (lane & 7)) * FL_STRIDE, lane < 8, tag); touch_w2(2); }
    LBAR();
    STAMP(8);
    {
      const char* src = p.ws + (size_t)(WS_H + (pn * 8 + w) * HB + lane * 16);
#pragma unroll
      for (int i = 0; i < 11; ++i) glds16_sc1(src + i * 1024, lds0 + w * HL + i * 1024);
      if (lane < 32) glds16_sc1(src + 11 * 1024, lds0 + w * HL + 11 * 1024);
    }
    WAIT_BAR(0);
    STAMP(9);
    {
#pragma unroll
      for (int i = 0; i < W2L; ++i) glds16_sc1(p.ws + (size_t)(w2off(i) + lane * 16), lds0 + W2LOFF + (w * W2L + i) * 1024);
    }
    auto bfrag = [&](int ks, v4u (&bf)[4]) {
#pragma unroll
      for (int mt = 0; mt < 4; ++mt) {
        if (ks < 16) bf[mt] = lds128(L + (ks >> 1) * HL + mt * 2560 + (ks & 1) * 1024 + lane * 16);
        else {
          const v2u a = lds64(L + (2 * (ks - 16)) * HL + mt * 2560 + 2048 + lane * 8), c = lds64(L + (2 * (ks - 16) + 1) * HL + mt * 2560 + 2048 + lane * 8);
          bf[mt] = v4u{a.x, a.y, c.x, c.y};
        }
      }
      SBAR();
    };
    if (w == 7) {
      float su = 0.f, sq = 0.f;
#pragma unroll
      for (int q = 0; q < 8; ++q)
#pragma unroll
        for (int part = 0; part < 3; ++part) {
          const f32x2 v = *(const LAS f32x2*)(smem + q * HL + 10240 + ((g * 3 + part) * 16 + tok) * 8);
          su += v.x; sq += v.y;
        }
      const float mean = su * (1.0f / DM);
      const float var = fmaxf(sq * (1.0f / DM) - mean * mean, 0.f);
      st[lane] = f32x2{mean, 1.0f / sqrtf(var + LN_EPS)};
    }
#pragma unroll
    for (int i = 0; i < 4; ++i) { acc[i][0] = f32x4{0, 0, 0, 0}; acc[i][1] = f32x4{0, 0, 0, 0}; }
    v4u bf[2][4];
    bfrag(0, bf[0]);
#pragma unroll
    for (int ks = 0; ks < 20; ++ks) {
      if (ks + 1 < 20) bfrag(ks + 1, bf[(ks + 1) & 1]);
#pragma unroll
      for (int mt = 0; mt < 4; ++mt) {
        acc[mt][0] = mfma16(w1f[ks][0], bf[ks & 1][mt], acc[mt][0]);
        acc[mt][1] = mfma16(w1f[ks][1], bf[ks & 1][mt], acc[mt][1]);
      }
      SBAR();
      if (ks >= 2 && ks < 18) {
#pragma unroll
        for (int e = 0; e < (ks < 9 ? 2 : 1); ++e) { const int f = ks < 9 ? W2L + 2 * (ks - 2) + e : W2L + 14 + (ks - 9); w2f[f] = gld128(rs, w2off(f) + lane * 16); }
        SBAR();
      }
    }
    STAMP(28);
    LBAR();
    if (gemm) {
#pragma unroll
      for (int mt = 0; mt < 4; ++mt) {
        const f32x2 ms = st[mt * 16 + tok];
        float v[8];
#pragma unroll
        for (int e = 0; e < 2; ++e)
#pragma unroll
          for (int r = 0; r < 4; ++r) v[4 * e + r] = fmaxf((acc[mt][e][r] - ms.x * accC[e][r]) * ms.y, 0.f);
        gst128(rs, WS_H1 + (((pn * 8 + j) * 7 + w) * 4 + mt) * 1024 + lane * 16, v4u{pk2(v[0], v[1]), pk2(v[2], v[3]), pk2(v[4], v[5]), pk2(v[6], v[7])});
      }
    }
    WAIT_BAR(0);
    if (tid == 0) set_flag(p.ws, WS_F4 + (pn * 8 + j) * FL_STRIDE, tag);
  }
  STAMP(10);

  {
    const bool full = w < 6;
#pragma unroll
    for (int f = 0; f < W2L; ++f) w2f[f] = lds128(L + W2LOFF + (w * W2L + f) * 1024 + lane * 16);
#pragma unroll
    for (int f = 30; f < 35; ++f) w2f[f] = gld128(rs, (full ? w2off(f) : 0x7F000000) + lane * 16);
    SBAR();
    if (w == 7) poll_flags(p.ws, WS_F4 + (pn * 8 + (lane & 7)) * FL_STRIDE, lane < 8, tag);
    LBAR();
    STAMP(11);
    const int bbase = WS_H1 + (pn * 8 + w) * 28672 + lane * 16;
    v4u bf[2][7][2];
#pragma unroll
    for (int ks = 0; ks < 7; ++ks)
#pragma unroll
      for (int ml = 0; ml < 2; ++ml) bf[0][ks][ml] = gld128(rs, bbase + (kso[ks] * 4 + ml) * 1024);
    SBAR();
    f32x4 acc[2][2][5];
#pragma unroll
    for (int hp = 0; hp < 2; ++hp) {
#pragma unroll
      for (int ml = 0; ml < 2; ++ml)
#pragma unroll
        for (int nt = 0; nt < 5; ++nt) acc[hp][ml][nt] = f32x4{0, 0, 0, 0};
#pragma unroll
      for (int ks = 0; ks < 7; ++ks) {
#pragma unroll
        for (int ml = 0; ml < 2; ++ml)
#pragma unroll
          for (int nt = 0; nt < 5; ++nt) acc[hp][ml][nt] = mfma16(w2f[ks * 5 + nt], bf[hp][ks][ml], acc[hp][ml][nt]);
        SBAR();
        if (hp == 0) {
#pragma unroll
          for (int ml = 0; ml < 2; ++ml) bf[1][ks][ml] = gld128(rs, bbase + (kso[ks] * 4 + 2 + ml) * 1024);
          SBAR();
        }
      }
      if (hp == 0) { red_write(acc[0]); SBAR(); STAMP(15); }
    }
    STAMP(29);
#pragma unroll
    for (int hp = 0; hp < 2; ++hp) {
      if (hp == 1) { LBAR(); red_write(acc[1]); }
      LBAR();
      const int u = w - 2 * hp;
      if (u >= 0 && u < 6) {
        const int part = u % 3, mt = 2 * hp + u / 3;
        f32x4 s0, s1;
        red_sum(u, s0, s1);
        const v4u hv = hres[hp];
        int lz = lane; asm volatile("" : "+v"(lz));
        char* dst = p.out + (size_t)((64 * pn + 16 * mt + (lz & 15)) * 1280 + (80 * j + 32 * part + 4 * (lz >> 4)) * 2);
        *(GAS v2u*)dst = v2u{pk2(s0[0] + bflo(hv.x), s0[1] + bfhi(hv.x)), pk2(s0[2] + bflo(hv.y), s0[3] + bfhi(hv.y))};
        if (part < 2) *(GAS v2u*)(dst + 32) = v2u{pk2(s1[0] + bflo(hv.z), s1[1] + bfhi(hv.z)), pk2(s1[2] + bflo(hv.w), s1[3] + bfhi(hv.w))};
      }
    }
  }
  STAMP(12);
  STAMP_DUMP();
  if (b == 0 && w == 1 && lane == 0) set_flag(p.ws, WS_EPOCH, tag);
}
}

extern "C" void kernel_launch(void* const* d_in, const int* in_sizes, int n_in,
                              void* d_out, int out_size, void* d_ws, size_t ws_size,
                              hipStream_t stream) {
  (void)in_sizes; (void)n_in; (void)out_size; (void)ws_size;
  Params p;
  p.x = (const char*)d_in[0]; p.wqkv = (const char*)d_in[1]; p.wo = (const char*)d_in[2];
  p.w1 = (const char*)d_in[4]; p.w2 = (const char*)d_in[5];
  p.out = (char*)d_out; p.ws = (char*)d_ws;
  (void)hipFuncSetAttribute((const void*)block_kernel, hipFuncAttributeMaxDynamicSharedMemorySize, LDS_BYTES);
  block_kernel<<<256, 512, LDS_BYTES, stream>>>(p);
}
```

```cpp
#include <hip/hip_runtime.h>
#include <hip/hip_bf16.h>

#define GAS __attribute__((address_space(1)))
#define LAS __attribute__((address_space(3)))
#define DI __device__ __forceinline__
typedef unsigned v4u __attribute__((ext_vector_type(4)));
typedef unsigned v2u __attribute__((ext_vector_type(2)));
typedef float f32x2 __attribute__((ext_vector_type(2)));
typedef float f32x4 __attribute__((ext_vector_type(4)));
typedef float f32x16 __attribute__((ext_vector_type(16)));
typedef short bf16x8 __attribute__((ext_vector_type(8)));
typedef short bf16x4 __attribute__((ext_vector_type(4)));
typedef __bf16 bfx2 __attribute__((ext_vector_type(2)));
typedef unsigned long long u64;
typedef GAS u64 gu64;
#define RLX_AGENT __ATOMIC_RELAXED, __HIP_MEMORY_SCOPE_AGENT

namespace {
constexpr int SEQ = 2048, DM = 640, NQKV = 1920, DFF = 1728;
constexpr float LN_EPS = 1e-5f;
constexpr float QSCALE = 0.22360679774997896f * 1.4426950408889634f;

constexpr int FL_STRIDE = 128;
constexpr int WS_EPOCH = 0, WS_TMO = 256, WS_ARR = 1024;
constexpr int WS_F1 = 4096;
constexpr int WS_F2 = WS_F1 + 256 * FL_STRIDE;
constexpr int WS_F3 = WS_F2 + 256 * FL_STRIDE;
constexpr int WS_F4 = WS_F3 + 256 * FL_STRIDE;
constexpr int WS_FP = WS_F4 + 256 * FL_STRIDE;
constexpr int WS_STAMP = WS_FP + 64 * FL_STRIDE;
constexpr int WS_QKV = 1 << 20;
constexpr int WS_O = WS_QKV + 480 * 2048 * 8;
constexpr int HB = 12288;
constexpr int WS_H = WS_O + 32 * 81920;
constexpr int WS_H1 = WS_H + 256 * HB;
constexpr int WS_WOP = WS_H1 + 32 * 8 * 28672;
constexpr int WS_W1P = WS_WOP + 64 * 12800;
constexpr int WS_W2P = WS_W1P + 108 * 20 * 1024;
constexpr int WS_END = WS_W2P + 64 * 35 * 1024;

constexpr int LDS_BYTES = 160 * 1024;
constexpr unsigned SPIN_CAP = 1u << 18;
constexpr int AUX_SC1 = 16;

struct Params {
  const char* x; const char* wqkv; const char* wo; const char* w1; const char* w2;
  char* out; char* ws;
};

DI f32x4 mfma16(v4u a, v4u b, f32x4 c) {
  return __builtin_amdgcn_mfma_f32_16x16x32_bf16(__builtin_bit_cast(bf16x8, a), __builtin_bit_cast(bf16x8, b), c, 0, 0, 0);
}
DI f32x4 mfma16h(v2u a, v2u b, f32x4 c) {
  return __builtin_amdgcn_mfma_f32_16x16x16bf16_1k(__builtin_bit_cast(bf16x4, a), __builtin_bit_cast(bf16x4, b), c, 0, 0, 0);
}
DI f32x16 mfma32(v4u a, v4u b, f32x16 c) {
  return __builtin_amdgcn_mfma_f32_32x32x16_bf16(__builtin_bit_cast(bf16x8, a), __builtin_bit_cast(bf16x8, b), c, 0, 0, 0);
}
DI unsigned pk2(float lo, float hi) {
  f32x2 v = {lo, hi};
  return __builtin_bit_cast(unsigned, __builtin_convertvector(v, bfx2));
}
DI float bflo(unsigned u) { return __builtin_bit_cast(float, u << 16); }
DI float bfhi(unsigned u) { return __builtin_bit_cast(float, u & 0xffff0000u); }

DI float xsum(float v) {
  const unsigned u = __builtin_bit_cast(unsigned, v);
  const auto a = __builtin_amdgcn_permlane32_swap(u, u, false, false);
  const float s = __builtin_bit_cast(float, (unsigned)a[0]) + __builtin_bit_cast(float, (unsigned)a[1]);
  const unsigned w = __builtin_bit_cast(unsigned, s);
  const auto c = __builtin_amdgcn_permlane16_swap(w, w, false, false);
  return __builtin_bit_cast(float, (unsigned)c[0]) + __builtin_bit_cast(float, (unsigned)c[1]);
}

DI void glds16(const char* g, unsigned lds_base) {
  unsigned sv;
  asm volatile("s_mov_b32 %0, m0\n\ts_mov_b32 m0, %2\n\ts_nop 0\n\tglobal_load_lds_dwordx4 %1, off\n\ts_mov_b32 m0, %0"
               : "=&s"(sv) : "v"(g), "s"(lds_base) : "memory");
}
DI void glds16_sc1(const char* g, unsigned lds_base) {
  unsigned sv;
  asm volatile("s_mov_b32 %0, m0\n\ts_mov_b32 m0, %2\n\ts_nop 0\n\tglobal_load_lds_dwordx4 %1, off sc1\n\ts_mov_b32 m0, %0"
               : "=&s"(sv) : "v"(g), "s"(lds_base) : "memory");
}
DI void touch4(const char* g, unsigned lds_base) {
  unsigned sv;
  asm volatile("s_mov_b32 %0, m0\n\ts_mov_b32 m0, %2\n\ts_nop 0\n\tglobal_load_lds_dword %1, off sc1\n\ts_mov_b32 m0, %0"
               : "=&s"(sv) : "v"(g), "s"(lds_base) : "memory");
}
#define WAITV(N) asm volatile("s_waitcnt vmcnt(" #N ")" ::: "memory")
#define WAIT_BAR(N) asm volatile("s_waitcnt vmcnt(" #N ") lgkmcnt(0)\n\ts_barrier" ::: "memory")
#define LBAR() asm volatile("s_waitcnt lgkmcnt(0)\n\ts_barrier" ::: "memory")
#define SBAR() __builtin_amdgcn_sched_barrier(0)
#define PIN(x) asm volatile("" : "+v"(x))

typedef LAS const char* lptr;
DI v4u lds128(lptr p) { return *(const LAS v4u*)p; }
DI v2u lds64(lptr p) { return *(const volatile LAS v2u*)p; }
DI v2u ldstr(lptr p) { return __builtin_bit_cast(v2u, __builtin_amdgcn_ds_read_tr16_b64_v4i16((LAS bf16x4*)p)); }

DI v4u gld128(__amdgpu_buffer_rsrc_t rs, int off) { return __builtin_amdgcn_raw_buffer_load_b128(rs, off, 0, AUX_SC1); }
DI v2u gld64(__amdgpu_buffer_rsrc_t rs, int off) { return __builtin_amdgcn_raw_buffer_load_b64(rs, off, 0, AUX_SC1); }
DI void gst128(__amdgpu_buffer_rsrc_t rs, int off, v4u v) { __builtin_amdgcn_raw_buffer_store_b128(v, rs, off, 0, AUX_SC1); }
DI void gst64(__amdgpu_buffer_rsrc_t rs, int off, v2u v) { __builtin_amdgcn_raw_buffer_store_b64(v, rs, off, 0, AUX_SC1); }

DI void poll_flags(char* ws, int off, bool want, u64 tag) {
  gu64* f = (gu64*)(ws + off);
  for (unsigned spins = 0;;) {
    u64 v = tag;
    if (want) v = __hip_atomic_load(f, RLX_AGENT);
    if (__all(v == tag)) break;
    if (++spins > SPIN_CAP) { __hip_atomic_store((gu64*)(ws + WS_TMO), tag, RLX_AGENT); break; }
    if ((spins & 1023u) == 0u && __builtin_amdgcn_readfirstlane((unsigned)(__hip_atomic_load((gu64*)(ws + WS_TMO), RLX_AGENT) == tag))) break;
  }
  __builtin_amdgcn_fence(__ATOMIC_ACQUIRE, "wavefront");
}
DI void set_flag(char* ws, int off, u64 tag) { __hip_atomic_store((gu64*)(ws + off), tag, RLX_AGENT); }

#define STAMP(i) do {} while (0)
#define STAMP_DUMP() do {} while (0)

DI void pack_issue(const Params& p, int u, unsigned lds, int lane) {
  const char* m; int r0, r1, pitch, cg;
  if (u < 240) {
    const int ru = u / 10, w = ru / 3, part = ru - 3 * w;
    cg = u - 10 * ru; m = p.wo; pitch = 1280; r0 = 80 * w + 32 * part; r1 = part == 2 ? r0 : r0 + 16;
  } else if (u < 780) {
    const int v = u - 240, ks = v / 27;
    cg = v - 27 * ks; m = p.w1; pitch = 3456;
    if (ks < 16) { r0 = 80 * (ks >> 1) + 32 * (ks & 1); r1 = r0 + 16; } else { r0 = 160 * (ks - 16) + 64; r1 = r0 + 80; }
  } else {
    const int v = u - 780, ks = v / 10;
    cg = v - 10 * ks; m = p.w2; pitch = 1280; r0 = 32 * ks; r1 = r0 + 16;
  }
  const int lr = lane >> 3, c = (lane & 7) ^ (((lr >> 1) & 3) << 1);
  const char* s0 = m + (size_t)((r0 + lr) * pitch + cg * 128 + c * 16);
  const char* s1 = m + (size_t)((r1 + lr) * pitch + cg * 128 + c * 16);
  glds16(s0, lds); glds16(s0 + 8 * pitch, lds + 1024); glds16(s1, lds + 2048); glds16(s1 + 8 * pitch, lds + 3072);
}

DI void pack_finish(__amdgpu_buffer_rsrc_t rs, lptr L, int u, int lane) {
  const int row = 4 * (lane >> 4) + ((lane & 15) >> 2);
  const int a0 = row * 128 + (lane & 3) * 8, sw = (row >> 1) & 3;
  v2u lo[4], hi[4];
#pragma unroll
  for (int t = 0; t < 4; ++t) { lo[t] = ldstr(L + a0 + ((t ^ sw) * 32)); hi[t] = ldstr(L + a0 + ((t ^ sw) * 32) + 2048); }
  int T0, base, sj, sn; bool half = false;
  if (u < 240) {
    const int ru = u / 10, w = ru / 3, part = ru - 3 * w;
    T0 = 4 * (u - 10 * ru); base = WS_WOP + w * 12800 + part * 1024; sj = 8 * 12800; sn = 2560; half = part == 2;
  } else if (u < 780) {
    const int v = u - 240, ks = v / 27;
    T0 = 4 * (v - 27 * ks); base = WS_W1P + ks * 1024; sj = 5 * 20480; sn = 20480;
  } else {
    const int v = u - 780, ru = v / 10;
    const int w = ru < 42 ? ru / 7 : 6 + (ru - 42) / 6, ks = ru < 42 ? ru - 7 * w : ru - 42 - 6 * (w - 6);
    T0 = 4 * (v - 10 * ru); base = WS_W2P + w * 35840 + ks * 5120; sj = 8 * 35840; sn = 1024;
  }
#pragma unroll
  for (int t = 0; t < 4; ++t) {
    const int T = T0 + t, jj = T / 5, nt = T - 5 * jj;
    const int d = base + jj * sj + nt * sn;
    if (half) gst64(rs, d + lane * 8, lo[t]);
    else gst128(rs, d + lane * 16, v4u{lo[t].x, lo[t].y, hi[t].x, hi[t].y});
  }
}

__global__ void __launch_bounds__(512, 2) block_kernel(Params p) {
  extern __shared__ __attribute__((aligned(16))) char smem[];
  const int tid = threadIdx.x, lane = tid & 63, w = __builtin_amdgcn_readfirstlane(tid >> 6);
  const int tok = lane & 15, g = lane >> 4;
  const int b = blockIdx.x, xcd = b & 7, t = b >> 3;
  const unsigned lds0 = (unsigned)(size_t)smem;
  lptr L = (lptr)smem;
  const __amdgpu_buffer_rsrc_t rs = __builtin_amdgcn_make_buffer_rsrc(p.ws, 0, WS_END, 0x00020000);
  STAMP(0);
  {
    unsigned len;
    asm volatile("mk_code_begin:\n\ts_mov_b32 %0, mk_code_end-mk_code_begin" : "=s"(len));
    const char* pc = (const char*)__builtin_amdgcn_s_getpc();
    for (unsigned off = (unsigned)tid * 128u; off < len; off += 512u * 128u) touch4(pc + off, lds0 + LDS_BYTES - 256);
  }
  const u64 e0 = __hip_atomic_load((gu64*)(p.ws + WS_EPOCH), RLX_AGENT);
  const v4u ONES = {0x3F803F80u, 0x3F803F80u, 0x3F803F80u, 0x3F803F80u};
  u64 tag;

  {
    const int P128 = 4 * (xcd & 3) + (t & 3), s = (xcd >> 2) * 8 + (t >> 2);
    if (s < 15) {
      LAS f32x2* st = (LAS f32x2*)(smem + 131072);
      LAS float* cs = (LAS float*)(smem + 132096);
      const int q4 = (lane & 15) >> 2, pp = lane & 3;
      const int fsw = q4 | ((g & 1) << 2);
      const int xlane = tok * 128 + ((g ^ (tok >> 1)) * 16);
      const int wlane = 16384 + (8 * g + q4) * 256 + pp * 8;
      if (w >= 4) {
        const int a = w - 4, lt = tid - 256;
        const int rot = (t * 3 + xcd) % 10;
        const char* xs; const char* wsrc;
        { const int row = lt >> 3, c = (lt & 7) ^ ((row >> 1) & 7); xs = p.x + (size_t)((P128 * 128 + row) * 1280 + c * 16); }
        { const int row = lt >> 4, f = (row & 3) | (((row >> 3) & 1) << 2), c = (lt & 15) ^ (f << 1); wsrc = p.wqkv + (size_t)(row * 3840 + s * 256 + c * 16); }
        auto stage = [&](int i) {
          int kc = i + rot; if (kc >= 10) kc -= 10;
          const unsigned d = lds0 + (i & 3) * 32768 + a * 1024;
          const char* xk = xs + kc * 128; const char* wk = wsrc + (size_t)(kc * (64 * 3840));
#pragma unroll
          for (int q = 0; q < 4; ++q) { glds16(wk + q * (16 * 3840), d + 16384 + q * 4096); glds16(xk + q * (32 * 1280), d + q * 4096); }
        };
        stage(0); stage(1); stage(2);
        f32x4 accC[2], accS[2], accG[2];
#pragma unroll
        for (int i = 0; i < 2; ++i) accC[i] = accS[i] = accG[i] = f32x4{0, 0, 0, 0};
        const int wo0 = wlane + (((2 * a) ^ fsw) * 32);
#pragma unroll
        for (int i = 0; i < 10; ++i) {
          if (i < 8) WAIT_BAR(16); else if (i == 8) WAIT_BAR(8); else WAIT_BAR(0);
          if (i + 3 < 10) stage(i + 3);
          SBAR();
          lptr S = L + (i & 3) * 32768;
          v4u xb[2][2], wa[2][2];
#pragma unroll
          for (int k2 = 0; k2 < 2; ++k2)
#pragma unroll
            for (int e = 0; e < 2; ++e) {
              xb[k2][e] = lds128(S + (2 * a + e) * 2048 + (xlane ^ (k2 * 64)));
              const v2u lo = ldstr(S + (wo0 ^ (e * 32)) + k2 * 8192), hi = ldstr(S + (wo0 ^ (e * 32)) + k2 * 8192 + 1024);
              wa[k2][e] = v4u{lo.x, lo.y, hi.x, hi.y};
            }
          SBAR();
#pragma unroll
          for (int k2 = 0; k2 < 2; ++k2)
#pragma unroll
            for (int e = 0; e < 2; ++e) {
              accC[e] = mfma16(wa[k2][e], ONES, accC[e]);
              accS[e] = mfma16(ONES, xb[k2][e], accS[e]);
              accG[e] = mfma16(xb[k2][e], xb[k2][e], accG[e]);
            }
          SBAR();
        }
        tag = (((u64)__builtin_amdgcn_readfirstlane((unsigned)(e0 >> 32)) << 32) | __builtin_amdgcn_readfirstlane((unsigned)e0)) + 0x9E3779B97F4A7C15ull;
        const int r = tok & 3;
#pragma unroll
        for (int e = 0; e < 2; ++e) {
          const float G = r == 0 ? accG[e][0] : r == 1 ? accG[e][1] : r == 2 ? accG[e][2] : accG[e][3];
          const float mean = accS[e][0] * (1.0f / DM);
          const float var = fmaxf(G * (1.0f / DM) - mean * mean, 0.f);
          if (g == (tok >> 2)) st[(2 * a + e) * 16 + tok] = f32x2{mean, 1.0f / sqrtf(var + LN_EPS)};
          if (tok == 0) *(LAS f32x4*)(cs + (2 * a + e) * 16 + 4 * g) = accC[e];
        }
        LBAR();
      } else {
        const int mh = w >> 1, nh = w & 1;
        const int xo = mh * 8192 + xlane;
        const int wo0 = wlane + (((4 * nh) ^ fsw) * 32);
        f32x4 acc[4][4];
#pragma unroll
        for (int m = 0; m < 4; ++m)
#pragma unroll
          for (int n = 0; n < 4; ++n) acc[m][n] = f32x4{0, 0, 0, 0};
        v4u xb[2][4], wa[2][4];
        auto rdfrag = [&](int i, int k2) {
          lptr S = L + (i & 3) * 32768;
#pragma unroll
          for (int n = 0; n < 4; ++n) {
            const v2u lo = ldstr(S + (wo0 ^ (n * 32)) + k2 * 8192), hi = ldstr(S + (wo0 ^ (n * 32)) + k2 * 8192 + 1024);
            wa[k2][n] = v4u{lo.x, lo.y, hi.x, hi.y};
            xb[k2][n] = lds128(S + n * 2048 + (xo ^ (k2 * 64)));
          }
          SBAR();
        };
        auto mm = [&](int k2) {
#pragma unroll
          for (int m = 0; m < 4; ++m)
#pragma unroll
            for (int n = 0; n < 4; ++n) acc[m][n] = mfma16(wa[k2][n], xb[k2][m], acc[m][n]);
          SBAR();
        };
        const int pu = 384 + (P128 * 15 + s) * 4 + w;
        const bool packer = pu < 1320;
        tag = (((u64)__builtin_amdgcn_readfirstlane((unsigned)(e0 >> 32)) << 32) | __builtin_amdgcn_readfirstlane((unsigned)e0)) + 0x9E3779B97F4A7C15ull;
#pragma unroll
        for (int i = 0; i < 10; ++i) {
          if (i == 8) WAIT_BAR(0); else LBAR();
          if (i == 8 && tid == 0) set_flag(p.ws, WS_ARR + b * 8, tag);
          STAMP(16 + i);
          rdfrag(i, 0);
          rdfrag(i, 1);
          mm(0);
          if (i == 1 && packer) pack_issue(p, pu, lds0 + 133120 + w * 4096, lane);
          if (i == 5 && packer) { WAITV(0); pack_finish(rs, L + 133120 + w * 4096, pu, lane); }
          mm(1);
        }
        STAMP(1);
        LBAR();
        const float qs = s < 5 ? QSCALE : 1.0f;
        f32x4 c4[4];
#pragma unroll
        for (int n = 0; n < 4; ++n) c4[n] = *(const LAS f32x4*)(cs + (4 * nh + n) * 16 + 4 * g);
        const bool odd = tok & 1;
#pragma unroll
        for (int m = 0; m < 4; ++m) {
          const int mt = 4 * mh + m;
          const f32x2 ms = st[mt * 16 + tok];
          const float sc = ms.y * qs;
#pragma unroll
          for (int n2 = 0; n2 < 2; ++n2) {
            unsigned pk[2][2];
#pragma unroll
            for (int e = 0; e < 2; ++e) {
              const f32x4 a4 = acc[m][2 * n2 + e], cc = c4[2 * n2 + e];
              pk[e][0] = pk2((a4[0] - ms.x * cc[0]) * sc, (a4[1] - ms.x * cc[1]) * sc);
              pk[e][1] = pk2((a4[2] - ms.x * cc[2]) * sc, (a4[3] - ms.x * cc[3]) * sc);
            }
            const unsigned s0 = odd ? pk[0][0] : pk[1][0], s1 = odd ? pk[0][1] : pk[1][1];
            const unsigned r0 = (unsigned)__builtin_amdgcn_mov_dpp((int)s0, 0xB1, 0xF, 0xF, true);
            const unsigned r1 = (unsigned)__builtin_amdgcn_mov_dpp((int)s1, 0xB1, 0xF, 0xF, true);
            const v4u v = odd ? v4u{r0, r1, pk[1][0], pk[1][1]} : v4u{pk[0][0], pk[0][1], r0, r1};
            const int quad = 32 * s + 4 * (4 * nh + 2 * n2 + (odd ? 1 : 0)) + g;
            gst128(rs, WS_QKV + (quad * 2048 + P128 * 128 + mt * 16 + (tok & 14)) * 8, v);
          }
        }
      }
      WAIT_BAR(0);
      if (tid == 0) set_flag(p.ws, WS_F1 + (P128 * 15 + s) * FL_STRIDE, tag);
    } else {
      const int pu = ((xcd - 4) * 4 + (t - 28)) * 24 + w * 3;
#pragma unroll 1
      for (int i = 0; i < 3; ++i) pack_issue(p, pu + i, lds0 + (w * 3 + i) * 4096, lane);
      tag = (((u64)__builtin_amdgcn_readfirstlane((unsigned)(e0 >> 32)) << 32) | __builtin_amdgcn_readfirstlane((unsigned)e0)) + 0x9E3779B97F4A7C15ull;
      WAITV(0);
#pragma unroll 1
      for (int i = 0; i < 3; ++i) pack_finish(rs, L + (w * 3 + i) * 4096, pu + i, lane);
      WAIT_BAR(0);
      if (tid == 0) set_flag(p.ws, WS_ARR + b * 8, tag);
    }
  }
  STAMP(2);

  const int pg = xcd * 4 + (t & 3), jg = t >> 2;
  auto touch_w2 = [&](int part) {
    const char* a = p.ws + (size_t)(WS_W2P + (t * 8 + w) * 8960 + (part * 24 + lane) * 128);
    if (lane < (part < 2 ? 24 : 22)) touch4(a, lds0 + LDS_BYTES - 256);
  };
  v4u wo[5][3];
  v2u xr[2][2];
  const int ntile = jg < 6 ? 14 : 12, tbase = jg < 7 ? 14 * jg : 96;
  const bool gemm = 2 * w < ntile;
  constexpr int W1E = 8;
  constexpr int W2L = 7;
  constexpr int W2LOFF = 8 * 11776 + 1024;
  v4u w1f[20][2];
  const int w1base = gemm ? WS_W1P + (tbase + 2 * w) * 20480 + lane * 16 : 0x7F000000;
  const int w2base = WS_W2P + (jg * 8 + w) * 35840;
  {
    constexpr int PL = 2592, QOFF = 40 * PL, ONESOFF = QOFF + 20 * 512;
    const int pn = t, j = xcd;
    const int tok0 = 64 * pn - 256;
    const int wobase = WS_WOP + (jg * 8 + w) * 12800;
    auto wo_loads = [&]() {
#pragma unroll
      for (int i = 0; i < 5; ++i) {
        wo[i][0] = gld128(rs, wobase + i * 2560 + lane * 16);
        wo[i][1] = gld128(rs, wobase + i * 2560 + 1024 + lane * 16);
        wo[i][2] = gld128(rs, wobase + i * 2560 + 2048 + lane * 8);
      }
#pragma unroll
      for (int hp = 0; hp < 2; ++hp) {
        int u = w - 2 * hp; u = u < 0 ? 0 : (u > 5 ? 5 : u);
        const int mt = 2 * hp + u / 3, part = u % 3;
        const char* src = p.x + (size_t)((64 * pg + 16 * mt + tok) * 1280 + (80 * jg + 32 * part + 4 * g) * 2);
        xr[hp][0] = *(const GAS v2u*)src;
        xr[hp][1] = *(const GAS v2u*)(src + (part < 2 ? 32 : 0));
      }
      SBAR();
    };
    if (w == 1) {
#pragma unroll 1
      for (int i = 0; i < 4; ++i) poll_flags(p.ws, WS_ARR + (i * 64 + lane) * 8, true, tag);
    } else if (w >= 2) {
      if (tid < 128 + 352) *(LAS v2u*)(smem + ONESOFF + (tid - 128) * 8) = v2u{0x00003F80u, 0u};
      if (pn < 4) {
        const int n16 = -tok0 / 2;
#pragma unroll 1
        for (int i = tid - 128; i < 40 * n16; i += 384) { const int pl = i / n16; *(LAS v4u*)(smem + pl * PL + (i - pl * n16) * 16) = v4u{0u, 0u, 0u, 0u}; }
      }
    }
    LBAR();
    if (w == 0) {
      const int R = lane / 6, rem = lane % 6, pi = rem >> 1;
      const int q0 = 160 * R + 20 * j, sl = (rem & 1) ? (q0 + 19) >> 5 : q0 >> 5;
      const int P = (pn >> 1) - pi;
      const bool want = lane < 18 && P >= 0 && (R > 0 || pi == 0);
      poll_flags(p.ws, WS_F1 + ((want ? P : 0) * 15 + (want ? sl : 0)) * FL_STRIDE, want, tag);
    } else {
      wo_loads();
    }
    LBAR();
    STAMP(3);
#pragma unroll
    for (int i = 0; i < 5; ++i) {
      const int pl = 5 * w + i;
      const int quad = (pl < 20 ? 160 + pl : 300 + pl) + 20 * j;
      const char* src = p.ws + (size_t)(WS_QKV + (quad * 2048 + tok0) * 8 + lane * 16);
#pragma unroll
      for (int c = 0; c < 3; ++c) {
        const int tk = tok0 + c * 128 + lane * 2;
        if (tk >= 0 && (c < 2 || lane < 32)) glds16_sc1(src + c * 1024, lds0 + pl * PL + c * 1024);
      }
    }
    if (w < 5) {
#pragma unroll
      for (int i = 0; i < 2; ++i) {
        const int pl = 4 * w + 2 * i + (lane >> 5);
        const char* src = p.ws + (size_t)(WS_QKV + ((20 * j + pl) * 2048 + 64 * pn) * 8 + (lane & 31) * 16);
        glds16_sc1(src, lds0 + QOFF + (4 * w + 2 * i) * 512);
      }
    }
    WAIT_BAR(0);
    STAMP(4);
    if (w == 0) wo_loads();
    auto pre = [&](int i) {
      const char* a = p.ws + (size_t)(WS_W1P + (t * 8 + w) * 8640 + (i * 34 + lane) * 128);
      if (lane < 34) touch4(a, lds0 + LDS_BYTES - 256);
    };
    const int hl = w >> 1, qh = w & 1, r32 = lane & 31, h = lane >> 5;
    const int bmin = 8 - 2 * pn - qh;
    v4u qf0, qf1;
    {
      const v2u a = lds64(L + QOFF + (5 * hl + 2 * h) * 512 + (32 * qh + r32) * 8), c = lds64(L + QOFF + (5 * hl + 2 * h + 1) * 512 + (32 * qh + r32) * 8);
      qf0 = v4u{a.x, a.y, c.x, c.y};
      const v2u d = lds64(L + QOFF + (5 * hl + 4) * 512 + (32 * qh + r32) * 8);
      qf1 = h ? v4u{0u, 0u, 0u, 0u} : v4u{d.x, d.y, 0x00003F80u, 0u};
    }
    lptr kp0 = L + (5 * hl + 2 * h) * PL + (32 * qh + r32) * 8;
    lptr kp1 = L + (5 * hl + 4) * PL + (32 * qh + r32) * 8;
    auto kfrag = [&](int blk, v4u (&kf)[2]) {
      const v2u a = lds64(kp0 + blk * 256), d = lds64(kp0 + PL + blk * 256), e = lds64(kp1 + blk * 256);
      kf[0] = v4u{a.x, a.y, d.x, d.y};
      kf[1] = v4u{e.x, e.y, blk >= bmin ? 0u : 0x0000F149u  , 0u};
    };
    auto scores = [&](const v4u (&kf)[2], f32x16 c) -> f32x16 { return mfma32(kf[1], qf1, mfma32(kf[0], qf0, c)); };
    f32x16 zero16;
#pragma unroll
    for (int r = 0; r < 16; ++r) zero16[r] = 0.f;
    float m;
    {
      v4u kf[9][2];
      kfrag(8, kf[8]);
#pragma unroll
      for (int blk = 0; blk < 8; ++blk) kfrag(blk, kf[blk]);
      SBAR();
      pre(0);
      const f32x16 s8 = scores(kf[8], zero16), s0 = scores(kf[0], zero16);
      f32x16 sn = scores(kf[1], zero16);
      SBAR();
      float m0 = -3.0e38f, m1 = -3.0e38f;
#pragma unroll
      for (int r = 0; r < 16; r += 2) {
        const int kk = (r & 3) + 8 * (r >> 2) + 4 * h;
        m0 = fmaxf(fmaxf(m0, kk <= r32 ? s8[r] : s0[r]), kk + 1 <= r32 ? s8[r + 1] : s0[r + 1]);
      }
      PIN(m0);
#pragma unroll
      for (int blk = 1; blk < 8; ++blk) {
        const f32x16 sb = sn;
        SBAR();
        if (blk < 7) sn = scores(kf[blk + 1], zero16);
        SBAR();
#pragma unroll
        for (int r = 0; r < 16; r += 4) { m0 = fmaxf(fmaxf(m0, sb[r]), sb[r + 1]); m1 = fmaxf(fmaxf(m1, sb[r + 2]), sb[r + 3]); }
        PIN(m0); PIN(m1);
      }
      m = fmaxf(m0, m1);
      const unsigned mu = __builtin_bit_cast(unsigned, m);
      const auto sw = __builtin_amdgcn_permlane32_swap(mu, mu, false, false);
      m = fmaxf(__builtin_bit_cast(float, (unsigned)sw[0]), __builtin_bit_cast(float, (unsigned)sw[1]));
    }
    STAMP(13);
    pre(1);
    asm volatile("" ::: "memory");
    f32x16 negm;
#pragma unroll
    for (int r = 0; r < 16; ++r) negm[r] = -m;
    const int blk16 = (lane >> 4) & 1, q4 = (lane & 15) >> 2, pp = lane & 3;
    const int vplane = (blk16 == 0) ? (20 + 5 * hl + pp) * PL : (pp == 0 ? (24 + 5 * hl) * PL : ONESOFF + (((20 + 5 * hl) * 32 + 160) & 255));
    lptr vp = L + vplane + (32 * qh + 4 * h + q4) * 8;
    f32x16 o = zero16;
    auto vfrag = [&](int blk, v4u (&vf)[2]) {
#pragma unroll
      for (int s2 = 0; s2 < 2; ++s2) {
        const v2u a = ldstr(vp + blk * 256 + s2 * 128), c = ldstr(vp + blk * 256 + s2 * 128 + 64);
        vf[s2] = v4u{a.x, a.y, c.x, c.y};
      }
    };
    auto pv = [&](const v4u (&vf)[2], const unsigned (&pk)[8]) {
      o = mfma32(vf[0], v4u{pk[0], pk[1], pk[2], pk[3]}, o);
      o = mfma32(vf[1], v4u{pk[4], pk[5], pk[6], pk[7]}, o);
    };
    {
      v4u k8[2], k0[2], kn[2][2], v8[2], v0[2];
      kfrag(8, k8); kfrag(0, k0); kfrag(1, kn[1]); kfrag(2, kn[0]);
      vfrag(8, v8); vfrag(0, v0);
      SBAR();
      const f32x16 sa = scores(k8, negm), sb = scores(k0, negm);
      f32x16 sn = scores(kn[1], negm);
      SBAR();
      unsigned p8[8], p0[8];
#pragma unroll
      for (int r = 0; r < 16; r += 2) {
        const int kk = (r & 3) + 8 * (r >> 2) + 4 * h;
        const float e0v = __builtin_amdgcn_exp2f(kk <= r32 ? sa[r] : sb[r]), e1v = __builtin_amdgcn_exp2f(kk + 1 <= r32 ? sa[r + 1] : sb[r + 1]);
        const unsigned pkd = pk2(e0v, e1v);
        const unsigned msk = kk + 1 <= r32 ? 0xffffffffu : (kk <= r32 ? 0x0000ffffu : 0u);
        p8[r >> 1] = pkd & msk; p0[r >> 1] = pkd & ~msk;
        PIN(p8[r >> 1]); PIN(p0[r >> 1]);
      }
      SBAR();
      pv(v8, p8);
      pv(v0, p0);
#pragma unroll
      for (int blk = 1; blk < 8; ++blk) {
        const f32x16 sc = sn;
        v4u vf[2];
        SBAR();
        if (blk < 7) sn = scores(kn[(blk + 1) & 1], negm);
        vfrag(blk, vf);
        if (blk < 6) kfrag(blk + 2, kn[blk & 1]);
        SBAR();
        if (blk == 3) touch_w2(0);
        unsigned pk[8];
#pragma unroll
        for (int r = 0; r < 16; r += 2) { pk[r >> 1] = pk2(__builtin_amdgcn_exp2f(sc[r]), __builtin_amdgcn_exp2f(sc[r + 1])); PIN(pk[r >> 1]); }
        SBAR();
        pv(vf, pk);
      }
    }
    STAMP(14);
    {
      const float den = __shfl(o[8], r32 + 32);
      const float inv = 1.0f / den;
      const int mt = 2 * qh + (r32 >> 4), tk = r32 & 15;
      const int obase = WS_O + ((pn * 8 + j) * 4 + mt) * 2560;
#pragma unroll
      for (int c = 0; c < 3; ++c) {
        const int Qd = 5 * hl + (c < 2 ? 2 * c + h : 4), nt = Qd >> 2, gg = Qd & 3;
        const int off = nt < 4 ? (nt >> 1) * 1024 + (tk + 16 * gg) * 16 + (nt & 1) * 8 : 2048 + (tk + 16 * gg) * 8;
        const v2u v = {pk2(o[4 * c] * inv, o[4 * c + 1] * inv), pk2(o[4 * c + 2] * inv, o[4 * c + 3] * inv)};
        if (c < 2 || h == 0) gst64(rs, obase + off, v);
      }
    }
    WAIT_BAR(0);
    if (tid == 0) set_flag(p.ws, WS_F2 + (pn * 8 + j) * FL_STRIDE, tag);
    if (w != 7) touch_w2(1);
  }
  const int pn = pg, j = jg;
  STAMP(5);

  v4u hres[2];
  hres[0] = hres[1] = v4u{0u, 0u, 0u, 0u};
  auto red_write = [&](const f32x4 (&acc)[2][5]) {
#pragma unroll
    for (int ml = 0; ml < 2; ++ml)
#pragma unroll
      for (int nt = 0; nt < 5; ++nt) *(LAS f32x4*)(smem + ((w * 10 + ml * 5 + nt) * 64 + lane) * 16) = acc[ml][nt];
  };
  auto red_sum = [&](int u, f32x4& s0, f32x4& s1) {
    const int ml = u / 3, part = u % 3;
    const int t0 = ml * 5 + 2 * part, t1 = part < 2 ? t0 + 1 : t0;
    f32x4 r0[4], r1[4];
#pragma unroll
    for (int ww = 0; ww < 4; ++ww) { r0[ww] = *(const LAS f32x4*)(smem + ((ww * 10 + t0) * 64 + lane) * 16); r1[ww] = *(const LAS f32x4*)(smem + ((ww * 10 + t1) * 64 + lane) * 16); }
    s0 = (r0[0] + r0[1]) + (r0[2] + r0[3]); s1 = (r1[0] + r1[1]) + (r1[2] + r1[3]);
#pragma unroll
    for (int ww = 0; ww < 4; ++ww) { r0[ww] = *(const LAS f32x4*)(smem + (((ww + 4) * 10 + t0) * 64 + lane) * 16); r1[ww] = *(const LAS f32x4*)(smem + (((ww + 4) * 10 + t1) * 64 + lane) * 16); }
    s0 += (r0[0] + r0[1]) + (r0[2] + r0[3]); s1 += (r1[0] + r1[1]) + (r1[2] + r1[3]);
  };
  {
#pragma unroll
    for (int ks = 0; ks < W1E; ++ks) { w1f[ks][0] = gld128(rs, w1base + ks * 1024); w1f[ks][1] = gld128(rs, w1base + (20 + ks) * 1024); }
    SBAR();
    if (w == 7) { poll_flags(p.ws, WS_F2 + (pn * 8 + (lane & 7)) * FL_STRIDE, lane < 8, tag); touch_w2(1); }
    LBAR();
    STAMP(6);
    v4u ba[4][3];
    {
      const int base = WS_O + (pn * 8 + w) * 10240;
#pragma unroll
      for (int mt = 0; mt < 4; ++mt) {
        ba[mt][0] = gld128(rs, base + mt * 2560 + lane * 16);
        ba[mt][1] = gld128(rs, base + mt * 2560 + 1024 + lane * 16);
        const v2u c = gld64(rs, base + mt * 2560 + 2048 + lane * 8);
        ba[mt][2] = v4u{c.x, c.y, 0u, 0u};
      }
      SBAR();
    }
    f32x4 acc[2][2][5];
#pragma unroll
    for (int hp = 0; hp < 2; ++hp) {
#pragma unroll
      for (int ml = 0; ml < 2; ++ml)
#pragma unroll
        for (int nt = 0; nt < 5; ++nt) acc[hp][ml][nt] = mfma16(wo[nt][0], ba[2 * hp + ml][0], f32x4{0, 0, 0, 0});
#pragma unroll
      for (int k = 1; k < 3; ++k)
#pragma unroll
        for (int ml = 0; ml < 2; ++ml)
#pragma unroll
          for (int nt = 0; nt < 5; ++nt) acc[hp][ml][nt] = mfma16(wo[nt][k], ba[2 * hp + ml][k], acc[hp][ml][nt]);
      if (hp == 0) red_write(acc[0]);
    }
    STAMP(26);
#pragma unroll
    for (int hp = 0; hp < 2; ++hp) {
      if (hp == 1) { LBAR(); red_write(acc[1]); }
      LBAR();
      if (hp == 1) STAMP(27);
      const int u = w - 2 * hp;
      if (u >= 0 && u < 6) {
        const int part = u % 3, mt = 2 * hp + u / 3;
        f32x4 s0, s1;
        red_sum(u, s0, s1);
        v4u hv;
        hv.x = pk2(s0[0] + bflo(xr[hp][0].x), s0[1] + bfhi(xr[hp][0].x));
        hv.y = pk2(s0[2] + bflo(xr[hp][0].y), s0[3] + bfhi(xr[hp][0].y));
        hv.z = pk2(s1[0] + bflo(xr[hp][1].x), s1[1] + bfhi(xr[hp][1].x));
        hv.w = pk2(s1[2] + bflo(xr[hp][1].y), s1[3] + bfhi(xr[hp][1].y));
        hres[hp] = hv;
        const int base = WS_H + (pn * 8 + j) * HB;
        if (part < 2) gst128(rs, base + mt * 2560 + part * 1024 + lane * 16, hv);
        else { gst64(rs, base + mt * 2560 + 2048 + lane * 8, v2u{hv.x, hv.y}); hv.z = 0u; hv.w = 0u; }
        float su = 0.f, sq = 0.f;
#pragma unroll
        for (int i = 0; i < 4; ++i) { const float a = bflo(hv[i]), c = bfhi(hv[i]); su += a + c; sq = fmaf(a, a, fmaf(c, c, sq)); }
        su = xsum(su); sq = xsum(sq);
        if (g == 0) gst64(rs, base + 10240 + ((mt * 3 + part) * 16 + tok) * 8, v2u{__builtin_bit_cast(unsigned, su), __builtin_bit_cast(unsigned, sq)});
      }
    }
    WAIT_BAR(0);
    if (tid == 0) set_flag(p.ws, WS_F3 + (pn * 8 + j) * FL_STRIDE, tag);
    if (w != 7) touch_w2(2);
#pragma unroll
    for (int ks = W1E; ks < 20; ++ks) { w1f[ks][0] = gld128(rs, w1base + ks * 1024); w1f[ks][1] = gld128(rs, w1base + (20 + ks) * 1024); }
    SBAR();
  }
  STAMP(7);

  v4u w2f[35];
  int kso[7];
  {
    const int nks = w < 6 ? 7 : 6, r0 = 0;
#pragma unroll
    for (int ks = 0; ks < 7; ++ks) { int a = ks + r0; if (a >= nks) a -= nks; kso[ks] = (ks == 6 && nks == 6) ? 0 : a; }
  }
  auto w2off = [&](int f) { return w2base + kso[f / 5] * 5120 + (f % 5) * 1024; };
  {
    constexpr int HL = 11776;
    LAS f32x2* st = (LAS f32x2*)(smem + 8 * HL);
    f32x4 acc[4][2], accC[2];
    accC[0] = accC[1] = f32x4{0, 0, 0, 0};
#pragma unroll
    for (int ks = 0; ks < 20; ++ks) { accC[0] = mfma16(w1f[ks][0], ONES, accC[0]); accC[1] = mfma16(w1f[ks][1], ONES, accC[1]); }
    SBAR();
    if (w == 7) { poll_flags(p.ws, WS_F3 + (pn * 8 + (lane & 7)) * FL_STRIDE, lane < 8, tag); touch_w2(2); }
    LBAR();
    STAMP(8);
    {
      const char* src = p.ws + (size_t)(WS_H + (pn * 8 + w) * HB + lane * 16);
#pragma unroll
      for (int i = 0; i < 11; ++i) glds16_sc1(src + i * 1024, lds0 + w * HL + i * 1024);
      if (lane < 32) glds16_sc1(src + 11 * 1024, lds0 + w * HL + 11 * 1024);
    }
    WAIT_BAR(0);
    STAMP(9);
    {
#pragma unroll
      for (int i = 0; i < W2L; ++i) glds16_sc1(p.ws + (size_t)(w2off(i) + lane * 16), lds0 + W2LOFF + (w * W2L + i) * 1024);
    }
    auto bfrag = [&](int ks, v4u (&bf)[4]) {
#pragma unroll
      for (int mt = 0; mt < 4; ++mt) {
        if (ks < 16) bf[mt] = lds128(L + (ks >> 1) * HL + mt * 2560 + (ks & 1) * 1024 + lane * 16);
        else {
          const v2u a = lds64(L + (2 * (ks - 16)) * HL + mt * 2560 + 2048 + lane * 8), c = lds64(L + (2 * (ks - 16) + 1) * HL + mt * 2560 + 2048 + lane * 8);
          bf[mt] = v4u{a.x, a.y, c.x, c.y};
        }
      }
      SBAR();
    };
    if (w == 7) {
      float su = 0.f, sq = 0.f;
#pragma unroll
      for (int q = 0; q < 8; ++q)
#pragma unroll
        for (int part = 0; part < 3; ++part) {
          const f32x2 v = *(const LAS f32x2*)(smem + q * HL + 10240 + ((g * 3 + part) * 16 + tok) * 8);
          su += v.x; sq += v.y;
        }
      const float mean = su * (1.0f / DM);
      const float var = fmaxf(sq * (1.0f / DM) - mean * mean, 0.f);
      st[lane] = f32x2{mean, 1.0f / sqrtf(var + LN_EPS)};
    }
#pragma unroll
    for (int i = 0; i < 4; ++i) { acc[i][0] = f32x4{0, 0, 0, 0}; acc[i][1] = f32x4{0, 0, 0, 0}; }
    v4u bf[2][4];
    bfrag(0, bf[0]);
#pragma unroll
    for (int ks = 0; ks < 20; ++ks) {
      if (ks + 1 < 20) bfrag(ks + 1, bf[(ks + 1) & 1]);
#pragma unroll
      for (int mt = 0; mt < 4; ++mt) {
        acc[mt][0] = mfma16(w1f[ks][0], bf[ks & 1][mt], acc[mt][0]);
        acc[mt][1] = mfma16(w1f[ks][1], bf[ks & 1][mt], acc[mt][1]);
      }
      SBAR();
      if (ks >= 2 && ks < 18) {
#pragma unroll
        for (int e = 0; e < (ks < 9 ? 2 : 1); ++e) { const int f = ks < 9 ? W2L + 2 * (ks - 2) + e : W2L + 14 + (ks - 9); w2f[f] = gld128(rs, w2off(f) + lane * 16); }
        SBAR();
      }
    }
    STAMP(28);
    LBAR();
    if (gemm) {
#pragma unroll
      for (int mt = 0; mt < 4; ++mt) {
        const f32x2 ms = st[mt * 16 + tok];
        float v[8];
#pragma unroll
        for (int e = 0; e < 2; ++e)
#pragma unroll
          for (int r = 0; r < 4; ++r) v[4 * e + r] = fmaxf((acc[mt][e][r] - ms.x * accC[e][r]) * ms.y, 0.f);
        gst128(rs, WS_H1 + (((pn * 8 + j) * 7 + w) * 4 + mt) * 1024 + lane * 16, v4u{pk2(v[0], v[1]), pk2(v[2], v[3]), pk2(v[4], v[5]), pk2(v[6], v[7])});
      }
    }
    WAIT_BAR(0);
    if (tid == 0) set_flag(p.ws, WS_F4 + (pn * 8 + j) * FL_STRIDE, tag);
  }
  STAMP(10);

  {
    const bool full = w < 6;
#pragma unroll
    for (int f = 0; f < W2L; ++f) w2f[f] = lds128(L + W2LOFF + (w * W2L + f) * 1024 + lane * 16);
#pragma unroll
    for (int f = 30; f < 35; ++f) w2f[f] = gld128(rs, (full ? w2off(f) : 0x7F000000) + lane * 16);
    SBAR();
    if (w == 7) poll_flags(p.ws, WS_F4 + (pn * 8 + (lane & 7)) * FL_STRIDE, lane < 8, tag);
    LBAR();
    STAMP(11);
    const int bbase = WS_H1 + (pn * 8 + w) * 28672 + lane * 16;
    v4u bf[2][7][2];
#pragma unroll
    for (int ks = 0; ks < 7; ++ks)
#pragma unroll
      for (int ml = 0; ml < 2; ++ml) bf[0][ks][ml] = gld128(rs, bbase + (kso[ks] * 4 + ml) * 1024);
    SBAR();
    f32x4 acc[2][2][5];
#pragma unroll
    for (int hp = 0; hp < 2; ++hp) {
#pragma unroll
      for (int ml = 0; ml < 2; ++ml)
#pragma unroll
        for (int nt = 0; nt < 5; ++nt) acc[hp][ml][nt] = f32x4{0, 0, 0, 0};
#pragma unroll
      for (int ks = 0; ks < 7; ++ks) {
#pragma unroll
        for (int ml = 0; ml < 2; ++ml)
#pragma unroll
          for (int nt = 0; nt < 5; ++nt) acc[hp][ml][nt] = mfma16(w2f[ks * 5 + nt], bf[hp][ks][ml], acc[hp][ml][nt]);
        SBAR();
        if (hp == 0) {
#pragma unroll
          for (int ml = 0; ml < 2; ++ml) bf[1][ks][ml] = gld128(rs, bbase + (kso[ks] * 4 + 2 + ml) * 1024);
          SBAR();
        }
      }
      if (hp == 0) { red_write(acc[0]); SBAR(); STAMP(15); }
    }
    STAMP(29);
#pragma unroll
    for (int hp = 0; hp < 2; ++hp) {
      if (hp == 1) { LBAR(); red_write(acc[1]); }
      LBAR();
      const int u = w - 2 * hp;
      if (u >= 0 && u < 6) {
        const int part = u % 3, mt = 2 * hp + u / 3;
        f32x4 s0, s1;
        red_sum(u, s0, s1);
        const v4u hv = hres[hp];
        int lz = lane; asm volatile("" : "+v"(lz));
        char* dst = p.out + (size_t)((64 * pn + 16 * mt + (lz & 15)) * 1280 + (80 * j + 32 * part + 4 * (lz >> 4)) * 2);
        *(GAS v2u*)dst = v2u{pk2(s0[0] + bflo(hv.x), s0[1] + bfhi(hv.x)), pk2(s0[2] + bflo(hv.y), s0[3] + bfhi(hv.y))};
        if (part < 2) *(GAS v2u*)(dst + 32) = v2u{pk2(s1[0] + bflo(hv.z), s1[1] + bfhi(hv.z)), pk2(s1[2] + bflo(hv.w), s1[3] + bfhi(hv.w))};
      }
    }
  }
  STAMP(12);
  STAMP_DUMP();
  if (b == 0 && w == 1 && lane == 0) set_flag(p.ws, WS_EPOCH, tag);
  asm volatile("mk_code_end:");
}
}

extern "C" void kernel_launch(void* const* d_in, const int* in_sizes, int n_in,
                              void* d_out, int out_size, void* d_ws, size_t ws_size,
                              hipStream_t stream) {
  (void)in_sizes; (void)n_in; (void)out_size; (void)ws_size;
  Params p;
  p.x = (const char*)d_in[0]; p.wqkv = (const char*)d_in[1]; p.wo = (const char*)d_in[2];
  p.w1 = (const char*)d_in[4]; p.w2 = (const char*)d_in[5];
  p.out = (char*)d_out; p.ws = (char*)d_ws;
  (void)hipFuncSetAttribute((const void*)block_kernel, hipFuncAttributeMaxDynamicSharedMemorySize, LDS_BYTES);
  block_kernel<<<256, 512, LDS_BYTES, stream>>>(p);
}
```

```cpp
#include <hip/hip_runtime.h>
#include <hip/hip_bf16.h>

#define GAS __attribute__((address_space(1)))
#define LAS __attribute__((address_space(3)))
#define DI __device__ __forceinline__
typedef unsigned v4u __attribute__((ext_vector_type(4)));
typedef unsigned v2u __attribute__((ext_vector_type(2)));
typedef float f32x2 __attribute__((ext_vector_type(2)));
typedef float f32x4 __attribute__((ext_vector_type(4)));
typedef float f32x16 __attribute__((ext_vector_type(16)));
typedef short bf16x8 __attribute__((ext_vector_type(8)));
typedef short bf16x4 __attribute__((ext_vector_type(4)));
typedef __bf16 bfx2 __attribute__((ext_vector_type(2)));
typedef unsigned long long u64;
typedef GAS u64 gu64;
#define RLX_AGENT __ATOMIC_RELAXED, __HIP_MEMORY_SCOPE_AGENT

namespace {
constexpr int SEQ = 2048, DM = 640, NQKV = 1920, DFF = 1728;
constexpr float LN_EPS = 1e-5f;
constexpr float QSCALE = 0.22360679774997896f * 1.4426950408889634f;

constexpr int FL_STRIDE = 128;
constexpr int WS_EPOCH = 0, WS_TMO = 256, WS_ARR = 1024;
constexpr int WS_F1 = 4096;
constexpr int WS_F2 = WS_F1 + 256 * FL_STRIDE;
constexpr int WS_F3 = WS_F2 + 256 * FL_STRIDE;
constexpr int WS_F4 = WS_F3 + 256 * FL_STRIDE;
constexpr int WS_F0 = WS_F4 + 256 * FL_STRIDE;
constexpr int WS_STAMP = WS_F0 + 256 * FL_STRIDE;
constexpr int WS_SP = 512 * 1024;
constexpr int WS_CP = WS_SP + 16 * 10 * 1024;
constexpr int WS_QKV = 1 << 20;
constexpr int WS_O = WS_QKV + 480 * 2048 * 8;
constexpr int HB = 12288;
constexpr int WS_H = WS_O + 32 * 81920;
constexpr int WS_H1 = WS_H + 256 * HB;
constexpr int WS_WOP = WS_H1 + 32 * 8 * 28672;
constexpr int WS_W1P = WS_WOP + 64 * 12800;
constexpr int WS_W2P = WS_W1P + 108 * 20 * 1024;
constexpr int WS_END = WS_W2P + 64 * 35 * 1024;

constexpr int LDS_BYTES = 160 * 1024;
constexpr unsigned SPIN_CAP = 1u << 18;
constexpr int AUX_SC1 = 16;
#define ATT_THR 8.0f
#define P1_SLEEP 8

struct Params {
  const char* x; const char* wqkv; const char* wo; const char* w1; const char* w2;
  char* out; char* ws;
};

DI f32x4 mfma16(v4u a, v4u b, f32x4 c) {
  return __builtin_amdgcn_mfma_f32_16x16x32_bf16(__builtin_bit_cast(bf16x8, a), __builtin_bit_cast(bf16x8, b), c, 0, 0, 0);
}
DI f32x4 mfma16h(v2u a, v2u b, f32x4 c) {
  return __builtin_amdgcn_mfma_f32_16x16x16bf16_1k(__builtin_bit_cast(bf16x4, a), __builtin_bit_cast(bf16x4, b), c, 0, 0, 0);
}
DI f32x16 mfma32(v4u a, v4u b, f32x16 c) {
  return __builtin_amdgcn_mfma_f32_32x32x16_bf16(__builtin_bit_cast(bf16x8, a), __builtin_bit_cast(bf16x8, b), c, 0, 0, 0);
}
DI unsigned pk2(float lo, float hi) {
  f32x2 v = {lo, hi};
  return __builtin_bit_cast(unsigned, __builtin_convertvector(v, bfx2));
}
DI float asf(unsigned u) { return __builtin_bit_cast(float, u); }
DI unsigned asu(float f) { return __builtin_bit_cast(unsigned, f); }
typedef short s16x2 __attribute__((ext_vector_type(2)));
DI unsigned relu2(unsigned u) {
  const s16x2 v = __builtin_bit_cast(s16x2, u);
  return __builtin_bit_cast(unsigned, __builtin_elementwise_max(v, s16x2{0, 0}));
}
DI u64 uni64(unsigned lo, unsigned hi) {
  const unsigned l = (unsigned)__builtin_amdgcn_readfirstlane(lo), h = (unsigned)__builtin_amdgcn_readfirstlane(hi);
  return ((u64)h << 32) | (u64)l;
}
DI float bflo(unsigned u) { return __builtin_bit_cast(float, u << 16); }
DI float bfhi(unsigned u) { return __builtin_bit_cast(float, u & 0xffff0000u); }

DI float xsum(float v) {
  const unsigned u = __builtin_bit_cast(unsigned, v);
  const auto a = __builtin_amdgcn_permlane32_swap(u, u, false, false);
  const float s = __builtin_bit_cast(float, (unsigned)a[0]) + __builtin_bit_cast(float, (unsigned)a[1]);
  const unsigned w = __builtin_bit_cast(unsigned, s);
  const auto c = __builtin_amdgcn_permlane16_swap(w, w, false, false);
  return __builtin_bit_cast(float, (unsigned)c[0]) + __builtin_bit_cast(float, (unsigned)c[1]);
}

DI void glds16(const char* g, unsigned lds_base) {
  unsigned sv;
  asm volatile("s_mov_b32 %0, m0\n\ts_mov_b32 m0, %2\n\ts_nop 0\n\tglobal_load_lds_dwordx4 %1, off\n\ts_mov_b32 m0, %0"
               : "=&s"(sv) : "v"(g), "s"(lds_base) : "memory");
}
DI void glds16_sc1(const char* g, unsigned lds_base) {
  unsigned sv;
  asm volatile("s_mov_b32 %0, m0\n\ts_mov_b32 m0, %2\n\ts_nop 0\n\tglobal_load_lds_dwordx4 %1, off sc1\n\ts_mov_b32 m0, %0"
               : "=&s"(sv) : "v"(g), "s"(lds_base) : "memory");
}
DI void touch4(const char* g, unsigned lds_base) {
  unsigned sv;
  asm volatile("s_mov_b32 %0, m0\n\ts_mov_b32 m0, %2\n\ts_nop 0\n\tglobal_load_lds_dword %1, off sc1\n\ts_mov_b32 m0, %0"
               : "=&s"(sv) : "v"(g), "s"(lds_base) : "memory");
}
#define WAITV(N) asm volatile("s_waitcnt vmcnt(" #N ")" ::: "memory")
#define WAIT_BAR(N) asm volatile("s_waitcnt vmcnt(" #N ") lgkmcnt(0)\n\ts_barrier" ::: "memory")
#define LBAR() asm volatile("s_waitcnt lgkmcnt(0)\n\ts_barrier" ::: "memory")
#define SBAR() __builtin_amdgcn_sched_barrier(0)
#define PIN(x) asm volatile("" : "+v"(x))

typedef LAS const char* lptr;
DI v4u lds128(lptr p) { return *(const LAS v4u*)p; }
DI v2u lds64(lptr p) { return *(const volatile LAS v2u*)p; }
DI v2u ldstr(lptr p) { return __builtin_bit_cast(v2u, __builtin_amdgcn_ds_read_tr16_b64_v4i16((LAS bf16x4*)p)); }

DI v4u gld128(__amdgpu_buffer_rsrc_t rs, int off) { return __builtin_amdgcn_raw_buffer_load_b128(rs, off, 0, AUX_SC1); }
DI v2u gld64(__amdgpu_buffer_rsrc_t rs, int off) { return __builtin_amdgcn_raw_buffer_load_b64(rs, off, 0, AUX_SC1); }
DI void gst128(__amdgpu_buffer_rsrc_t rs, int off, v4u v) { __builtin_amdgcn_raw_buffer_store_b128(v, rs, off, 0, AUX_SC1); }
DI void gst64(__amdgpu_buffer_rsrc_t rs, int off, v2u v) { __builtin_amdgcn_raw_buffer_store_b64(v, rs, off, 0, AUX_SC1); }

DI void poll_flags(char* ws, int off, bool want, u64 tag) {
  gu64* f = (gu64*)(ws + off);
  for (unsigned spins = 0;;) {
    u64 v = tag;
    if (want) v = __hip_atomic_load(f, RLX_AGENT);
    if (__all(v == tag)) break;
    if (++spins > SPIN_CAP) { __hip_atomic_store((gu64*)(ws + WS_TMO), tag, RLX_AGENT); break; }
    if ((spins & 1023u) == 0u && __builtin_amdgcn_readfirstlane((unsigned)(__hip_atomic_load((gu64*)(ws + WS_TMO), RLX_AGENT) == tag))) break;
  }
  __builtin_amdgcn_fence(__ATOMIC_ACQUIRE, "wavefront");
}
DI void poll_arr(char* ws, u64 tag, int lane) {
  gu64* f = (gu64*)(ws + WS_ARR) + lane;
  for (unsigned spins = 0;;) {
    const u64 a = __hip_atomic_load(f, RLX_AGENT), b = __hip_atomic_load(f + 64, RLX_AGENT), c = __hip_atomic_load(f + 128, RLX_AGENT), d = __hip_atomic_load(f + 192, RLX_AGENT);
    if (__all(a == tag && b == tag && c == tag && d == tag)) break;
    if (++spins > SPIN_CAP) { __hip_atomic_store((gu64*)(ws + WS_TMO), tag, RLX_AGENT); break; }
    if ((spins & 1023u) == 0u && __builtin_amdgcn_readfirstlane((unsigned)(__hip_atomic_load((gu64*)(ws + WS_TMO), RLX_AGENT) == tag))) break;
  }
  __builtin_amdgcn_fence(__ATOMIC_ACQUIRE, "wavefront");
}
DI void ring_wait(volatile LAS unsigned* r, unsigned want) {
  for (;;) {
    const v4u f = *(const volatile LAS v4u*)r;
    if ((unsigned)__builtin_amdgcn_readfirstlane(min(min(f.x, f.y), min(f.z, f.w))) >= want) break;
    __builtin_amdgcn_s_sleep(1);
  }
  asm volatile("" ::: "memory");
}
DI void ring_wait2(volatile LAS unsigned* r, unsigned want) {
  for (;;) {
    const v2u f = *(const volatile LAS v2u*)r;
    if ((unsigned)__builtin_amdgcn_readfirstlane(min(f.x, f.y)) >= want) break;
    __builtin_amdgcn_s_sleep(1);
  }
  asm volatile("" ::: "memory");
}
DI void set_flag(char* ws, int off, u64 tag) { __hip_atomic_store((gu64*)(ws + off), tag, RLX_AGENT); }

#define STAMP(i) do {} while (0)
#define STAMP_DUMP() do {} while (0)
#define STAMPC(i) do {} while (0)
#define STAMPL(i) do {} while (0)

DI void pack_src(const Params& p, int u, int lane, const char*& s0, const char*& s1, int& pitch) {
  const char* m; int r0, r1, cg;
  if (u < 240) {
    const int ru = u / 10, w = ru / 3, part = ru - 3 * w;
    cg = u - 10 * ru; m = p.wo; pitch = 1280; r0 = 80 * w + 32 * part; r1 = part == 2 ? r0 : r0 + 16;
  } else if (u < 780) {
    const int v = u - 240, ks = v / 27;
    cg = v - 27 * ks; m = p.w1; pitch = 3456;
    if (ks < 16) { r0 = 80 * (ks >> 1) + 32 * (ks & 1); r1 = r0 + 16; } else { r0 = 160 * (ks - 16) + 64; r1 = r0 + 80; }
  } else {
    const int v = u - 780, ks = v / 10;
    cg = v - 10 * ks; m = p.w2; pitch = 1280; r0 = 32 * ks; r1 = r0 + 16;
  }
  const int lr = lane >> 3, c = (lane & 7) ^ (((lr >> 1) & 3) << 1);
  s0 = m + (size_t)((r0 + lr) * pitch + cg * 128 + c * 16);
  s1 = m + (size_t)((r1 + lr) * pitch + cg * 128 + c * 16);
}
DI void pack_issue(const char* s0, const char* s1, int pitch, unsigned lds) {
  glds16(s0, lds); glds16(s0 + 8 * pitch, lds + 1024); glds16(s1, lds + 2048); glds16(s1 + 8 * pitch, lds + 3072);
}

DI void pack_dest(int u, int (&d)[4], bool& half) {
  int T0, base, sj, sn;
  half = false;
  if (u < 240) {
    const int ru = u / 10, w = ru / 3, part = ru - 3 * w;
    T0 = 4 * (u - 10 * ru); base = WS_WOP + w * 12800 + part * 1024; sj = 8 * 12800; sn = 2560; half = part == 2;
  } else if (u < 780) {
    const int v = u - 240, ks = v / 27;
    T0 = 4 * (v - 27 * ks); base = WS_W1P + ks * 1024; sj = 5 * 20480; sn = 20480;
  } else {
    const int v = u - 780, ru = v / 10;
    const int w = ru < 42 ? ru / 7 : 6 + (ru - 42) / 6, ks = ru < 42 ? ru - 7 * w : ru - 42 - 6 * (w - 6);
    T0 = 4 * (v - 10 * ru); base = WS_W2P + w * 35840 + ks * 5120; sj = 8 * 35840; sn = 1024;
  }
#pragma unroll
  for (int t = 0; t < 4; ++t) { const int T = T0 + t, jj = T / 5; d[t] = base + jj * sj + (T - 5 * jj) * sn; }
}
DI void pack_finish(__amdgpu_buffer_rsrc_t rs, lptr L, const int (&d)[4], bool half, int lane) {
  const int row = 4 * (lane >> 4) + ((lane & 15) >> 2);
  const int a0 = row * 128 + (lane & 3) * 8, sw = (row >> 1) & 3;
  v2u lo[4], hi[4];
#pragma unroll
  for (int t = 0; t < 4; ++t) { lo[t] = ldstr(L + a0 + ((t ^ sw) * 32)); hi[t] = ldstr(L + a0 + ((t ^ sw) * 32) + 2048); }
  if (half) {
#pragma unroll
    for (int t = 0; t < 4; ++t) gst64(rs, d[t] + lane * 8, lo[t]);
  } else {
#pragma unroll
    for (int t = 0; t < 4; ++t) gst128(rs, d[t] + lane * 16, v4u{lo[t].x, lo[t].y, hi[t].x, hi[t].y});
  }
}

__global__ void __launch_bounds__(512, 2) block_kernel(Params p) {
  extern __shared__ __attribute__((aligned(16))) char smem[];
  const int tid = threadIdx.x, lane = tid & 63, w = __builtin_amdgcn_readfirstlane(tid >> 6);
  const int tok = lane & 15, g = lane >> 4;
  const int b = blockIdx.x, xcd = b & 7, t = b >> 3;
  const unsigned lds0 = (unsigned)(size_t)smem;
  lptr L = (lptr)smem;
  const __amdgpu_buffer_rsrc_t rs = __builtin_amdgcn_make_buffer_rsrc(p.ws, 0, WS_END, 0x00020000);
  STAMP(0);
  {
    unsigned len;
    asm volatile("mk_code_begin:\n\ts_mov_b32 %0, mk_code_end-mk_code_begin" : "=s"(len));
    const char* pc = (const char*)__builtin_amdgcn_s_getpc();
    if (w < 4) for (unsigned off = (unsigned)tid * 128u; off < len; off += 256u * 128u) touch4(pc + off, lds0 + LDS_BYTES - 256);
  }
  auto read_tag = [&]() -> u64 {
    const u64 e0 = __hip_atomic_load((gu64*)(p.ws + WS_EPOCH), RLX_AGENT);
    return uni64((unsigned)e0, (unsigned)(e0 >> 32)) + 0x9E3779B97F4A7C15ull;
  };
  const v4u ONES = {0x3F803F80u, 0x3F803F80u, 0x3F803F80u, 0x3F803F80u};
  u64 tag;
  LAS unsigned* cnt = (LAS unsigned*)(smem + LDS_BYTES - 1792);
  if (tid < 8) cnt[tid] = 0u;
  auto publish = [&](int which, unsigned n, int flag_off) {
    WAITV(0);
    unsigned old = 0;
    if (lane == 0) old = __hip_atomic_fetch_add(cnt + which, 1u, __ATOMIC_RELAXED, __HIP_MEMORY_SCOPE_WORKGROUP);
    if (lane == 0 && old == n - 1u) set_flag(p.ws, flag_off, tag);
  };

  auto poll_early = [&]() { poll_flags(p.ws, WS_ARR + ((28 + (lane & 3)) * 8 + 4 + ((lane >> 2) & 3)) * 8, lane < 16, tag); };

  {
    const int P128 = 4 * (xcd & 3) + (t & 3), s = (xcd >> 2) * 8 + (t >> 2);
    if (s < 15) {
      LAS f32x2* st = (LAS f32x2*)(smem + 131072);
      LAS float* cs = (LAS float*)(smem + 132096);
      volatile LAS unsigned* ring = (volatile LAS unsigned*)(smem + 132640);
      const int q4 = (lane & 15) >> 2, pp = lane & 3;
      const int fsw = q4 | ((g & 1) << 2);
      const int xlane = tok * 128 + ((g ^ (tok >> 1)) * 16);
      const int wlane = 16384 + (8 * g + q4) * 256 + pp * 8;
      const int rot = (3 * (s + P128)) % 10;
      if (w >= 4) {
        const int a = w - 4, lt = tid - 256;
        const char* xs; const char* wsrc;
        { const int row = lt >> 3, c = (lt & 7) ^ ((row >> 1) & 7); xs = p.x + (size_t)((P128 * 128 + row) * 1280 + c * 16); }
        { const int row = lt >> 4, f = (row & 3) | (((row >> 3) & 1) << 2), c = (lt & 15) ^ (f << 1); wsrc = p.wqkv + (size_t)(row * 3840 + s * 256 + c * 16); }
        auto stage = [&](int i) {
          int kc = i + rot; if (kc >= 10) kc -= 10;
          const unsigned d = lds0 + (i & 3) * 32768 + a * 1024;
          const char* xk = xs + kc * 128; const char* wk = wsrc + (size_t)(kc * (64 * 3840));
#pragma unroll
          for (int q = 0; q < 4; ++q) { glds16(wk + q * (16 * 3840), d + 16384 + q * 4096); glds16(xk + q * (32 * 1280), d + q * 4096); }
        };
        stage(0);
        __builtin_amdgcn_s_sleep(P1_SLEEP);
        stage(1); stage(2);
        WAIT_BAR(16);
        {
          const v2u tv = *(const LAS v2u*)(smem + 132616);
          tag = uni64(tv.x, tv.y);
        }
        v4u xb[2][2], wa[2][2];
#pragma unroll
        for (int k2 = 0; k2 < 2; ++k2)
#pragma unroll
          for (int e = 0; e < 2; ++e) {
            xb[k2][e] = lds128(L + (2 * a + e) * 2048 + (xlane ^ (k2 * 64)));
            const int wo0 = wlane + (((2 * a + e) ^ fsw) * 32) + k2 * 8192;
            const v2u lo = ldstr(L + wo0), hi = ldstr(L + wo0 + 1024);
            wa[k2][e] = v4u{lo.x, lo.y, hi.x, hi.y};
          }
        SBAR();
        stage(3);
#pragma unroll 1
        for (int i = 0; i < 7; ++i) {
          if (i == 0 || i == 4) WAITV(16); else if (i < 4) WAITV(20); else if (i < 6) WAITV(17); else WAITV(0);
          LBAR();
          if (i == 4) gst64(rs, tid == 256 ? WS_F0 + (P128 * 15 + s) * FL_STRIDE : 0x7F000000, v2u{(unsigned)tag, (unsigned)(tag >> 32)});
          if (i + 4 < 10) stage(i + 4);
          if (i == 0) {
            const int r = tok & 3;
#pragma unroll
            for (int e = 0; e < 2; ++e) {
              const f32x4 aC = mfma16(wa[1][e], ONES, mfma16(wa[0][e], ONES, f32x4{0, 0, 0, 0}));
              const f32x4 aS = mfma16(ONES, xb[1][e], mfma16(ONES, xb[0][e], f32x4{0, 0, 0, 0}));
              const f32x4 aG = mfma16(xb[1][e], xb[1][e], mfma16(xb[0][e], xb[0][e], f32x4{0, 0, 0, 0}));
              const float G = r == 0 ? aG[0] : r == 1 ? aG[1] : r == 2 ? aG[2] : aG[3];
              gst64(rs, (s < 10 && g == (tok >> 2)) ? WS_SP + ((P128 * 10 + rot) * 128 + (2 * a + e) * 16 + tok) * 8 : 0x7F000000, v2u{asu(aS[0]), asu(G)});
              gst128(rs, (P128 < 10 && tok == 0) ? WS_CP + ((s * 10 + rot) * 128 + (2 * a + e) * 16 + 4 * g) * 4 : 0x7F000000, __builtin_bit_cast(v4u, aC));
            }
          }
        }
        if (a < 2) {
          poll_flags(p.ws, WS_F0 + (a == 0 ? P128 * 15 + (lane < 10 ? lane : 0) : (lane < 10 ? lane : 0) * 15 + s) * FL_STRIDE, lane < 10, tag);
          if (a == 0) {
            v4u v[10];
#pragma unroll
            for (int c = 0; c < 10; ++c) v[c] = gld128(rs, WS_SP + ((P128 * 10 + c) * 128 + 2 * lane) * 8);
            float su = 0.f, sq = 0.f, tu = 0.f, tq = 0.f;
#pragma unroll
            for (int c = 0; c < 10; ++c) { su += asf(v[c].x); sq += asf(v[c].y); tu += asf(v[c].z); tq += asf(v[c].w); }
            const float m0 = su * (1.0f / DM), m1 = tu * (1.0f / DM);
            st[2 * lane] = f32x2{m0, 1.0f / sqrtf(fmaxf(sq * (1.0f / DM) - m0 * m0, 0.f) + LN_EPS)};
            st[2 * lane + 1] = f32x2{m1, 1.0f / sqrtf(fmaxf(tq * (1.0f / DM) - m1 * m1, 0.f) + LN_EPS)};
          } else {
            v2u v[10];
#pragma unroll
            for (int c = 0; c < 10; ++c) v[c] = gld64(rs, WS_CP + ((s * 10 + c) * 128 + 2 * lane) * 4);
            float su = 0.f, tu = 0.f;
#pragma unroll
            for (int c = 0; c < 10; ++c) { su += asf(v[c].x); tu += asf(v[c].y); }
            cs[2 * lane] = su; cs[2 * lane + 1] = tu;
          }
        } else if (a == 2) {
          poll_early();
        }
        LBAR();
        {
          const int pu = 375 + P128 * 15 + s + 240 * a;
          if (pu < 1320) { int pd[4]; bool phalf; pack_dest(pu, pd, phalf); pack_finish(rs, L + 133120 + a * 4096, pd, phalf, lane); }
          publish(4, 4u, WS_ARR + b * 8);
        }

      } else {
        const int mh = w >> 1, nh = w & 1;
        int xa[2], wq[4];
        xa[0] = mh * 8192 + xlane; xa[1] = xa[0] ^ 64;
#pragma unroll
        for (int n = 0; n < 4; ++n) wq[n] = wlane + (((4 * nh + n) ^ fsw) * 32);
        f32x4 acc[4][4];
#pragma unroll
        for (int m = 0; m < 4; ++m)
#pragma unroll
          for (int n = 0; n < 4; ++n) acc[m][n] = f32x4{0, 0, 0, 0};
        v4u xb[2][4], wa[2][4];
        auto rdfrag = [&](int k2) {
#pragma unroll
          for (int n = 0; n < 4; ++n) {
            const v2u lo = ldstr(L + wq[n] + k2 * 8192), hi = ldstr(L + wq[n] + k2 * 8192 + 1024);
            wa[k2][n] = v4u{lo.x, lo.y, hi.x, hi.y};
            xb[k2][n] = lds128(L + xa[k2] + n * 2048);
          }
        };
        auto mm = [&](int k2) {
#pragma unroll
          for (int m = 0; m < 4; ++m)
#pragma unroll
            for (int n = 0; n < 4; ++n) acc[m][n] = mfma16(wa[k2][n], xb[k2][m], acc[m][n]);
#pragma unroll
          for (int q = 0; q < 12; ++q) { __builtin_amdgcn_sched_group_barrier(0x008, 1, 0); __builtin_amdgcn_sched_group_barrier(0x100, 1, 0); }
          __builtin_amdgcn_sched_group_barrier(0x008, 4, 0);
          SBAR();
        };
        tag = read_tag();
        if (tid == 0) *(LAS u64*)(smem + 132616) = tag;
        if (tid < 12) ((LAS unsigned*)(smem + 132624))[tid] = 0u;
        const int pu = 375 + P128 * 15 + s + 240 * w;
        const char *ps0, *ps1; int ppitch;
        pack_src(p, pu < 1320 ? pu : 0, lane, ps0, ps1, ppitch);
        LBAR();
        STAMP(16);
        rdfrag(0);
        pack_issue(ps0, ps1, ppitch, lds0 + 133120 + w * 4096);
        SBAR();
#pragma unroll 1
        for (int i = 0; i < 10; ++i) {
          rdfrag(1);
          mm(0);
#pragma unroll
          for (int n = 0; n < 4; ++n) wq[n] = (wq[n] + 32768) & 0x1FFFF;
          xa[0] = (xa[0] + 32768) & 0x1FFFF; xa[1] = (xa[1] + 32768) & 0x1FFFF;
          if (i < 7) LBAR();
          SBAR();
          rdfrag(0);
          mm(1);
        }
        STAMP(1);
        WAIT_BAR(0);
        STAMP(17);
        const float qs = s < 5 ? QSCALE : 1.0f;
        f32x4 c4[4];
#pragma unroll
        for (int n = 0; n < 4; ++n) c4[n] = *(const LAS f32x4*)(cs + (4 * nh + n) * 16 + 4 * g);
        const bool odd = tok & 1;
#pragma unroll
        for (int m = 0; m < 4; ++m) {
          const int mt = 4 * mh + m;
          const f32x2 ms = st[mt * 16 + tok];
          const float sc = ms.y * qs;
#pragma unroll
          for (int n2 = 0; n2 < 2; ++n2) {
            unsigned pk[2][2];
#pragma unroll
            for (int e = 0; e < 2; ++e) {
              const f32x4 a4 = acc[m][2 * n2 + e], cc = c4[2 * n2 + e];
              pk[e][0] = pk2((a4[0] - ms.x * cc[0]) * sc, (a4[1] - ms.x * cc[1]) * sc);
              pk[e][1] = pk2((a4[2] - ms.x * cc[2]) * sc, (a4[3] - ms.x * cc[3]) * sc);
            }
            const unsigned s0 = odd ? pk[0][0] : pk[1][0], s1 = odd ? pk[0][1] : pk[1][1];
            const unsigned r0 = (unsigned)__builtin_amdgcn_mov_dpp((int)s0, 0xB1, 0xF, 0xF, true);
            const unsigned r1 = (unsigned)__builtin_amdgcn_mov_dpp((int)s1, 0xB1, 0xF, 0xF, true);
            const v4u v = odd ? v4u{r0, r1, pk[1][0], pk[1][1]} : v4u{pk[0][0], pk[0][1], r0, r1};
            const int quad = 32 * s + 4 * (4 * nh + 2 * n2 + (odd ? 1 : 0)) + g;
            gst128(rs, WS_QKV + (quad * 2048 + P128 * 128 + mt * 16 + (tok & 14)) * 8, v);
          }
        }
      }
      STAMP(18);
      if (w < 4) publish(0, 4u, WS_F1 + (P128 * 15 + s) * FL_STRIDE);
    } else {
      const int v = ((xcd - 4) * 4 + (t - 28)) * 8 + w;
#pragma unroll 1
      for (int i = 0; i < 3; ++i) {
        const char *ps0, *ps1; int ppitch;
        pack_src(p, v + 128 * i < 375 ? v + 128 * i : 0, lane, ps0, ps1, ppitch);
        pack_issue(ps0, ps1, ppitch, lds0 + (w * 3 + i) * 4096);
      }
      tag = read_tag();
#pragma unroll 1
      for (int i = 0; i < 3; ++i) {
        const int u = v + 128 * i;
        if (u < 375) { int pd[4]; bool phalf; pack_dest(u, pd, phalf); pack_finish(rs, L + (w * 3 + i) * 4096, pd, phalf, lane); }
      }
      WAIT_BAR(0);
      if (tid == 0) set_flag(p.ws, WS_ARR + b * 8, tag);
      if (w == 6) poll_early();
      LBAR();
    }
  }
  STAMP(2);

  const int pg = xcd * 4 + (t & 3), jg = t >> 2;
  auto touch_w2 = [&](int part) {
    const char* a = p.ws + (size_t)(WS_W2P + (t * 8 + w) * 8960 + (part * 24 + lane) * 128);
    if (lane < (part < 2 ? 24 : 22)) touch4(a, lds0 + LDS_BYTES - 256);
  };
  v4u wo[5][3];
  v2u xr[3];
  const int ntile = jg < 6 ? 14 : 12, tbase = jg < 7 ? 14 * jg : 96;
  const bool gemm = 2 * w < ntile;
  constexpr int W1E = 8;
  constexpr int W1M = 16;
  constexpr int W2L = 7;
  constexpr int W2LOFF = 8 * 11776 + 1024;
  v4u w1f[20][2];
  const int w1base = gemm ? WS_W1P + (tbase + 2 * w) * 20480 + lane * 16 : 0x7F000000;
  const int w2base = WS_W2P + (jg * 8 + w) * 35840;
  {
    constexpr int PL = 2592, QOFF = 40 * PL, ONESOFF = QOFF + 20 * 512;
    const int pn = t, j = xcd;
    const int tok0 = 64 * pn - 256;
    const int wobase = WS_WOP + (jg * 8 + w) * 12800;
    auto wo_loads = [&](int i) {
      if (i < 5) {
        wo[i][0] = gld128(rs, wobase + i * 2560 + lane * 16);
        wo[i][1] = gld128(rs, wobase + i * 2560 + 1024 + lane * 16);
        wo[i][2] = gld128(rs, wobase + i * 2560 + 2048 + lane * 8);
      } else {
        const char* src = p.x + (size_t)((64 * pg + 16 * (w >> 1) + tok) * 1280 + (80 * jg + 32 * (w & 1) + 4 * g) * 2);
        xr[0] = *(const GAS v2u*)src;
        xr[1] = *(const GAS v2u*)(src + 32);
        xr[2] = *(const GAS v2u*)(p.x + (size_t)((64 * pg + 16 * (w & 3) + tok) * 1280 + (80 * jg + 64 + 4 * g) * 2));
      }
      SBAR();
    };
    auto pre = [&](int i) {
      const int q = t * 8 + w;
      if (q < 216 && lane < 16) touch4(p.ws + (size_t)(WS_W1P + (q >> 1) * 20480 + (32 * i + 16 * (q & 1) + lane) * 128), lds0 + LDS_BYTES - 256);
    };
    auto wo_all = [&]() {
#pragma unroll
      for (int i = 0; i < 6; ++i) wo_loads(i);
    };
    if (w != 5) wo_all();
    if (w == 5) {
      const int R = lane / 6, rem = lane % 6, pi = rem >> 1;
      const int q0 = 160 * R + 20 * j, sl = (rem & 1) ? (q0 + 19) >> 5 : q0 >> 5;
      const int P = (pn >> 1) - pi;
      const bool want = lane < 18 && P >= 0 && (R > 0 || pi == 0);
      poll_flags(p.ws, WS_F1 + ((want ? P : 0) * 15 + (want ? sl : 0)) * FL_STRIDE, want, tag);
    }
    LBAR();
    STAMP(3);
#pragma unroll
    for (int i = 0; i < 5; ++i) {
      const int pl = 5 * w + i;
      const int quad = (pl < 20 ? 160 + pl : 300 + pl) + 20 * j;
      const char* src = p.ws + (size_t)(WS_QKV + (quad * 2048 + tok0) * 8 + lane * 16);
#pragma unroll
      for (int c = 0; c < 3; ++c) {
        const int tk = tok0 + c * 128 + lane * 2;
        if (tk >= 0 && (c < 2 || lane < 32)) glds16_sc1(src + c * 1024, lds0 + pl * PL + c * 1024);
      }
    }
    if (w < 5) {
#pragma unroll
      for (int i = 0; i < 2; ++i) {
        const int pl = 4 * w + 2 * i + (lane >> 5);
        const char* src = p.ws + (size_t)(WS_QKV + ((20 * j + pl) * 2048 + 64 * pn) * 8 + (lane & 31) * 16);
        glds16_sc1(src, lds0 + QOFF + (4 * w + 2 * i) * 512);
      }
    }
    if (tid < 352) *(LAS v2u*)(smem + ONESOFF + tid * 8) = v2u{0x00003F80u, 0u};
    if (pn < 4) {
      const int n16 = -tok0 / 2;
#pragma unroll 1
      for (int i = tid; i < 40 * n16; i += 512) { const int pl = i / n16; *(LAS v4u*)(smem + pl * PL + (i - pl * n16) * 16) = v4u{0u, 0u, 0u, 0u}; }
    }
    if (w == 6) poll_arr(p.ws, tag, lane);
    WAIT_BAR(0);
    STAMP(4);
    if (w == 5) wo_all();
    pre(0);
    const int hl = w >> 1, qh = w & 1, r32 = lane & 31, h = lane >> 5;
    const int bmin = 8 - 2 * pn - qh;
    v4u qf0, qf1;
    {
      const v2u a = lds64(L + QOFF + (5 * hl + 2 * h) * 512 + (32 * qh + r32) * 8), c = lds64(L + QOFF + (5 * hl + 2 * h + 1) * 512 + (32 * qh + r32) * 8);
      qf0 = v4u{a.x, a.y, c.x, c.y};
      const v2u d = lds64(L + QOFF + (5 * hl + 4) * 512 + (32 * qh + r32) * 8);
      qf1 = h ? v4u{0u, 0u, 0u, 0u} : v4u{d.x, d.y, 0x00003F80u, 0u};
    }
    lptr kp0 = L + (5 * hl + 2 * h) * PL + (32 * qh + r32) * 8;
    lptr kp1 = L + (5 * hl + 4) * PL + (32 * qh + r32) * 8;
    auto kfrag = [&](int blk, v4u (&kf)[2]) {
      const v2u a = lds64(kp0 + blk * 256), d = lds64(kp0 + PL + blk * 256), e = lds64(kp1 + blk * 256);
      kf[0] = v4u{a.x, a.y, d.x, d.y};
      kf[1] = v4u{e.x, e.y, blk >= bmin ? 0u : 0x0000F149u  , 0u};
    };
    auto scores = [&](const v4u (&kf)[2], f32x16 c) -> f32x16 { return mfma32(kf[1], qf1, mfma32(kf[0], qf0, c)); };
    f32x16 zero16;
#pragma unroll
    for (int r = 0; r < 16; ++r) zero16[r] = 0.f;
    const int blk16 = (lane >> 4) & 1, q4 = (lane & 15) >> 2, pp = lane & 3;
    const int vplane = (blk16 == 0) ? (20 + 5 * hl + pp) * PL : (pp == 0 ? (24 + 5 * hl) * PL : ONESOFF + (((20 + 5 * hl) * 32 + 160) & 255));
    lptr vp = L + vplane + (32 * qh + 4 * h + q4) * 8;
    f32x16 o = zero16;
    auto vfrag = [&](int blk, v4u (&vf)[2]) {
#pragma unroll
      for (int s2 = 0; s2 < 2; ++s2) {
        const v2u a = ldstr(vp + blk * 256 + s2 * 128), c = ldstr(vp + blk * 256 + s2 * 128 + 64);
        vf[s2] = v4u{a.x, a.y, c.x, c.y};
      }
    };
    auto pv = [&](const v4u (&vf)[2], const unsigned (&pk)[8]) {
      o = mfma32(vf[0], v4u{pk[0], pk[1], pk[2], pk[3]}, o);
      o = mfma32(vf[1], v4u{pk[4], pk[5], pk[6], pk[7]}, o);
    };
    auto xmax = [&](float v) {
      const unsigned u = asu(v);
      const auto sw = __builtin_amdgcn_permlane32_swap(u, u, false, false);
      return fmaxf(asf((unsigned)sw[0]), asf((unsigned)sw[1]));
    };
    {
      constexpr float THR = ATT_THR;
      v4u k8[2], k0[2], kn[2][2], v8[2], v0[2];
      kfrag(8, k8); kfrag(0, k0); kfrag(1, kn[1]); kfrag(2, kn[0]);
      vfrag(8, v8); vfrag(0, v0);
      SBAR();
      const f32x16 sa = scores(k8, zero16), sb = scores(k0, zero16);
      SBAR();
      float sm[16];
#pragma unroll
      for (int r = 0; r < 16; ++r) { const int kk = (r & 3) + 8 * (r >> 2) + 4 * h; sm[r] = kk <= r32 ? sa[r] : sb[r]; }
      float m = fmaxf(sm[0], sm[1]);
#pragma unroll
      for (int r = 2; r < 16; r += 2) m = fmaxf(fmaxf(m, sm[r]), sm[r + 1]);
      m = xmax(m);
      f32x16 negm;
#pragma unroll
      for (int r = 0; r < 16; ++r) negm[r] = -m;
      SBAR();
      f32x16 sn = scores(kn[1], negm);
      SBAR();
      unsigned p8[8], p0[8];
#pragma unroll
      for (int r = 0; r < 16; r += 2) {
        const int kk = (r & 3) + 8 * (r >> 2) + 4 * h;
        const unsigned pkd = pk2(__builtin_amdgcn_exp2f(sm[r] - m), __builtin_amdgcn_exp2f(sm[r + 1] - m));
        const unsigned msk = kk + 1 <= r32 ? 0xffffffffu : (kk <= r32 ? 0x0000ffffu : 0u);
        p8[r >> 1] = pkd & msk; p0[r >> 1] = pkd & ~msk;
        PIN(p8[r >> 1]); PIN(p0[r >> 1]);
      }
      SBAR();
      pv(v8, p8);
      pv(v0, p0);
      STAMP(13);
#pragma unroll
      for (int blk = 1; blk < 8; ++blk) {
        f32x16 sc = sn;
        v4u vf[2];
        SBAR();
        if (blk < 7) sn = scores(kn[(blk + 1) & 1], negm);
        vfrag(blk, vf);
        if (blk < 6) kfrag(blk + 2, kn[blk & 1]);
        if (blk < 5) pre(blk);
        SBAR();
        float b0 = fmaxf(fmaxf(sc[0], sc[1]), sc[2]), b1 = fmaxf(fmaxf(sc[3], sc[4]), sc[5]);
#pragma unroll
        for (int r = 6; r < 16; r += 4) { b0 = fmaxf(fmaxf(b0, sc[r]), sc[r + 1]); if (r + 3 < 16) b1 = fmaxf(fmaxf(b1, sc[r + 2]), sc[r + 3]); }
        b0 = fmaxf(b0, b1);
        if (__builtin_expect(__any(b0 > THR), 0)) {
          const float d = fmaxf(xmax(b0), 0.f), f = __builtin_amdgcn_exp2f(-d);
#pragma unroll
          for (int r = 0; r < 16; ++r) { sc[r] -= d; sn[r] -= d; negm[r] -= d; o[r] *= f; }
        }
        SBAR();
        unsigned pk[8];
#pragma unroll
        for (int r = 0; r < 16; r += 2) { pk[r >> 1] = pk2(__builtin_amdgcn_exp2f(sc[r]), __builtin_amdgcn_exp2f(sc[r + 1])); PIN(pk[r >> 1]); }
        SBAR();
        pv(vf, pk);
      }
    }
    STAMP(14);
    {
      const unsigned du = asu(o[8]);
      const auto dsw = __builtin_amdgcn_permlane32_swap(du, du, false, false);
      const float inv = __builtin_amdgcn_rcpf(asf((unsigned)dsw[1]));
      const int mt = 2 * qh + (r32 >> 4), tk = r32 & 15;
      const int obase = WS_O + ((pn * 8 + j) * 4 + mt) * 2560;
#pragma unroll
      for (int c = 0; c < 3; ++c) {
        const int Qd = 5 * hl + (c < 2 ? 2 * c + h : 4), nt = Qd >> 2, gg = Qd & 3;
        const int off = nt < 4 ? (nt >> 1) * 1024 + (tk + 16 * gg) * 16 + (nt & 1) * 8 : 2048 + (tk + 16 * gg) * 8;
        const v2u v = {pk2(o[4 * c] * inv, o[4 * c + 1] * inv), pk2(o[4 * c + 2] * inv, o[4 * c + 3] * inv)};
        if (c < 2 || h == 0) gst64(rs, obase + off, v);
      }
    }
    STAMP(19);
    publish(1, 8u, WS_F2 + (pn * 8 + j) * FL_STRIDE);
    if (w != 7) touch_w2(0);
  }
  const int pn = pg, j = jg;
  STAMP(5);

  auto red_write = [&](int lane, int hp, const f32x4 (&acc)[2][5]) {
#pragma unroll
    for (int ml = 0; ml < 2; ++ml) {
      LAS char* d = (LAS char*)smem + ((2 * hp + ml) * 8 + w) * 2560;
#pragma unroll
      for (int q = 0; q < 2; ++q) {
        const f32x4 a = acc[ml][2 * q], c = acc[ml][2 * q + 1];
        *(LAS v4u*)(d + q * 1024 + lane * 16) = v4u{pk2(a[0], a[1]), pk2(a[2], a[3]), pk2(c[0], c[1]), pk2(c[2], c[3])};
      }
      const f32x4 a = acc[ml][4];
      *(LAS v2u*)(d + 2048 + lane * 8) = v2u{pk2(a[0], a[1]), pk2(a[2], a[3])};
    }
  };
  auto red_sum = [&](int lane, f32x4& s0, f32x4& s1, f32x4& s2) {
    const int n = lane & 15, gg = lane >> 4;
    const unsigned one = (n >> 2) == gg ? ((n & 1) ? 0x3F800000u : 0x00003F80u) : 0u;
    const unsigned e01 = (n & 2) ? 0u : one, e23 = (n & 2) ? one : 0u;
    const v4u selA = {e01, e23, 0u, 0u}, selB = {0u, 0u, e01, e23};
    v4u r[8]; v2u r2[8];
#pragma unroll
    for (int ww = 0; ww < 8; ++ww) {
      r[ww] = lds128(L + ((w >> 1) * 8 + ww) * 2560 + (w & 1) * 1024 + lane * 16);
      r2[ww] = lds64(L + ((w & 3) * 8 + ww) * 2560 + 2048 + lane * 8);
    }
    SBAR();
#pragma unroll
    for (int ww = 0; ww < 8; ++ww) { s0 = mfma16(selA, r[ww], s0); s1 = mfma16(selB, r[ww], s1); }
    const v4u selC = {e01, e23, e01, e23};
#pragma unroll
    for (int ww = 0; ww < 8; ww += 2) s2 = mfma16(selC, v4u{r2[ww].x, r2[ww].y, r2[ww + 1].x, r2[ww + 1].y}, s2);
    SBAR();
  };
  {
#pragma unroll
    for (int ks = 0; ks < W1E; ++ks) { w1f[ks][0] = gld128(rs, w1base + ks * 1024); w1f[ks][1] = gld128(rs, w1base + (20 + ks) * 1024); }
    SBAR();
    if (w == 7) { poll_flags(p.ws, WS_F2 + (pn * 8 + (lane & 7)) * FL_STRIDE, lane < 8, tag); touch_w2(0); }
    LBAR();
    STAMP(6);
    const int obase = WS_O + (pn * 8 + w) * 10240;
    v4u ba[2][2][3];
    auto oload = [&](int hp) {
#pragma unroll
      for (int ml = 0; ml < 2; ++ml) {
        ba[hp][ml][0] = gld128(rs, obase + (2 * hp + ml) * 2560 + lane * 16);
        ba[hp][ml][1] = gld128(rs, obase + (2 * hp + ml) * 2560 + 1024 + lane * 16);
        const v2u c = gld64(rs, obase + (2 * hp + ml) * 2560 + 2048 + lane * 8);
        ba[hp][ml][2] = v4u{c.x, c.y, 0u, 0u};
      }
      SBAR();
    };
    oload(0); oload(1);
#pragma unroll
    for (int hp = 0; hp < 2; ++hp) {
      f32x4 acc[2][5];
#pragma unroll
      for (int ml = 0; ml < 2; ++ml)
#pragma unroll
        for (int nt = 0; nt < 5; ++nt) acc[ml][nt] = mfma16(wo[nt][0], ba[hp][ml][0], f32x4{0, 0, 0, 0});
#pragma unroll
      for (int k = 1; k < 3; ++k)
#pragma unroll
        for (int ml = 0; ml < 2; ++ml)
#pragma unroll
          for (int nt = 0; nt < 5; ++nt) acc[ml][nt] = mfma16(wo[nt][k], ba[hp][ml][k], acc[ml][nt]);
      if (hp == 0) touch_w2(1);
      red_write(lane, hp, acc);
      SBAR();
    }
    STAMP(27);
    LBAR();
    STAMP(20);
    {
      f32x4 s0 = {bflo(xr[0].x), bfhi(xr[0].x), bflo(xr[0].y), bfhi(xr[0].y)};
      f32x4 s1 = {bflo(xr[1].x), bfhi(xr[1].x), bflo(xr[1].y), bfhi(xr[1].y)};
      f32x4 s2 = {bflo(xr[2].x), bfhi(xr[2].x), bflo(xr[2].y), bfhi(xr[2].y)};
      red_sum(lane, s0, s1, s2);
      const v4u hres = v4u{pk2(s0[0], s0[1]), pk2(s0[2], s0[3]), pk2(s1[0], s1[1]), pk2(s1[2], s1[3])};
      const v4u hres2 = v4u{pk2(s2[0], s2[1]), pk2(s2[2], s2[3]), 0u, 0u};
      const int base = WS_H + (pn * 8 + j) * HB;
      gst128(rs, base + (w >> 1) * 2560 + (w & 1) * 1024 + lane * 16, hres);
      gst64(rs, w < 4 ? base + w * 2560 + 2048 + lane * 8 : 0x7F000000, v2u{hres2.x, hres2.y});
      const f32x4 z4 = {0, 0, 0, 0};
      const f32x4 su = mfma16(ONES, hres, z4), sq = mfma16(hres, hres, z4), tu = mfma16(ONES, hres2, z4), tq = mfma16(hres2, hres2, z4);
      const int r = tok & 3;
      const float q1 = r == 0 ? sq[0] : r == 1 ? sq[1] : r == 2 ? sq[2] : sq[3], q2 = r == 0 ? tq[0] : r == 1 ? tq[1] : r == 2 ? tq[2] : tq[3];
      const bool diag = g == (tok >> 2);
      gst64(rs, diag ? base + 10240 + (((w >> 1) * 3 + (w & 1)) * 16 + tok) * 8 : 0x7F000000, v2u{asu(su[0]), asu(q1)});
      gst64(rs, diag && w < 4 ? base + 10240 + ((w * 3 + 2) * 16 + tok) * 8 : 0x7F000000, v2u{asu(tu[0]), asu(q2)});
    }
    STAMP(21);
    publish(2, 8u, WS_F3 + (pn * 8 + j) * FL_STRIDE);
    STAMP(22);
    if (w != 7) touch_w2(2);
#pragma unroll
    for (int ks = W1E; ks < W1M; ++ks) { w1f[ks][0] = gld128(rs, w1base + ks * 1024); w1f[ks][1] = gld128(rs, w1base + (20 + ks) * 1024); }
    SBAR();
  }
  STAMP(7);

  v4u w2f[35];
  auto w2off = [&](int f) { return w2base + f * 1024; };
  {
    constexpr int HL = 11776;
    LAS f32x2* st = (LAS f32x2*)(smem + 8 * HL);
    f32x4 acc[4][2], accC[2];
    if (w == 7) { poll_flags(p.ws, WS_F3 + (pn * 8 + (lane & 7)) * FL_STRIDE, lane < 8, tag); touch_w2(2); }
    LBAR();
    STAMP(8);
    {
      const char* src = p.ws + (size_t)(WS_H + (pn * 8 + w) * HB + lane * 16);
#pragma unroll
      for (int i = 0; i < 11; ++i) glds16_sc1(src + i * 1024, lds0 + w * HL + i * 1024);
      if (lane < 32) glds16_sc1(src + 11 * 1024, lds0 + w * HL + 11 * 1024);
    }
    accC[0] = accC[1] = f32x4{0, 0, 0, 0};
#pragma unroll
    for (int ks = 0; ks < W1M; ++ks) { accC[0] = mfma16(w1f[ks][0], ONES, accC[0]); accC[1] = mfma16(w1f[ks][1], ONES, accC[1]); }
    SBAR();
    WAIT_BAR(0);
    STAMP(9);
    auto bfrag = [&](int ks, v4u (&bf)[4]) {
#pragma unroll
      for (int mt = 0; mt < 4; ++mt) {
        if (ks < 16) bf[mt] = lds128(L + (ks >> 1) * HL + mt * 2560 + (ks & 1) * 1024 + lane * 16);
        else {
          const v2u a = lds64(L + (2 * (ks - 16)) * HL + mt * 2560 + 2048 + lane * 8), c = lds64(L + (2 * (ks - 16) + 1) * HL + mt * 2560 + 2048 + lane * 8);
          bf[mt] = v4u{a.x, a.y, c.x, c.y};
        }
      }
      SBAR();
    };
    if (w == 7) {
      float su = 0.f, sq = 0.f;
#pragma unroll
      for (int q = 0; q < 8; ++q)
#pragma unroll
        for (int part = 0; part < 3; ++part) {
          const f32x2 v = *(const LAS f32x2*)(smem + q * HL + 10240 + ((g * 3 + part) * 16 + tok) * 8);
          su += v.x; sq += v.y;
        }
      const float mean = su * (1.0f / DM);
      const float var = fmaxf(sq * (1.0f / DM) - mean * mean, 0.f);
      st[lane] = f32x2{mean, 1.0f / sqrtf(var + LN_EPS)};
    }
#pragma unroll
    for (int i = 0; i < 4; ++i) { acc[i][0] = f32x4{0, 0, 0, 0}; acc[i][1] = f32x4{0, 0, 0, 0}; }
    v4u bf[2][4];
    bfrag(0, bf[0]);
#pragma unroll
    for (int ks = 0; ks < 20; ++ks) {
      if (ks + 1 < 20) bfrag(ks + 1, bf[(ks + 1) & 1]);
#pragma unroll
      for (int mt = 0; mt < 4; ++mt) {
        acc[mt][0] = mfma16(w1f[ks][0], bf[ks & 1][mt], acc[mt][0]);
        acc[mt][1] = mfma16(w1f[ks][1], bf[ks & 1][mt], acc[mt][1]);
      }
      if (ks >= W1M) { accC[0] = mfma16(w1f[ks][0], ONES, accC[0]); accC[1] = mfma16(w1f[ks][1], ONES, accC[1]); }
      SBAR();
      if (ks < 20 - W1M) { w1f[W1M + ks][0] = gld128(rs, w1base + (W1M + ks) * 1024); w1f[W1M + ks][1] = gld128(rs, w1base + (20 + W1M + ks) * 1024); SBAR(); }
      if (ks >= 4 && ks < 4 + W2L) glds16_sc1(p.ws + (size_t)(w2off(ks - 4) + lane * 16), lds0 + W2LOFF + (w * W2L + ks - 4) * 1024);
      if (ks >= 4 && ks < 19) {
#pragma unroll
        for (int e = 0; e < (ks >= 11 ? 2 : 1); ++e) { const int f = ks < 11 ? W2L + (ks - 4) : W2L + 7 + 2 * (ks - 11) + e; if (f < 30) w2f[f] = gld128(rs, w2off(f) + lane * 16); }
        SBAR();
      }
    }
    STAMP(28);
    LBAR();
    STAMP(23);
    {
#pragma unroll
      for (int mt = 0; mt < 4; ++mt) {
        const f32x2 ms = st[mt * 16 + tok];
        float v[8];
#pragma unroll
        for (int e = 0; e < 2; ++e)
#pragma unroll
          for (int r = 0; r < 4; ++r) v[4 * e + r] = acc[mt][e][r] - ms.x * accC[e][r];
        gst128(rs, gemm ? WS_H1 + (((pn * 8 + j) * 7 + w) * 4 + mt) * 1024 + lane * 16 : 0x7F000000, v4u{relu2(pk2(v[0], v[1])), relu2(pk2(v[2], v[3])), relu2(pk2(v[4], v[5])), relu2(pk2(v[6], v[7]))});
      }
    }
    STAMP(24);
    publish(3, 8u, WS_F4 + (pn * 8 + j) * FL_STRIDE);
  }
  STAMP(10);

  {
    const bool full = w < 6;
    int lz = lane; asm volatile("" : "+v"(lz));
    v4u hres; v2u hres2;
#pragma unroll
    for (int f = 30; f < 35; ++f) w2f[f] = gld128(rs, (full ? w2off(f) : 0x7F000000) + lane * 16);
    SBAR();
    if (w == 7) poll_flags(p.ws, WS_F4 + (pn * 8 + (lane & 7)) * FL_STRIDE, lane < 8, tag);
    LBAR();
    STAMP(11);
    const int bbase = WS_H1 + (pn * 8 + w) * 28672 + lane * 16;
    v4u bf[2][7][2];
#pragma unroll
    for (int q = 0; q < 7; ++q) {
      const int ks = (q + 2) % 7;
#pragma unroll
      for (int ml = 0; ml < 2; ++ml) bf[0][ks][ml] = gld128(rs, bbase + ((ks < 6 || full ? ks : 0) * 4 + ml) * 1024);
    }
    SBAR();
#pragma unroll
    for (int hp = 0; hp < 2; ++hp) {
      f32x4 acc[2][5];
#pragma unroll
      for (int ml = 0; ml < 2; ++ml)
#pragma unroll
        for (int nt = 0; nt < 5; ++nt) acc[ml][nt] = f32x4{0, 0, 0, 0};
#pragma unroll
      for (int q = 0; q < 7; ++q) {
        const int ks = (q + 2) % 7;
        if (q == 4 || q == 5) {
#pragma unroll
          for (int f = 5 * (q - 4); f < 5 * (q - 3); ++f) if (f < W2L) w2f[f] = lds128(L + W2LOFF + (w * W2L + f) * 1024 + lane * 16);
          SBAR();
        }
#pragma unroll
        for (int ml = 0; ml < 2; ++ml)
#pragma unroll
          for (int nt = 0; nt < 5; ++nt) acc[ml][nt] = mfma16(w2f[ks * 5 + nt], bf[hp][ks][ml], acc[ml][nt]);
        SBAR();
        if (hp == 0) {
#pragma unroll
          for (int ml = 0; ml < 2; ++ml) bf[1][ks][ml] = gld128(rs, bbase + ((ks < 6 || full ? ks : 0) * 4 + 2 + ml) * 1024);
          SBAR();
        }
      }
      red_write(lz, hp, acc);
      SBAR();
      if (hp == 0) {
        STAMP(15); asm volatile("" ::: "memory");
        const int base = WS_H + (pn * 8 + j) * HB;
        hres = gld128(rs, base + (w >> 1) * 2560 + (w & 1) * 1024 + lz * 16);
        hres2 = gld64(rs, base + (w & 3) * 2560 + 2048 + lz * 8);
        SBAR();
      }
    }
    STAMP(29);
    LBAR();
    STAMP(25);
    {
      f32x4 s0 = {0, 0, 0, 0}, s1 = s0, s2 = s0;
      red_sum(lz, s0, s1, s2);
      const LAS f32x2* st = (const LAS f32x2*)(smem + 8 * 11776);
      const float r1 = st[16 * (w >> 1) + (lz & 15)].y, r2 = st[16 * (w & 3) + (lz & 15)].y;
      char* dst = p.out + (size_t)((64 * pn + 16 * (w >> 1) + (lz & 15)) * 1280 + (80 * j + 32 * (w & 1) + 4 * (lz >> 4)) * 2);
      *(GAS v2u*)dst = v2u{pk2(fmaf(s0[0], r1, bflo(hres.x)), fmaf(s0[1], r1, bfhi(hres.x))), pk2(fmaf(s0[2], r1, bflo(hres.y)), fmaf(s0[3], r1, bfhi(hres.y)))};
      *(GAS v2u*)(dst + 32) = v2u{pk2(fmaf(s1[0], r1, bflo(hres.z)), fmaf(s1[1], r1, bfhi(hres.z))), pk2(fmaf(s1[2], r1, bflo(hres.w)), fmaf(s1[3], r1, bfhi(hres.w)))};
      if (w < 4) *(GAS v2u*)(p.out + (size_t)((64 * pn + 16 * w + (lz & 15)) * 1280 + (80 * j + 64 + 4 * (lz >> 4)) * 2)) =
          v2u{pk2(fmaf(s2[0], r2, bflo(hres2.x)), fmaf(s2[1], r2, bfhi(hres2.x))), pk2(fmaf(s2[2], r2, bflo(hres2.y)), fmaf(s2[3], r2, bfhi(hres2.y)))};
    }
  }
  STAMP(12);
  STAMP_DUMP();
  if (b == 0 && tid == 0) set_flag(p.ws, WS_EPOCH, tag);
  asm volatile("mk_code_end:");
}
}

extern "C" void kernel_launch(void* const* d_in, const int* in_sizes, int n_in,
                              void* d_out, int out_size, void* d_ws, size_t ws_size,
                              hipStream_t stream) {
  (void)in_sizes; (void)n_in; (void)out_size; (void)ws_size;
  Params p;
  p.x = (const char*)d_in[0]; p.wqkv = (const char*)d_in[1]; p.wo = (const char*)d_in[2];
  p.w1 = (const char*)d_in[4]; p.w2 = (const char*)d_in[5];
  p.out = (char*)d_out; p.ws = (char*)d_ws;
  (void)hipFuncSetAttribute((const void*)block_kernel, hipFuncAttributeMaxDynamicSharedMemorySize, LDS_BYTES);
  block_kernel<<<256, 512, LDS_BYTES, stream>>>(p);
}
```

```cpp
#include <hip/hip_runtime.h>
#include <hip/hip_bf16.h>

#define GAS __attribute__((address_space(1)))
#define LAS __attribute__((address_space(3)))
#define DI __device__ __forceinline__
typedef unsigned v4u __attribute__((ext_vector_type(4)));
typedef unsigned v2u __attribute__((ext_vector_type(2)));
typedef float f32x2 __attribute__((ext_vector_type(2)));
typedef float f32x4 __attribute__((ext_vector_type(4)));
typedef float f32x16 __attribute__((ext_vector_type(16)));
typedef short bf16x8 __attribute__((ext_vector_type(8)));
typedef short bf16x4 __attribute__((ext_vector_type(4)));
typedef __bf16 bfx2 __attribute__((ext_vector_type(2)));
typedef unsigned long long u64;
typedef GAS u64 gu64;
#define RLX_AGENT __ATOMIC_RELAXED, __HIP_MEMORY_SCOPE_AGENT

namespace {
constexpr int SEQ = 2048, DM = 640, NQKV = 1920, DFF = 1728;
constexpr float LN_EPS = 1e-5f;
constexpr float QSCALE = 0.22360679774997896f * 1.4426950408889634f;

constexpr int FL_STRIDE = 128;
constexpr int WS_EPOCH = 0, WS_TMO = 256, WS_ARR = 1024;
constexpr int WS_F1 = 4096;
constexpr int WS_F2 = WS_F1 + 256 * FL_STRIDE;
constexpr int WS_F3 = WS_F2 + 256 * FL_STRIDE;
constexpr int WS_F4 = WS_F3 + 256 * FL_STRIDE;
constexpr int WS_F0 = WS_F4 + 256 * FL_STRIDE;
constexpr int WS_STAMP = WS_F0 + 256 * FL_STRIDE;
constexpr int WS_SP = 512 * 1024;
constexpr int WS_CP = WS_SP + 16 * 10 * 1024;
constexpr int WS_QKV = 1 << 20;
constexpr int WS_O = WS_QKV + 480 * 2048 * 8;
constexpr int HB = 12288;
constexpr int WS_H = WS_O + 32 * 81920;
constexpr int WS_H1 = WS_H + 256 * HB;
constexpr int WS_WOP = WS_H1 + 32 * 8 * 28672;
constexpr int WS_W1P = WS_WOP + 64 * 12800;
constexpr int WS_W2P = WS_W1P + 108 * 20 * 1024;
constexpr int WS_END = WS_W2P + 64 * 35 * 1024;

constexpr int LDS_BYTES = 160 * 1024;
constexpr unsigned SPIN_CAP = 1u << 18;
constexpr int AUX_SC1 = 16;
#define ATT_THR 8.0f
#define P1_SLEEP 8

struct Params {
  const char* x; const char* wqkv; const char* wo; const char* w1; const char* w2;
  char* out; char* ws;
};

DI f32x4 mfma16(v4u a, v4u b, f32x4 c) {
  return __builtin_amdgcn_mfma_f32_16x16x32_bf16(__builtin_bit_cast(bf16x8, a), __builtin_bit_cast(bf16x8, b), c, 0, 0, 0);
}
DI f32x4 mfma16h(v2u a, v2u b, f32x4 c) {
  return __builtin_amdgcn_mfma_f32_16x16x16bf16_1k(__builtin_bit_cast(bf16x4, a), __builtin_bit_cast(bf16x4, b), c, 0, 0, 0);
}
DI f32x16 mfma32(v4u a, v4u b, f32x16 c) {
  return __builtin_amdgcn_mfma_f32_32x32x16_bf16(__builtin_bit_cast(bf16x8, a), __builtin_bit_cast(bf16x8, b), c, 0, 0, 0);
}
DI unsigned pk2(float lo, float hi) {
  f32x2 v = {lo, hi};
  return __builtin_bit_cast(unsigned, __builtin_convertvector(v, bfx2));
}
DI float asf(unsigned u) { return __builtin_bit_cast(float, u); }
DI unsigned asu(float f) { return __builtin_bit_cast(unsigned, f); }
typedef short s16x2 __attribute__((ext_vector_type(2)));
DI unsigned relu2(unsigned u) {
  const s16x2 v = __builtin_bit_cast(s16x2, u);
  return __builtin_bit_cast(unsigned, __builtin_elementwise_max(v, s16x2{0, 0}));
}
DI u64 uni64(unsigned lo, unsigned hi) {
  const unsigned l = (unsigned)__builtin_amdgcn_readfirstlane(lo), h = (unsigned)__builtin_amdgcn_readfirstlane(hi);
  return ((u64)h << 32) | (u64)l;
}
DI float bflo(unsigned u) { return __builtin_bit_cast(float, u << 16); }
DI float bfhi(unsigned u) { return __builtin_bit_cast(float, u & 0xffff0000u); }

DI float xsum(float v) {
  const unsigned u = __builtin_bit_cast(unsigned, v);
  const auto a = __builtin_amdgcn_permlane32_swap(u, u, false, false);
  const float s = __builtin_bit_cast(float, (unsigned)a[0]) + __builtin_bit_cast(float, (unsigned)a[1]);
  const unsigned w = __builtin_bit_cast(unsigned, s);
  const auto c = __builtin_amdgcn_permlane16_swap(w, w, false, false);
  return __builtin_bit_cast(float, (unsigned)c[0]) + __builtin_bit_cast(float, (unsigned)c[1]);
}

DI void glds16(const char* g, unsigned lds_base) {
  unsigned sv;
  asm volatile("s_mov_b32 %0, m0\n\ts_mov_b32 m0, %2\n\ts_nop 0\n\tglobal_load_lds_dwordx4 %1, off\n\ts_mov_b32 m0, %0"
               : "=&s"(sv) : "v"(g), "s"(lds_base) : "memory");
}
DI void glds16_sc1(const char* g, unsigned lds_base) {
  unsigned sv;
  asm volatile("s_mov_b32 %0, m0\n\ts_mov_b32 m0, %2\n\ts_nop 0\n\tglobal_load_lds_dwordx4 %1, off sc1\n\ts_mov_b32 m0, %0"
               : "=&s"(sv) : "v"(g), "s"(lds_base) : "memory");
}
DI void touch4(const char* g, unsigned lds_base) {
  unsigned sv;
  asm volatile("s_mov_b32 %0, m0\n\ts_mov_b32 m0, %2\n\ts_nop 0\n\tglobal_load_lds_dword %1, off sc1\n\ts_mov_b32 m0, %0"
               : "=&s"(sv) : "v"(g), "s"(lds_base) : "memory");
}
#define WAITV(N) asm volatile("s_waitcnt vmcnt(" #N ")" ::: "memory")
#define WAIT_BAR(N) asm volatile("s_waitcnt vmcnt(" #N ") lgkmcnt(0)\n\ts_barrier" ::: "memory")
#define LBAR() asm volatile("s_waitcnt lgkmcnt(0)\n\ts_barrier" ::: "memory")
#define SBAR() __builtin_amdgcn_sched_barrier(0)
#define PIN(x) asm volatile("" : "+v"(x))

typedef LAS const char* lptr;
DI v4u lds128(lptr p) { return *(const LAS v4u*)p; }
DI v2u lds64(lptr p) { return *(const volatile LAS v2u*)p; }
DI v2u ldstr(lptr p) { return __builtin_bit_cast(v2u, __builtin_amdgcn_ds_read_tr16_b64_v4i16((LAS bf16x4*)p)); }

DI v4u gld128(__amdgpu_buffer_rsrc_t rs, int off) { return __builtin_amdgcn_raw_buffer_load_b128(rs, off, 0, AUX_SC1); }
DI v2u gld64(__amdgpu_buffer_rsrc_t rs, int off) { return __builtin_amdgcn_raw_buffer_load_b64(rs, off, 0, AUX_SC1); }
DI void gst128(__amdgpu_buffer_rsrc_t rs, int off, v4u v) { __builtin_amdgcn_raw_buffer_store_b128(v, rs, off, 0, AUX_SC1); }
DI void gst64(__amdgpu_buffer_rsrc_t rs, int off, v2u v) { __builtin_amdgcn_raw_buffer_store_b64(v, rs, off, 0, AUX_SC1); }

DI void poll_flags(char* ws, int off, bool want, u64 tag) {
  gu64* f = (gu64*)(ws + off);
  for (unsigned spins = 0;;) {
    u64 v = tag;
    if (want) v = __hip_atomic_load(f, RLX_AGENT);
    if (__all(v == tag)) break;
    if (++spins > SPIN_CAP) { __hip_atomic_store((gu64*)(ws + WS_TMO), tag, RLX_AGENT); break; }
    if ((spins & 1023u) == 0u && __builtin_amdgcn_readfirstlane((unsigned)(__hip_atomic_load((gu64*)(ws + WS_TMO), RLX_AGENT) == tag))) break;
  }
  __builtin_amdgcn_fence(__ATOMIC_ACQUIRE, "wavefront");
}
DI void poll_arr(char* ws, u64 tag, int lane) {
  gu64* f = (gu64*)(ws + WS_ARR) + lane;
  for (unsigned spins = 0;;) {
    const u64 a = __hip_atomic_load(f, RLX_AGENT), b = __hip_atomic_load(f + 64, RLX_AGENT), c = __hip_atomic_load(f + 128, RLX_AGENT), d = __hip_atomic_load(f + 192, RLX_AGENT);
    if (__all(a == tag && b == tag && c == tag && d == tag)) break;
    if (++spins > SPIN_CAP) { __hip_atomic_store((gu64*)(ws + WS_TMO), tag, RLX_AGENT); break; }
    if ((spins & 1023u) == 0u && __builtin_amdgcn_readfirstlane((unsigned)(__hip_atomic_load((gu64*)(ws + WS_TMO), RLX_AGENT) == tag))) break;
  }
  __builtin_amdgcn_fence(__ATOMIC_ACQUIRE, "wavefront");
}
DI void ring_wait(volatile LAS unsigned* r, unsigned want) {
  for (;;) {
    const v4u f = *(const volatile LAS v4u*)r;
    if ((unsigned)__builtin_amdgcn_readfirstlane(min(min(f.x, f.y), min(f.z, f.w))) >= want) break;
    __builtin_amdgcn_s_sleep(1);
  }
  asm volatile("" ::: "memory");
}
DI void ring_wait2(volatile LAS unsigned* r, unsigned want) {
  for (;;) {
    const v2u f = *(const volatile LAS v2u*)r;
    if ((unsigned)__builtin_amdgcn_readfirstlane(min(f.x, f.y)) >= want) break;
    __builtin_amdgcn_s_sleep(1);
  }
  asm volatile("" ::: "memory");
}
DI void set_flag(char* ws, int off, u64 tag) { __hip_atomic_store((gu64*)(ws + off), tag, RLX_AGENT); }

#define STAMP(i) do {} while (0)
#define STAMP_DUMP() do {} while (0)
#define STAMPC(i) do {} while (0)
#define STAMPL(i) do {} while (0)

DI void pack_src(const Params& p, int u, int lane, const char*& s0, const char*& s1, int& pitch) {
  const char* m; int r0, r1, cg;
  if (u < 240) {
    const int ru = u / 10, w = ru / 3, part = ru - 3 * w;
    cg = u - 10 * ru; m = p.wo; pitch = 1280; r0 = 80 * w + 32 * part; r1 = part == 2 ? r0 : r0 + 16;
  } else if (u < 780) {
    const int v = u - 240, ks = v / 27;
    cg = v - 27 * ks; m = p.w1; pitch = 3456;
    if (ks < 16) { r0 = 80 * (ks >> 1) + 32 * (ks & 1); r1 = r0 + 16; } else { r0 = 160 * (ks - 16) + 64; r1 = r0 + 80; }
  } else {
    const int v = u - 780, ks = v / 10;
    cg = v - 10 * ks; m = p.w2; pitch = 1280; r0 = 32 * ks; r1 = r0 + 16;
  }
  const int lr = lane >> 3, c = (lane & 7) ^ (((lr >> 1) & 3) << 1);
  s0 = m + (size_t)((r0 + lr) * pitch + cg * 128 + c * 16);
  s1 = m + (size_t)((r1 + lr) * pitch + cg * 128 + c * 16);
}
DI void pack_issue(const char* s0, const char* s1, int pitch, unsigned lds) {
  glds16(s0, lds); glds16(s0 + 8 * pitch, lds + 1024); glds16(s1, lds + 2048); glds16(s1 + 8 * pitch, lds + 3072);
}

DI void pack_dest(int u, int (&d)[4], bool& half) {
  int T0, base, sj, sn;
  half = false;
  if (u < 240) {
    const int ru = u / 10, w = ru / 3, part = ru - 3 * w;
    T0 = 4 * (u - 10 * ru); base = WS_WOP + w * 12800 + part * 1024; sj = 8 * 12800; sn = 2560; half = part == 2;
  } else if (u < 780) {
    const int v = u - 240, ks = v / 27;
    T0 = 4 * (v - 27 * ks); base = WS_W1P + ks * 1024; sj = 5 * 20480; sn = 20480;
  } else {
    const int v = u - 780, ru = v / 10;
    const int w = ru < 42 ? ru / 7 : 6 + (ru - 42) / 6, ks = ru < 42 ? ru - 7 * w : ru - 42 - 6 * (w - 6);
    T0 = 4 * (v - 10 * ru); base = WS_W2P + w * 35840 + ks * 5120; sj = 8 * 35840; sn = 1024;
  }
#pragma unroll
  for (int t = 0; t < 4; ++t) { const int T = T0 + t, jj = T / 5; d[t] = base + jj * sj + (T - 5 * jj) * sn; }
}
DI void pack_finish(__amdgpu_buffer_rsrc_t rs, lptr L, const int (&d)[4], bool half, int lane) {
  const int row = 4 * (lane >> 4) + ((lane & 15) >> 2);
  const int a0 = row * 128 + (lane & 3) * 8, sw = (row >> 1) & 3;
  v2u lo[4], hi[4];
#pragma unroll
  for (int t = 0; t < 4; ++t) { lo[t] = ldstr(L + a0 + ((t ^ sw) * 32)); hi[t] = ldstr(L + a0 + ((t ^ sw) * 32) + 2048); }
  if (half) {
#pragma unroll
    for (int t = 0; t < 4; ++t) gst64(rs, d[t] + lane * 8, lo[t]);
  } else {
#pragma unroll
    for (int t = 0; t < 4; ++t) gst128(rs, d[t] + lane * 16, v4u{lo[t].x, lo[t].y, hi[t].x, hi[t].y});
  }
}

__global__ void __launch_bounds__(512, 2) block_kernel(Params p) {
  extern __shared__ __attribute__((aligned(16))) char smem[];
  const int tid = threadIdx.x, lane = tid & 63, w = __builtin_amdgcn_readfirstlane(tid >> 6);
  const int tok = lane & 15, g = lane >> 4;
  const int b = blockIdx.x, xcd = b & 7, t = b >> 3;
  const unsigned lds0 = (unsigned)(size_t)smem;
  lptr L = (lptr)smem;
  const __amdgpu_buffer_rsrc_t rs = __builtin_amdgcn_make_buffer_rsrc(p.ws, 0, WS_END, 0x00020000);
  STAMP(0);
  {
    unsigned len;
    asm volatile("mk_code_begin:\n\ts_mov_b32 %0, mk_code_end-mk_code_begin" : "=s"(len));
    const char* pc = (const char*)__builtin_amdgcn_s_getpc();
    if (w < 4) for (unsigned off = (unsigned)tid * 128u; off < len; off += 256u * 128u) touch4(pc + off, lds0 + LDS_BYTES - 256);
  }
  auto read_tag = [&]() -> u64 {
    const u64 e0 = __hip_atomic_load((gu64*)(p.ws + WS_EPOCH), RLX_AGENT);
    return uni64((unsigned)e0, (unsigned)(e0 >> 32)) + 0x9E3779B97F4A7C15ull;
  };
  const v4u ONES = {0x3F803F80u, 0x3F803F80u, 0x3F803F80u, 0x3F803F80u};
  u64 tag;
  LAS unsigned* cnt = (LAS unsigned*)(smem + LDS_BYTES - 1792);
  if (tid < 8) cnt[tid] = 0u;
  auto publish = [&](int which, unsigned n, int flag_off) {
    WAITV(0);
    unsigned old = 0;
    if (lane == 0) old = __hip_atomic_fetch_add(cnt + which, 1u, __ATOMIC_RELAXED, __HIP_MEMORY_SCOPE_WORKGROUP);
    if (lane == 0 && old == n - 1u) set_flag(p.ws, flag_off, tag);
  };

  auto poll_early = [&]() { poll_flags(p.ws, WS_ARR + ((28 + (lane & 3)) * 8 + 4 + ((lane >> 2) & 3)) * 8, lane < 16, tag); };

  {
    const int P128 = 4 * (xcd & 3) + (t & 3), s = (xcd >> 2) * 8 + (t >> 2);
    if (s < 15) {
      LAS f32x2* st = (LAS f32x2*)(smem + 131072);
      LAS float* cs = (LAS float*)(smem + 132096);
      volatile LAS unsigned* ring = (volatile LAS unsigned*)(smem + 132640);
      const int q4 = (lane & 15) >> 2, pp = lane & 3;
      const int fsw = q4 | ((g & 1) << 2);
      const int xlane = tok * 128 + ((g ^ (tok >> 1)) * 16);
      const int wlane = 16384 + (8 * g + q4) * 256 + pp * 8;
      const int cres = (3 * (s + P128)) % 10;
      const int rot = cres >= 5 ? 5 : 0, jres = cres - rot;
      if (w >= 4) {
        const int a = w - 4, lt = tid - 256;
        const char* xs; const char* wsrc;
        { const int row = lt >> 3, c = (lt & 7) ^ ((row >> 1) & 7); xs = p.x + (size_t)((P128 * 128 + row) * 1280 + c * 16); }
        { const int row = lt >> 4, f = (row & 3) | (((row >> 3) & 1) << 2), c = (lt & 15) ^ (f << 1); wsrc = p.wqkv + (size_t)(row * 3840 + s * 256 + c * 16); }
        auto stage = [&](int i) {
          int kc = i + rot; if (kc >= 10) kc -= 10;
          const unsigned d = lds0 + (i & 3) * 32768 + a * 1024;
          const char* xk = xs + kc * 128; const char* wk = wsrc + (size_t)(kc * (64 * 3840));
#pragma unroll
          for (int q = 0; q < 4; ++q) { glds16(wk + q * (16 * 3840), d + 16384 + q * 4096); glds16(xk + q * (32 * 1280), d + q * 4096); }
        };
        stage(0);
        __builtin_amdgcn_s_sleep(P1_SLEEP);
        stage(1); stage(2);
        WAIT_BAR(16);
        {
          const v2u tv = *(const LAS v2u*)(smem + 132616);
          tag = uni64(tv.x, tv.y);
        }
        stage(3);
#pragma unroll 1
        for (int i = 0; i < 7; ++i) {
          if (i == jres) {
            lptr S = L + (i & 3) * 32768;
            const int r = tok & 3;
#pragma unroll
            for (int e = 0; e < 2; ++e) {
              const v4u x0 = lds128(S + (2 * a + e) * 2048 + xlane), x1 = lds128(S + (2 * a + e) * 2048 + (xlane ^ 64));
              const int wo0 = wlane + (((2 * a + e) ^ fsw) * 32);
              const v2u l0 = ldstr(S + wo0), h0 = ldstr(S + wo0 + 1024), l1 = ldstr(S + wo0 + 8192), h1 = ldstr(S + wo0 + 9216);
              const f32x4 aC = mfma16(v4u{l1.x, l1.y, h1.x, h1.y}, ONES, mfma16(v4u{l0.x, l0.y, h0.x, h0.y}, ONES, f32x4{0, 0, 0, 0}));
              const f32x4 aS = mfma16(ONES, x1, mfma16(ONES, x0, f32x4{0, 0, 0, 0}));
              const f32x4 aG = mfma16(x1, x1, mfma16(x0, x0, f32x4{0, 0, 0, 0}));
              const float G = r == 0 ? aG[0] : r == 1 ? aG[1] : r == 2 ? aG[2] : aG[3];
              gst64(rs, (s < 10 && g == (tok >> 2)) ? WS_SP + ((P128 * 10 + cres) * 128 + (2 * a + e) * 16 + tok) * 8 : 0x7F000000, v2u{asu(aS[0]), asu(G)});
              gst128(rs, (P128 < 10 && tok == 0) ? WS_CP + ((s * 10 + cres) * 128 + (2 * a + e) * 16 + 4 * g) * 4 : 0x7F000000, __builtin_bit_cast(v4u, aC));
            }
          }
          if (i < 6) WAITV(16); else WAITV(0);
          LBAR();
          if (i == jres + 2 && tid == 256) set_flag(p.ws, WS_F0 + (P128 * 15 + s) * FL_STRIDE, tag);
          if (i + 4 < 10) stage(i + 4);
        }
        if (a < 2) {
          poll_flags(p.ws, WS_F0 + (a == 0 ? P128 * 15 + (lane < 10 ? lane : 0) : (lane < 10 ? lane : 0) * 15 + s) * FL_STRIDE, lane < 10, tag);
          if (a == 0) {
            v4u v[10];
#pragma unroll
            for (int c = 0; c < 10; ++c) v[c] = gld128(rs, WS_SP + ((P128 * 10 + c) * 128 + 2 * lane) * 8);
            float su = 0.f, sq = 0.f, tu = 0.f, tq = 0.f;
#pragma unroll
            for (int c = 0; c < 10; ++c) { su += asf(v[c].x); sq += asf(v[c].y); tu += asf(v[c].z); tq += asf(v[c].w); }
            const float m0 = su * (1.0f / DM), m1 = tu * (1.0f / DM);
            st[2 * lane] = f32x2{m0, 1.0f / sqrtf(fmaxf(sq * (1.0f / DM) - m0 * m0, 0.f) + LN_EPS)};
            st[2 * lane + 1] = f32x2{m1, 1.0f / sqrtf(fmaxf(tq * (1.0f / DM) - m1 * m1, 0.f) + LN_EPS)};
          } else {
            v2u v[10];
#pragma unroll
            for (int c = 0; c < 10; ++c) v[c] = gld64(rs, WS_CP + ((s * 10 + c) * 128 + 2 * lane) * 4);
            float su = 0.f, tu = 0.f;
#pragma unroll
            for (int c = 0; c < 10; ++c) { su += asf(v[c].x); tu += asf(v[c].y); }
            cs[2 * lane] = su; cs[2 * lane + 1] = tu;
          }
          if (a == 1) poll_early();
          LBAR();
        } else {
#pragma unroll 1
          for (int i = 0; i < 2; ++i) {
            const int pu = 375 + P128 * 15 + s + 240 * (2 * (a - 2) + i);
            const char *ps0, *ps1; int ppitch;
            pack_src(p, pu < 1320 ? pu : 0, lane, ps0, ps1, ppitch);
            pack_issue(ps0, ps1, ppitch, lds0 + 133120 + (2 * (a - 2) + i) * 4096);
          }
          LBAR();
          WAITV(0);
#pragma unroll 1
          for (int i = 0; i < 2; ++i) {
            const int pu = 375 + P128 * 15 + s + 240 * (2 * (a - 2) + i);
            if (pu < 1320) { int pd[4]; bool phalf; pack_dest(pu, pd, phalf); pack_finish(rs, L + 133120 + (2 * (a - 2) + i) * 4096, pd, phalf, lane); }
          }
          publish(4, 2u, WS_ARR + b * 8);
        }

      } else {
        const int mh = w >> 1, nh = w & 1;
        int xa[2], wq[4];
        xa[0] = mh * 8192 + xlane; xa[1] = xa[0] ^ 64;
#pragma unroll
        for (int n = 0; n < 4; ++n) wq[n] = wlane + (((4 * nh + n) ^ fsw) * 32);
        f32x4 acc[4][4];
#pragma unroll
        for (int m = 0; m < 4; ++m)
#pragma unroll
          for (int n = 0; n < 4; ++n) acc[m][n] = f32x4{0, 0, 0, 0};
        v4u xb[2][4], wa[2][4];
        auto rdfrag = [&](int k2) {
#pragma unroll
          for (int n = 0; n < 4; ++n) {
            const v2u lo = ldstr(L + wq[n] + k2 * 8192), hi = ldstr(L + wq[n] + k2 * 8192 + 1024);
            wa[k2][n] = v4u{lo.x, lo.y, hi.x, hi.y};
            xb[k2][n] = lds128(L + xa[k2] + n * 2048);
          }
        };
        auto mm = [&](int k2) {
#pragma unroll
          for (int m = 0; m < 4; ++m)
#pragma unroll
            for (int n = 0; n < 4; ++n) acc[m][n] = mfma16(wa[k2][n], xb[k2][m], acc[m][n]);
#pragma unroll
          for (int q = 0; q < 12; ++q) { __builtin_amdgcn_sched_group_barrier(0x008, 1, 0); __builtin_amdgcn_sched_group_barrier(0x100, 1, 0); }
          __builtin_amdgcn_sched_group_barrier(0x008, 4, 0);
          SBAR();
        };
        tag = read_tag();
        if (tid == 0) *(LAS u64*)(smem + 132616) = tag;
        if (tid < 12) ((LAS unsigned*)(smem + 132624))[tid] = 0u;
        LBAR();
        STAMP(16);
        rdfrag(0);
        SBAR();
#pragma unroll 1
        for (int i = 0; i < 10; ++i) {
          rdfrag(1);
          mm(0);
#pragma unroll
          for (int n = 0; n < 4; ++n) wq[n] = (wq[n] + 32768) & 0x1FFFF;
          xa[0] = (xa[0] + 32768) & 0x1FFFF; xa[1] = (xa[1] + 32768) & 0x1FFFF;
          if (i < 7) LBAR();
          SBAR();
          rdfrag(0);
          mm(1);
        }
        STAMP(1);
        LBAR();
        STAMP(17);
        const float qs = s < 5 ? QSCALE : 1.0f;
        f32x4 c4[4];
#pragma unroll
        for (int n = 0; n < 4; ++n) c4[n] = *(const LAS f32x4*)(cs + (4 * nh + n) * 16 + 4 * g);
        const bool odd = tok & 1;
#pragma unroll
        for (int m = 0; m < 4; ++m) {
          const int mt = 4 * mh + m;
          const f32x2 ms = st[mt * 16 + tok];
          const float sc = ms.y * qs;
#pragma unroll
          for (int n2 = 0; n2 < 2; ++n2) {
            unsigned pk[2][2];
#pragma unroll
            for (int e = 0; e < 2; ++e) {
              const f32x4 a4 = acc[m][2 * n2 + e], cc = c4[2 * n2 + e];
              pk[e][0] = pk2((a4[0] - ms.x * cc[0]) * sc, (a4[1] - ms.x * cc[1]) * sc);
              pk[e][1] = pk2((a4[2] - ms.x * cc[2]) * sc, (a4[3] - ms.x * cc[3]) * sc);
            }
            const unsigned s0 = odd ? pk[0][0] : pk[1][0], s1 = odd ? pk[0][1] : pk[1][1];
            const unsigned r0 = (unsigned)__builtin_amdgcn_mov_dpp((int)s0, 0xB1, 0xF, 0xF, true);
            const unsigned r1 = (unsigned)__builtin_amdgcn_mov_dpp((int)s1, 0xB1, 0xF, 0xF, true);
            const v4u v = odd ? v4u{r0, r1, pk[1][0], pk[1][1]} : v4u{pk[0][0], pk[0][1], r0, r1};
            const int quad = 32 * s + 4 * (4 * nh + 2 * n2 + (odd ? 1 : 0)) + g;
            gst128(rs, WS_QKV + (quad * 2048 + P128 * 128 + mt * 16 + (tok & 14)) * 8, v);
          }
        }
      }
      STAMP(18);
      if (w < 4) publish(0, 4u, WS_F1 + (P128 * 15 + s) * FL_STRIDE);
    } else {
      const int v = ((xcd - 4) * 4 + (t - 28)) * 8 + w;
#pragma unroll 1
      for (int i = 0; i < 3; ++i) {
        const char *ps0, *ps1; int ppitch;
        pack_src(p, v + 128 * i < 375 ? v + 128 * i : 0, lane, ps0, ps1, ppitch);
        pack_issue(ps0, ps1, ppitch, lds0 + (w * 3 + i) * 4096);
      }
      tag = read_tag();
#pragma unroll 1
      for (int i = 0; i < 3; ++i) {
        const int u = v + 128 * i;
        if (u < 375) { int pd[4]; bool phalf; pack_dest(u, pd, phalf); pack_finish(rs, L + (w * 3 + i) * 4096, pd, phalf, lane); }
      }
      WAIT_BAR(0);
      if (tid == 0) set_flag(p.ws, WS_ARR + b * 8, tag);
      if (w == 6) poll_early();
    }
  }
  STAMP(2);

  const int pg = xcd * 4 + (t & 3), jg = t >> 2;
  v4u wo[5][3];
  v2u xr[3];
  const int ntile = jg < 6 ? 14 : 12, tbase = jg < 7 ? 14 * jg : 96;
  const bool gemm = 2 * w < ntile;
  constexpr int W1E = 8;
  constexpr int W1M = 14;
  constexpr int W2L = 7;
  constexpr int W2LOFF = 8 * 11776 + 1024;
  v4u w1f[20][2];
  const int w1base = gemm ? WS_W1P + (tbase + 2 * w) * 20480 + lane * 16 : 0x7F000000;
  const int w2base = WS_W2P + (jg * 8 + w) * 35840;
  {
    constexpr int PL = 2592, QOFF = 40 * PL, ONESOFF = QOFF + 20 * 512;
    const int pn = t, j = xcd;
    const int tok0 = 64 * pn - 256;
    const int wobase = WS_WOP + (jg * 8 + w) * 12800;
    auto wo_loads = [&](int i) {
      if (i < 5) {
        wo[i][0] = gld128(rs, wobase + i * 2560 + lane * 16);
        wo[i][1] = gld128(rs, wobase + i * 2560 + 1024 + lane * 16);
        wo[i][2] = gld128(rs, wobase + i * 2560 + 2048 + lane * 8);
      } else {
        const char* src = p.x + (size_t)((64 * pg + 16 * (w >> 1) + tok) * 1280 + (80 * jg + 32 * (w & 1) + 4 * g) * 2);
        xr[0] = *(const GAS v2u*)src;
        xr[1] = *(const GAS v2u*)(src + 32);
        xr[2] = *(const GAS v2u*)(p.x + (size_t)((64 * pg + 16 * (w & 3) + tok) * 1280 + (80 * jg + 64 + 4 * g) * 2));
      }
      SBAR();
    };
    auto pre = [&](int i) {
      const int q = t * 8 + w;
      if (q < 216 && lane < 16) touch4(p.ws + (size_t)(WS_W1P + (q >> 1) * 20480 + (32 * i + 16 * (q & 1) + lane) * 128), lds0 + LDS_BYTES - 256);
    };
    auto wo_all = [&]() {
#pragma unroll
      for (int i = 0; i < 6; ++i) wo_loads(i);
    };
    LBAR();
    if (w != 7) wo_all();
    if (w == 7) {
      const int R = lane / 6, rem = lane % 6, pi = rem >> 1;
      const int q0 = 160 * R + 20 * j, sl = (rem & 1) ? (q0 + 19) >> 5 : q0 >> 5;
      const int P = (pn >> 1) - pi;
      const bool want = lane < 18 && P >= 0 && (R > 0 || pi == 0);
      poll_flags(p.ws, WS_F1 + ((want ? P : 0) * 15 + (want ? sl : 0)) * FL_STRIDE, want, tag);
    }
    LBAR();
    STAMP(3);
#pragma unroll
    for (int i = 0; i < 5; ++i) {
      const int pl = 5 * w + i;
      const int quad = (pl < 20 ? 160 + pl : 300 + pl) + 20 * j;
      const char* src = p.ws + (size_t)(WS_QKV + (quad * 2048 + tok0) * 8 + lane * 16);
#pragma unroll
      for (int c = 0; c < 3; ++c) {
        const int tk = tok0 + c * 128 + lane * 2;
        if (tk >= 0 && (c < 2 || lane < 32)) glds16_sc1(src + c * 1024, lds0 + pl * PL + c * 1024);
      }
    }
    if (w < 5) {
#pragma unroll
      for (int i = 0; i < 2; ++i) {
        const int pl = 4 * w + 2 * i + (lane >> 5);
        const char* src = p.ws + (size_t)(WS_QKV + ((20 * j + pl) * 2048 + 64 * pn) * 8 + (lane & 31) * 16);
        glds16_sc1(src, lds0 + QOFF + (4 * w + 2 * i) * 512);
      }
    }
    if (tid < 352) *(LAS v2u*)(smem + ONESOFF + tid * 8) = v2u{0x00003F80u, 0u};
    if (pn < 4) {
      const int n16 = -tok0 / 2;
#pragma unroll 1
      for (int i = tid; i < 40 * n16; i += 512) { const int pl = i / n16; *(LAS v4u*)(smem + pl * PL + (i - pl * n16) * 16) = v4u{0u, 0u, 0u, 0u}; }
    }
    if (w == 6) poll_arr(p.ws, tag, lane);
    WAIT_BAR(0);
    STAMP(4);
    if (w == 7) wo_all();
    pre(0);
    const int hl = w >> 1, qh = w & 1, r32 = lane & 31, h = lane >> 5;
    const int bmin = 8 - 2 * pn - qh;
    v4u qf0, qf1;
    {
      const v2u a = lds64(L + QOFF + (5 * hl + 2 * h) * 512 + (32 * qh + r32) * 8), c = lds64(L + QOFF + (5 * hl + 2 * h + 1) * 512 + (32 * qh + r32) * 8);
      qf0 = v4u{a.x, a.y, c.x, c.y};
      const v2u d = lds64(L + QOFF + (5 * hl + 4) * 512 + (32 * qh + r32) * 8);
      qf1 = h ? v4u{0u, 0u, 0u, 0u} : v4u{d.x, d.y, 0x00003F80u, 0u};
    }
    lptr kp0 = L + (5 * hl + 2 * h) * PL + (32 * qh + r32) * 8;
    lptr kp1 = L + (5 * hl + 4) * PL + (32 * qh + r32) * 8;
    auto kfrag = [&](int blk, v4u (&kf)[2]) {
      const v2u a = lds64(kp0 + blk * 256), d = lds64(kp0 + PL + blk * 256), e = lds64(kp1 + blk * 256);
      kf[0] = v4u{a.x, a.y, d.x, d.y};
      kf[1] = v4u{e.x, e.y, blk >= bmin ? 0u : 0x0000F149u  , 0u};
    };
    auto scores = [&](const v4u (&kf)[2], f32x16 c) -> f32x16 { return mfma32(kf[1], qf1, mfma32(kf[0], qf0, c)); };
    f32x16 zero16;
#pragma unroll
    for (int r = 0; r < 16; ++r) zero16[r] = 0.f;
    const int blk16 = (lane >> 4) & 1, q4 = (lane & 15) >> 2, pp = lane & 3;
    const int vplane = (blk16 == 0) ? (20 + 5 * hl + pp) * PL : (pp == 0 ? (24 + 5 * hl) * PL : ONESOFF + (((20 + 5 * hl) * 32 + 160) & 255));
    lptr vp = L + vplane + (32 * qh + 4 * h + q4) * 8;
    f32x16 o = zero16;
    auto vfrag = [&](int blk, v4u (&vf)[2]) {
#pragma unroll
      for (int s2 = 0; s2 < 2; ++s2) {
        const v2u a = ldstr(vp + blk * 256 + s2 * 128), c = ldstr(vp + blk * 256 + s2 * 128 + 64);
        vf[s2] = v4u{a.x, a.y, c.x, c.y};
      }
    };
    auto pv = [&](const v4u (&vf)[2], const unsigned (&pk)[8]) {
      o = mfma32(vf[0], v4u{pk[0], pk[1], pk[2], pk[3]}, o);
      o = mfma32(vf[1], v4u{pk[4], pk[5], pk[6], pk[7]}, o);
    };
    auto xmax = [&](float v) {
      const unsigned u = asu(v);
      const auto sw = __builtin_amdgcn_permlane32_swap(u, u, false, false);
      return fmaxf(asf((unsigned)sw[0]), asf((unsigned)sw[1]));
    };
    {
      constexpr float THR = ATT_THR;
      v4u k8[2], k0[2], kn[2][2], v8[2], v0[2];
      kfrag(8, k8); kfrag(0, k0); kfrag(1, kn[1]); kfrag(2, kn[0]);
      vfrag(8, v8); vfrag(0, v0);
      SBAR();
      const f32x16 sa = scores(k8, zero16), sb = scores(k0, zero16);
      SBAR();
      float sm[16];
#pragma unroll
      for (int r = 0; r < 16; ++r) { const int kk = (r & 3) + 8 * (r >> 2) + 4 * h; sm[r] = kk <= r32 ? sa[r] : sb[r]; }
      float m = fmaxf(sm[0], sm[1]);
#pragma unroll
      for (int r = 2; r < 16; r += 2) m = fmaxf(fmaxf(m, sm[r]), sm[r + 1]);
      m = xmax(m);
      f32x16 negm;
#pragma unroll
      for (int r = 0; r < 16; ++r) negm[r] = -m;
      SBAR();
      f32x16 sn = scores(kn[1], negm);
      SBAR();
      unsigned p8[8], p0[8];
#pragma unroll
      for (int r = 0; r < 16; r += 2) {
        const int kk = (r & 3) + 8 * (r >> 2) + 4 * h;
        const unsigned pkd = pk2(__builtin_amdgcn_exp2f(sm[r] - m), __builtin_amdgcn_exp2f(sm[r + 1] - m));
        const unsigned msk = kk + 1 <= r32 ? 0xffffffffu : (kk <= r32 ? 0x0000ffffu : 0u);
        p8[r >> 1] = pkd & msk; p0[r >> 1] = pkd & ~msk;
        PIN(p8[r >> 1]); PIN(p0[r >> 1]);
      }
      SBAR();
      pv(v8, p8);
      pv(v0, p0);
      STAMP(13);
#pragma unroll
      for (int blk = 1; blk < 8; ++blk) {
        f32x16 sc = sn;
        v4u vf[2];
        SBAR();
        if (blk < 7) sn = scores(kn[(blk + 1) & 1], negm);
        vfrag(blk, vf);
        if (blk < 6) kfrag(blk + 2, kn[blk & 1]);
        if (blk < 5) pre(blk);
        SBAR();
        float b0 = fmaxf(fmaxf(sc[0], sc[1]), sc[2]), b1 = fmaxf(fmaxf(sc[3], sc[4]), sc[5]);
#pragma unroll
        for (int r = 6; r < 16; r += 4) { b0 = fmaxf(fmaxf(b0, sc[r]), sc[r + 1]); if (r + 3 < 16) b1 = fmaxf(fmaxf(b1, sc[r + 2]), sc[r + 3]); }
        b0 = fmaxf(b0, b1);
        if (__builtin_expect(__any(b0 > THR), 0)) {
          const float d = fmaxf(xmax(b0), 0.f), f = __builtin_amdgcn_exp2f(-d);
#pragma unroll
          for (int r = 0; r < 16; ++r) { sc[r] -= d; sn[r] -= d; negm[r] -= d; o[r] *= f; }
        }
        SBAR();
        unsigned pk[8];
#pragma unroll
        for (int r = 0; r < 16; r += 2) { pk[r >> 1] = pk2(__builtin_amdgcn_exp2f(sc[r]), __builtin_amdgcn_exp2f(sc[r + 1])); PIN(pk[r >> 1]); }
        SBAR();
        pv(vf, pk);
      }
    }
    STAMP(14);
    {
      const unsigned du = asu(o[8]);
      const auto dsw = __builtin_amdgcn_permlane32_swap(du, du, false, false);
      const float inv = __builtin_amdgcn_rcpf(asf((unsigned)dsw[1]));
      const int mt = 2 * qh + (r32 >> 4), tk = r32 & 15;
      const int obase = WS_O + ((pn * 8 + j) * 4 + mt) * 2560;
#pragma unroll
      for (int c = 0; c < 3; ++c) {
        const int Qd = 5 * hl + (c < 2 ? 2 * c + h : 4), nt = Qd >> 2, gg = Qd & 3;
        const int off = nt < 4 ? (nt >> 1) * 1024 + (tk + 16 * gg) * 16 + (nt & 1) * 8 : 2048 + (tk + 16 * gg) * 8;
        const v2u v = {pk2(o[4 * c] * inv, o[4 * c + 1] * inv), pk2(o[4 * c + 2] * inv, o[4 * c + 3] * inv)};
        if (c < 2 || h == 0) gst64(rs, obase + off, v);
      }
    }
    STAMP(19);
    publish(1, 8u, WS_F2 + (pn * 8 + j) * FL_STRIDE);
    LBAR();
  }
  const int pn = pg, j = jg;
  STAMP(5);

  auto red_write = [&](int lane, int hp, const f32x4 (&acc)[2][5]) {
#pragma unroll
    for (int ml = 0; ml < 2; ++ml) {
      LAS char* d = (LAS char*)smem + ((2 * hp + ml) * 8 + w) * 2560;
#pragma unroll
      for (int q = 0; q < 2; ++q) {
        const f32x4 a = acc[ml][2 * q], c = acc[ml][2 * q + 1];
        *(LAS v4u*)(d + q * 1024 + lane * 16) = v4u{pk2(a[0], a[1]), pk2(a[2], a[3]), pk2(c[0], c[1]), pk2(c[2], c[3])};
      }
      const f32x4 a = acc[ml][4];
      *(LAS v2u*)(d + 2048 + lane * 8) = v2u{pk2(a[0], a[1]), pk2(a[2], a[3])};
    }
  };
  auto red_sum = [&](int lane, f32x4& s0, f32x4& s1, f32x4& s2) {
    const int n = lane & 15, gg = lane >> 4;
    const unsigned one = (n >> 2) == gg ? ((n & 1) ? 0x3F800000u : 0x00003F80u) : 0u;
    const unsigned e01 = (n & 2) ? 0u : one, e23 = (n & 2) ? one : 0u;
    const v4u selA = {e01, e23, 0u, 0u}, selB = {0u, 0u, e01, e23};
    v4u r[8]; v2u r2[8];
#pragma unroll
    for (int ww = 0; ww < 8; ++ww) {
      r[ww] = lds128(L + ((w >> 1) * 8 + ww) * 2560 + (w & 1) * 1024 + lane * 16);
      r2[ww] = lds64(L + ((w & 3) * 8 + ww) * 2560 + 2048 + lane * 8);
    }
    SBAR();
#pragma unroll
    for (int ww = 0; ww < 8; ++ww) { s0 = mfma16(selA, r[ww], s0); s1 = mfma16(selB, r[ww], s1); }
    const v4u selC = {e01, e23, e01, e23};
#pragma unroll
    for (int ww = 0; ww < 8; ww += 2) s2 = mfma16(selC, v4u{r2[ww].x, r2[ww].y, r2[ww + 1].x, r2[ww + 1].y}, s2);
    SBAR();
  };
  {
#pragma unroll
    for (int ks = 0; ks < W1E; ++ks) { w1f[ks][0] = gld128(rs, w1base + ks * 1024); w1f[ks][1] = gld128(rs, w1base + (20 + ks) * 1024); }
    SBAR();
    poll_flags(p.ws, WS_F2 + (pn * 8 + w) * FL_STRIDE, lane == 0, tag);
    STAMP(6);
    const int obase = WS_O + (pn * 8 + w) * 10240;
    v4u ba[2][2][3];
    auto oload = [&](int hp) {
#pragma unroll
      for (int ml = 0; ml < 2; ++ml) {
        ba[hp][ml][0] = gld128(rs, obase + (2 * hp + ml) * 2560 + lane * 16);
        ba[hp][ml][1] = gld128(rs, obase + (2 * hp + ml) * 2560 + 1024 + lane * 16);
        const v2u c = gld64(rs, obase + (2 * hp + ml) * 2560 + 2048 + lane * 8);
        ba[hp][ml][2] = v4u{c.x, c.y, 0u, 0u};
      }
      SBAR();
    };
    oload(0); oload(1);
#pragma unroll
    for (int hp = 0; hp < 2; ++hp) {
      f32x4 acc[2][5];
#pragma unroll
      for (int ml = 0; ml < 2; ++ml)
#pragma unroll
        for (int nt = 0; nt < 5; ++nt) acc[ml][nt] = mfma16(wo[nt][0], ba[hp][ml][0], f32x4{0, 0, 0, 0});
#pragma unroll
      for (int k = 1; k < 3; ++k)
#pragma unroll
        for (int ml = 0; ml < 2; ++ml)
#pragma unroll
          for (int nt = 0; nt < 5; ++nt) acc[ml][nt] = mfma16(wo[nt][k], ba[hp][ml][k], acc[ml][nt]);
      red_write(lane, hp, acc);
      SBAR();
    }
    STAMP(27);
    LBAR();
    STAMP(20);
    {
      f32x4 s0 = {bflo(xr[0].x), bfhi(xr[0].x), bflo(xr[0].y), bfhi(xr[0].y)};
      f32x4 s1 = {bflo(xr[1].x), bfhi(xr[1].x), bflo(xr[1].y), bfhi(xr[1].y)};
      f32x4 s2 = {bflo(xr[2].x), bfhi(xr[2].x), bflo(xr[2].y), bfhi(xr[2].y)};
      red_sum(lane, s0, s1, s2);
      const v4u hres = v4u{pk2(s0[0], s0[1]), pk2(s0[2], s0[3]), pk2(s1[0], s1[1]), pk2(s1[2], s1[3])};
      const v4u hres2 = v4u{pk2(s2[0], s2[1]), pk2(s2[2], s2[3]), 0u, 0u};
      const int base = WS_H + (pn * 8 + j) * HB;
      gst128(rs, base + (w >> 1) * 2560 + (w & 1) * 1024 + lane * 16, hres);
      gst64(rs, w < 4 ? base + w * 2560 + 2048 + lane * 8 : 0x7F000000, v2u{hres2.x, hres2.y});
      const f32x4 z4 = {0, 0, 0, 0};
      const f32x4 su = mfma16(ONES, hres, z4), sq = mfma16(hres, hres, z4), tu = mfma16(ONES, hres2, z4), tq = mfma16(hres2, hres2, z4);
      const int r = tok & 3;
      const float q1 = r == 0 ? sq[0] : r == 1 ? sq[1] : r == 2 ? sq[2] : sq[3], q2 = r == 0 ? tq[0] : r == 1 ? tq[1] : r == 2 ? tq[2] : tq[3];
      const bool diag = g == (tok >> 2);
      gst64(rs, diag ? base + 10240 + (((w >> 1) * 3 + (w & 1)) * 16 + tok) * 8 : 0x7F000000, v2u{asu(su[0]), asu(q1)});
      gst64(rs, diag && w < 4 ? base + 10240 + ((w * 3 + 2) * 16 + tok) * 8 : 0x7F000000, v2u{asu(tu[0]), asu(q2)});
    }
    STAMP(21);
    publish(2, 8u, WS_F3 + (pn * 8 + j) * FL_STRIDE);
    STAMP(22);
    LBAR();
#pragma unroll
    for (int ks = W1E; ks < W1M; ++ks) { w1f[ks][0] = gld128(rs, w1base + ks * 1024); w1f[ks][1] = gld128(rs, w1base + (20 + ks) * 1024); }
    SBAR();
  }
  STAMP(7);

  v4u w2f[35];
  auto w2off = [&](int f) { return w2base + f * 1024; };
  {
    constexpr int HL = 11776;
    LAS f32x2* st = (LAS f32x2*)(smem + 8 * HL);
    f32x4 acc[4][2], accC[2];
    poll_flags(p.ws, WS_F3 + (pn * 8 + w) * FL_STRIDE, lane == 0, tag);
    STAMP(8);
    {
      const char* src = p.ws + (size_t)(WS_H + (pn * 8 + w) * HB + lane * 16);
#pragma unroll
      for (int i = 0; i < 11; ++i) glds16_sc1(src + i * 1024, lds0 + w * HL + i * 1024);
      if (lane < 32) glds16_sc1(src + 11 * 1024, lds0 + w * HL + 11 * 1024);
    }
    accC[0] = accC[1] = f32x4{0, 0, 0, 0};
#pragma unroll
    for (int ks = 0; ks < W1M; ++ks) { accC[0] = mfma16(w1f[ks][0], ONES, accC[0]); accC[1] = mfma16(w1f[ks][1], ONES, accC[1]); }
    SBAR();
    WAIT_BAR(0);
    STAMP(9);
    auto bfrag = [&](int ks, v4u (&bf)[4]) {
#pragma unroll
      for (int mt = 0; mt < 4; ++mt) {
        if (ks < 16) bf[mt] = lds128(L + (ks >> 1) * HL + mt * 2560 + (ks & 1) * 1024 + lane * 16);
        else {
          const v2u a = lds64(L + (2 * (ks - 16)) * HL + mt * 2560 + 2048 + lane * 8), c = lds64(L + (2 * (ks - 16) + 1) * HL + mt * 2560 + 2048 + lane * 8);
          bf[mt] = v4u{a.x, a.y, c.x, c.y};
        }
      }
      SBAR();
    };
    if (w == 7) {
      float su = 0.f, sq = 0.f;
#pragma unroll
      for (int q = 0; q < 8; ++q)
#pragma unroll
        for (int part = 0; part < 3; ++part) {
          const f32x2 v = *(const LAS f32x2*)(smem + q * HL + 10240 + ((g * 3 + part) * 16 + tok) * 8);
          su += v.x; sq += v.y;
        }
      const float mean = su * (1.0f / DM);
      const float var = fmaxf(sq * (1.0f / DM) - mean * mean, 0.f);
      st[lane] = f32x2{mean, 1.0f / sqrtf(var + LN_EPS)};
    }
#pragma unroll
    for (int i = 0; i < 4; ++i) { acc[i][0] = f32x4{0, 0, 0, 0}; acc[i][1] = f32x4{0, 0, 0, 0}; }
    v4u bf[2][4];
    bfrag(0, bf[0]);
#pragma unroll
    for (int ks = 0; ks < 20; ++ks) {
      if (ks + 1 < 20) bfrag(ks + 1, bf[(ks + 1) & 1]);
#pragma unroll
      for (int mt = 0; mt < 4; ++mt) {
        acc[mt][0] = mfma16(w1f[ks][0], bf[ks & 1][mt], acc[mt][0]);
        acc[mt][1] = mfma16(w1f[ks][1], bf[ks & 1][mt], acc[mt][1]);
      }
      if (ks >= W1M) { accC[0] = mfma16(w1f[ks][0], ONES, accC[0]); accC[1] = mfma16(w1f[ks][1], ONES, accC[1]); }
      SBAR();
      if (ks < 20 - W1M) { w1f[W1M + ks][0] = gld128(rs, w1base + (W1M + ks) * 1024); w1f[W1M + ks][1] = gld128(rs, w1base + (20 + W1M + ks) * 1024); SBAR(); }
      if (ks >= 6 && ks < 6 + W2L) glds16_sc1(p.ws + (size_t)(w2off(ks - 6) + lane * 16), lds0 + W2LOFF + (w * W2L + ks - 6) * 1024);
      if (ks >= 6 && ks < 19) {
#pragma unroll
        for (int e = 0; e < (ks >= 13 ? 2 : 1); ++e) { const int f = ks < 13 ? W2L + (ks - 6) : W2L + 7 + 2 * (ks - 13) + e; w2f[f] = gld128(rs, w2off(f) + lane * 16); }
        SBAR();
      }
    }
    STAMP(28);
    LBAR();
    STAMP(23);
    {
#pragma unroll
      for (int mt = 0; mt < 4; ++mt) {
        const f32x2 ms = st[mt * 16 + tok];
        float v[8];
#pragma unroll
        for (int e = 0; e < 2; ++e)
#pragma unroll
          for (int r = 0; r < 4; ++r) v[4 * e + r] = acc[mt][e][r] - ms.x * accC[e][r];
        gst128(rs, gemm ? WS_H1 + (((pn * 8 + j) * 7 + w) * 4 + mt) * 1024 + lane * 16 : 0x7F000000, v4u{relu2(pk2(v[0], v[1])), relu2(pk2(v[2], v[3])), relu2(pk2(v[4], v[5])), relu2(pk2(v[6], v[7]))});
      }
    }
    STAMP(24);
    publish(3, 8u, WS_F4 + (pn * 8 + j) * FL_STRIDE);
    LBAR();
  }
  STAMP(10);

  {
    const bool full = w < 6;
    int lz = lane; asm volatile("" : "+v"(lz));
    v4u hres; v2u hres2;
#pragma unroll
    for (int f = 26; f < 35; ++f) w2f[f] = gld128(rs, (f < 30 || full ? w2off(f) : 0x7F000000) + lane * 16);
    SBAR();
    poll_flags(p.ws, WS_F4 + (pn * 8 + w) * FL_STRIDE, lane == 0, tag);
    STAMP(11);
    const int bbase = WS_H1 + (pn * 8 + w) * 28672 + lane * 16;
    v4u bf[2][7][2];
#pragma unroll
    for (int q = 0; q < 7; ++q) {
      const int ks = (q + 2) % 7;
#pragma unroll
      for (int ml = 0; ml < 2; ++ml) bf[0][ks][ml] = gld128(rs, bbase + ((ks < 6 || full ? ks : 0) * 4 + ml) * 1024);
    }
    SBAR();
#pragma unroll
    for (int hp = 0; hp < 2; ++hp) {
      f32x4 acc[2][5];
#pragma unroll
      for (int ml = 0; ml < 2; ++ml)
#pragma unroll
        for (int nt = 0; nt < 5; ++nt) acc[ml][nt] = f32x4{0, 0, 0, 0};
#pragma unroll
      for (int q = 0; q < 7; ++q) {
        const int ks = (q + 2) % 7;
        if (q == 4 || q == 5) {
#pragma unroll
          for (int f = 5 * (q - 4); f < 5 * (q - 3); ++f) if (f < W2L) w2f[f] = lds128(L + W2LOFF + (w * W2L + f) * 1024 + lane * 16);
          SBAR();
        }
#pragma unroll
        for (int ml = 0; ml < 2; ++ml)
#pragma unroll
          for (int nt = 0; nt < 5; ++nt) acc[ml][nt] = mfma16(w2f[ks * 5 + nt], bf[hp][ks][ml], acc[ml][nt]);
        SBAR();
        if (hp == 0) {
#pragma unroll
          for (int ml = 0; ml < 2; ++ml) bf[1][ks][ml] = gld128(rs, bbase + ((ks < 6 || full ? ks : 0) * 4 + 2 + ml) * 1024);
          SBAR();
        }
      }
      red_write(lz, hp, acc);
      SBAR();
      if (hp == 0) {
        STAMP(15); asm volatile("" ::: "memory");
        const int base = WS_H + (pn * 8 + j) * HB;
        hres = gld128(rs, base + (w >> 1) * 2560 + (w & 1) * 1024 + lz * 16);
        hres2 = gld64(rs, base + (w & 3) * 2560 + 2048 + lz * 8);
        SBAR();
      }
    }
    STAMP(29);
    LBAR();
    STAMP(25);
    {
      f32x4 s0 = {0, 0, 0, 0}, s1 = s0, s2 = s0;
      red_sum(lz, s0, s1, s2);
      const LAS f32x2* st = (const LAS f32x2*)(smem + 8 * 11776);
      const float r1 = st[16 * (w >> 1) + (lz & 15)].y, r2 = st[16 * (w & 3) + (lz & 15)].y;
      char* dst = p.out + (size_t)((64 * pn + 16 * (w >> 1) + (lz & 15)) * 1280 + (80 * j + 32 * (w & 1) + 4 * (lz >> 4)) * 2);
      *(GAS v2u*)dst = v2u{pk2(fmaf(s0[0], r1, bflo(hres.x)), fmaf(s0[1], r1, bfhi(hres.x))), pk2(fmaf(s0[2], r1, bflo(hres.y)), fmaf(s0[3], r1, bfhi(hres.y)))};
      *(GAS v2u*)(dst + 32) = v2u{pk2(fmaf(s1[0], r1, bflo(hres.z)), fmaf(s1[1], r1, bfhi(hres.z))), pk2(fmaf(s1[2], r1, bflo(hres.w)), fmaf(s1[3], r1, bfhi(hres.w)))};
      if (w < 4) *(GAS v2u*)(p.out + (size_t)((64 * pn + 16 * w + (lz & 15)) * 1280 + (80 * j + 64 + 4 * (lz >> 4)) * 2)) =
          v2u{pk2(fmaf(s2[0], r2, bflo(hres2.x)), fmaf(s2[1], r2, bfhi(hres2.x))), pk2(fmaf(s2[2], r2, bflo(hres2.y)), fmaf(s2[3], r2, bfhi(hres2.y)))};
    }
  }
  STAMP(12);
  STAMP_DUMP();
  if (b == 0 && tid == 0) set_flag(p.ws, WS_EPOCH, tag);
  asm volatile("mk_code_end:");
}
}

extern "C" void kernel_launch(void* const* d_in, const int* in_sizes, int n_in,
                              void* d_out, int out_size, void* d_ws, size_t ws_size,
                              hipStream_t stream) {
  (void)in_sizes; (void)n_in; (void)out_size; (void)ws_size;
  Params p;
  p.x = (const char*)d_in[0]; p.wqkv = (const char*)d_in[1]; p.wo = (const char*)d_in[2];
  p.w1 = (const char*)d_in[4]; p.w2 = (const char*)d_in[5];
  p.out = (char*)d_out; p.ws = (char*)d_ws;
  (void)hipFuncSetAttribute((const void*)block_kernel, hipFuncAttributeMaxDynamicSharedMemorySize, LDS_BYTES);
  block_kernel<<<256, 512, LDS_BYTES, stream>>>(p);
}
```

```cpp
#include <hip/hip_runtime.h>
#include <hip/hip_bf16.h>

#define GAS __attribute__((address_space(1)))
#define LAS __attribute__((address_space(3)))
#define DI __device__ __forceinline__
typedef unsigned v4u __attribute__((ext_vector_type(4)));
typedef unsigned v2u __attribute__((ext_vector_type(2)));
typedef float f32x2 __attribute__((ext_vector_type(2)));
typedef float f32x4 __attribute__((ext_vector_type(4)));
typedef float f32x16 __attribute__((ext_vector_type(16)));
typedef short bf16x8 __attribute__((ext_vector_type(8)));
typedef short bf16x4 __attribute__((ext_vector_type(4)));
typedef __bf16 bfx2 __attribute__((ext_vector_type(2)));
typedef unsigned long long u64;
typedef GAS u64 gu64;
#define RLX_AGENT __ATOMIC_RELAXED, __HIP_MEMORY_SCOPE_AGENT

namespace {
constexpr int SEQ = 2048, DM = 640, NQKV = 1920, DFF = 1728;
constexpr float LN_EPS = 1e-5f;
constexpr float QSCALE = 0.22360679774997896f * 1.4426950408889634f;

constexpr int FL_STRIDE = 128;
constexpr int WS_EPOCH = 0, WS_TMO = 256, WS_ARR = 1024;
constexpr int WS_F1 = 4096;
constexpr int WS_F2 = WS_F1 + 256 * FL_STRIDE;
constexpr int WS_F3 = WS_F2 + 256 * FL_STRIDE;
constexpr int WS_F4 = WS_F3 + 256 * FL_STRIDE;
constexpr int WS_F0 = WS_F4 + 256 * FL_STRIDE;
constexpr int WS_STAMP = WS_F0 + 256 * FL_STRIDE;
constexpr int WS_SP = 512 * 1024;
constexpr int WS_CP = WS_SP + 16 * 10 * 1024;
constexpr int WS_QKV = 1 << 20;
constexpr int WS_O = WS_QKV + 480 * 2048 * 8;
constexpr int HB = 12288;
constexpr int WS_H = WS_O + 32 * 81920;
constexpr int WS_H1 = WS_H + 256 * HB;
constexpr int WS_WOP = WS_H1 + 32 * 8 * 28672;
constexpr int WS_W1P = WS_WOP + 64 * 12800;
constexpr int WS_W2P = WS_W1P + 108 * 20 * 1024;
constexpr int WS_END = WS_W2P + 64 * 35 * 1024;

constexpr int LDS_BYTES = 160 * 1024;
constexpr unsigned SPIN_CAP = 1u << 18;
constexpr int AUX_SC1 = 16;
#define ATT_THR 8.0f

constexpr u64 le_mask(int r) {
  u64 m = 0;
  for (int l = 0; l < 64; ++l) if ((r & 3) + 8 * (r >> 2) + 4 * (l >> 5) <= (l & 31)) m |= 1ull << l;
  return m;
}

struct Params {
  const char* x; const char* wqkv; const char* wo; const char* w1; const char* w2;
  char* out; char* ws;
};

DI f32x4 mfma16(v4u a, v4u b, f32x4 c) {
  return __builtin_amdgcn_mfma_f32_16x16x32_bf16(__builtin_bit_cast(bf16x8, a), __builtin_bit_cast(bf16x8, b), c, 0, 0, 0);
}
DI f32x16 mfma32(v4u a, v4u b, f32x16 c) {
  return __builtin_amdgcn_mfma_f32_32x32x16_bf16(__builtin_bit_cast(bf16x8, a), __builtin_bit_cast(bf16x8, b), c, 0, 0, 0);
}
DI unsigned pk2(float lo, float hi) {
  f32x2 v = {lo, hi};
  return __builtin_bit_cast(unsigned, __builtin_convertvector(v, bfx2));
}
DI float asf(unsigned u) { return __builtin_bit_cast(float, u); }
DI unsigned asu(float f) { return __builtin_bit_cast(unsigned, f); }
typedef short s16x2 __attribute__((ext_vector_type(2)));
DI unsigned relu2(unsigned u) {
  const s16x2 v = __builtin_bit_cast(s16x2, u);
  return __builtin_bit_cast(unsigned, __builtin_elementwise_max(v, s16x2{0, 0}));
}
DI u64 uni64(unsigned lo, unsigned hi) {
  const unsigned l = (unsigned)__builtin_amdgcn_readfirstlane(lo), h = (unsigned)__builtin_amdgcn_readfirstlane(hi);
  return ((u64)h << 32) | (u64)l;
}
DI float bflo(unsigned u) { return __builtin_bit_cast(float, u << 16); }
DI float bfhi(unsigned u) { return __builtin_bit_cast(float, u & 0xffff0000u); }

DI void glds16(const char* g, unsigned lds_base) {
  unsigned sv;
  asm volatile("s_mov_b32 %0, m0\n\ts_mov_b32 m0, %2\n\ts_nop 0\n\tglobal_load_lds_dwordx4 %1, off\n\ts_mov_b32 m0, %0"
               : "=&s"(sv) : "v"(g), "s"(lds_base) : "memory");
}
DI void glds16_sc1(const char* g, unsigned lds_base) {
  unsigned sv;
  asm volatile("s_mov_b32 %0, m0\n\ts_mov_b32 m0, %2\n\ts_nop 0\n\tglobal_load_lds_dwordx4 %1, off sc1\n\ts_mov_b32 m0, %0"
               : "=&s"(sv) : "v"(g), "s"(lds_base) : "memory");
}
DI void touch4(const char* g, unsigned lds_base) {
  unsigned sv;
  asm volatile("s_mov_b32 %0, m0\n\ts_mov_b32 m0, %2\n\ts_nop 0\n\tglobal_load_lds_dword %1, off sc1\n\ts_mov_b32 m0, %0"
               : "=&s"(sv) : "v"(g), "s"(lds_base) : "memory");
}
#define WAITV(N) asm volatile("s_waitcnt vmcnt(" #N ")" ::: "memory")
#define WAIT_BAR(N) asm volatile("s_waitcnt vmcnt(" #N ") lgkmcnt(0)\n\ts_barrier" ::: "memory")
#define LBAR() asm volatile("s_waitcnt lgkmcnt(0)\n\ts_barrier" ::: "memory")
#define SBAR() __builtin_amdgcn_sched_barrier(0)
#define PIN(x) asm volatile("" : "+v"(x))

typedef LAS const char* lptr;
DI v4u lds128(lptr p) { return *(const LAS v4u*)p; }
DI v2u lds64(lptr p) { return *(const volatile LAS v2u*)p; }
DI v2u ldstr(lptr p) { return __builtin_bit_cast(v2u, __builtin_amdgcn_ds_read_tr16_b64_v4i16((LAS bf16x4*)p)); }

DI v4u gld128(__amdgpu_buffer_rsrc_t rs, int off) { return __builtin_amdgcn_raw_buffer_load_b128(rs, off, 0, AUX_SC1); }
DI v2u gld64(__amdgpu_buffer_rsrc_t rs, int off) { return __builtin_amdgcn_raw_buffer_load_b64(rs, off, 0, AUX_SC1); }
DI void gst128(__amdgpu_buffer_rsrc_t rs, int off, v4u v) { __builtin_amdgcn_raw_buffer_store_b128(v, rs, off, 0, AUX_SC1); }
DI void gst64(__amdgpu_buffer_rsrc_t rs, int off, v2u v) { __builtin_amdgcn_raw_buffer_store_b64(v, rs, off, 0, AUX_SC1); }

DI void poll_flags(char* ws, int off, bool want, u64 tag) {
  gu64* f = (gu64*)(ws + off);
  for (unsigned spins = 0;;) {
    u64 v = tag;
    if (want) v = __hip_atomic_load(f, RLX_AGENT);
    if (__all(v == tag)) break;
    if (++spins > SPIN_CAP) { __hip_atomic_store((gu64*)(ws + WS_TMO), tag, RLX_AGENT); break; }
    if ((spins & 1023u) == 0u && __builtin_amdgcn_readfirstlane((unsigned)(__hip_atomic_load((gu64*)(ws + WS_TMO), RLX_AGENT) == tag))) break;
  }
  __builtin_amdgcn_fence(__ATOMIC_ACQUIRE, "wavefront");
}
DI void poll_arr(char* ws, u64 tag, int lane) {
  gu64* f = (gu64*)(ws + WS_ARR) + lane;
  for (unsigned spins = 0;;) {
    const u64 a = __hip_atomic_load(f, RLX_AGENT), b = __hip_atomic_load(f + 64, RLX_AGENT), c = __hip_atomic_load(f + 128, RLX_AGENT), d = __hip_atomic_load(f + 192, RLX_AGENT);
    if (__all(a == tag && b == tag && c == tag && d == tag)) break;
    if (++spins > SPIN_CAP) { __hip_atomic_store((gu64*)(ws + WS_TMO), tag, RLX_AGENT); break; }
    if ((spins & 1023u) == 0u && __builtin_amdgcn_readfirstlane((unsigned)(__hip_atomic_load((gu64*)(ws + WS_TMO), RLX_AGENT) == tag))) break;
  }
  __builtin_amdgcn_fence(__ATOMIC_ACQUIRE, "wavefront");
}
DI void set_flag(char* ws, int off, u64 tag) { __hip_atomic_store((gu64*)(ws + off), tag, RLX_AGENT); }

#define STAMP(i) do {} while (0)
#define STAMP_DUMP() do {} while (0)
#define STAMPC(i) do {} while (0)
#define STAMPL(i) do {} while (0)

DI void pack_src(const Params& p, int u, int lane, const char*& s0, const char*& s1, int& pitch) {
  const char* m; int r0, r1, cg;
  if (u < 240) {
    const int ru = u / 10, w = ru / 3, part = ru - 3 * w;
    cg = u - 10 * ru; m = p.wo; pitch = 1280; r0 = 80 * w + 32 * part; r1 = part == 2 ? r0 : r0 + 16;
  } else if (u < 780) {
    const int v = u - 240, ks = v / 27;
    cg = v - 27 * ks; m = p.w1; pitch = 3456;
    if (ks < 16) { r0 = 80 * (ks >> 1) + 32 * (ks & 1); r1 = r0 + 16; } else { r0 = 160 * (ks - 16) + 64; r1 = r0 + 80; }
  } else {
    const int v = u - 780, ks = v / 10;
    cg = v - 10 * ks; m = p.w2; pitch = 1280; r0 = 32 * ks; r1 = r0 + 16;
  }
  const int lr = lane >> 3, c = (lane & 7) ^ (((lr >> 1) & 3) << 1);
  s0 = m + (size_t)((r0 + lr) * pitch + cg * 128 + c * 16);
  s1 = m + (size_t)((r1 + lr) * pitch + cg * 128 + c * 16);
}
DI void pack_issue(const char* s0, const char* s1, int pitch, unsigned lds) {
  glds16(s0, lds); glds16(s0 + 8 * pitch, lds + 1024); glds16(s1, lds + 2048); glds16(s1 + 8 * pitch, lds + 3072);
}

DI void pack_dest(int u, int (&d)[4], bool& half) {
  int T0, base, sj, sn;
  half = false;
  if (u < 240) {
    const int ru = u / 10, w = ru / 3, part = ru - 3 * w;
    T0 = 4 * (u - 10 * ru); base = WS_WOP + w * 12800 + part * 1024; sj = 8 * 12800; sn = 2560; half = part == 2;
  } else if (u < 780) {
    const int v = u - 240, ks = v / 27;
    T0 = 4 * (v - 27 * ks); base = WS_W1P + ks * 1024; sj = 5 * 20480; sn = 20480;
  } else {
    const int v = u - 780, ru = v / 10;
    const int w = ru < 42 ? ru / 7 : 6 + (ru - 42) / 6, ks = ru < 42 ? ru - 7 * w : ru - 42 - 6 * (w - 6);
    T0 = 4 * (v - 10 * ru); base = WS_W2P + w * 35840 + ks * 5120; sj = 8 * 35840; sn = 1024;
  }
#pragma unroll
  for (int t = 0; t < 4; ++t) { const int T = T0 + t, jj = T / 5; d[t] = base + jj * sj + (T - 5 * jj) * sn; }
}
DI void pack_finish(__amdgpu_buffer_rsrc_t rs, lptr L, const int (&d)[4], bool half, int lane) {
  const int row = 4 * (lane >> 4) + ((lane & 15) >> 2);
  const int a0 = row * 128 + (lane & 3) * 8, sw = (row >> 1) & 3;
  v2u lo[4], hi[4];
#pragma unroll
  for (int t = 0; t < 4; ++t) { lo[t] = ldstr(L + a0 + ((t ^ sw) * 32)); hi[t] = ldstr(L + a0 + ((t ^ sw) * 32) + 2048); }
  if (half) {
#pragma unroll
    for (int t = 0; t < 4; ++t) gst64(rs, d[t] + lane * 8, lo[t]);
  } else {
#pragma unroll
    for (int t = 0; t < 4; ++t) gst128(rs, d[t] + lane * 16, v4u{lo[t].x, lo[t].y, hi[t].x, hi[t].y});
  }
}

__global__ void __launch_bounds__(512, 2) block_kernel(Params p) {
  extern __shared__ __attribute__((aligned(16))) char smem[];
  const int tid = threadIdx.x, lane = tid & 63, w = __builtin_amdgcn_readfirstlane(tid >> 6);
  const int tok = lane & 15, g = lane >> 4;
  const int b = blockIdx.x, xcd = b & 7, t = b >> 3;
  const unsigned lds0 = (unsigned)(size_t)smem;
  lptr L = (lptr)smem;
  const __amdgpu_buffer_rsrc_t rs = __builtin_amdgcn_make_buffer_rsrc(p.ws, 0, WS_END, 0x00020000);
  STAMP(0);
  {
    unsigned len;
    asm volatile("mk_code_begin:\n\ts_mov_b32 %0, mk_code_end-mk_code_begin" : "=s"(len));
    const char* pc = (const char*)__builtin_amdgcn_s_getpc();
    if (w < 4) for (unsigned off = (unsigned)tid * 128u; off < len; off += 256u * 128u) touch4(pc + off, lds0 + LDS_BYTES - 256);
  }
  auto read_tag = [&]() -> u64 {
    const u64 e0 = __hip_atomic_load((gu64*)(p.ws + WS_EPOCH), RLX_AGENT);
    return uni64((unsigned)e0, (unsigned)(e0 >> 32)) + 0x9E3779B97F4A7C15ull;
  };
  const v4u ONES = {0x3F803F80u, 0x3F803F80u, 0x3F803F80u, 0x3F803F80u};
  u64 tag;
  LAS unsigned* cnt = (LAS unsigned*)(smem + LDS_BYTES - 1792);
  if (tid < 8) cnt[tid] = 0u;
  auto publish = [&](int which, unsigned n, int flag_off) {
    WAITV(0);
    unsigned old = 0;
    if (lane == 0) old = __hip_atomic_fetch_add(cnt + which, 1u, __ATOMIC_RELAXED, __HIP_MEMORY_SCOPE_WORKGROUP);
    if (lane == 0 && old == n - 1u) set_flag(p.ws, flag_off, tag);
  };

  auto poll_early = [&]() { poll_flags(p.ws, WS_ARR + ((28 + (lane & 3)) * 8 + 4 + ((lane >> 2) & 3)) * 8, lane < 16, tag); };

  constexpr int PL = 2592, QOFF = 40 * PL, ONESOFF = QOFF + 20 * 512;
  const int hl = w >> 1, qh = w & 1, r32 = lane & 31, h = lane >> 5;
  const int bmin = 8 - 2 * t - qh;
  int qa, qb, ka, kb, va, kvsrc, oadr[3];
  unsigned msk[8];
  auto att_consts = [&]() {
    qa = QOFF + (5 * hl + 2 * h) * 512 + (32 * qh + r32) * 8; qb = QOFF + (5 * hl + 4) * 512 + (32 * qh + r32) * 8;
    ka = (5 * hl + 2 * h) * PL + (32 * qh + r32) * 8; kb = (5 * hl + 4) * PL + (32 * qh + r32) * 8;
    const int blk16 = (lane >> 4) & 1, q4 = (lane & 15) >> 2, pp = lane & 3;
    va = ((blk16 == 0) ? (20 + 5 * hl + pp) * PL : (pp == 0 ? (24 + 5 * hl) * PL : ONESOFF + (((20 + 5 * hl) * 32 + 160) & 255))) + (32 * qh + 4 * h + q4) * 8;
#pragma unroll
    for (int c = 0; c < 3; ++c) {
      const int Qd = 5 * hl + (c < 2 ? 2 * c + h : 4), nt = Qd >> 2, gg = Qd & 3, tk = r32 & 15;
      const int off = nt < 4 ? (nt >> 1) * 1024 + (tk + 16 * gg) * 16 + (nt & 1) * 8 : 2048 + (tk + 16 * gg) * 8;
      oadr[c] = (c < 2 || h == 0) ? WS_O + ((t * 8 + xcd) * 4 + 2 * qh + (r32 >> 4)) * 2560 + off : 0x7F000000;
      PIN(oadr[c]);
    }
#pragma unroll
    for (int r = 0; r < 16; r += 2) {
      const int kk = (r & 3) + 8 * (r >> 2) + 4 * h;
      msk[r >> 1] = kk + 1 <= r32 ? 0xffffffffu : (kk <= r32 ? 0x0000ffffu : 0u);
      PIN(msk[r >> 1]);
    }
    kvsrc = WS_QKV + (((w < 4 ? 160 : 300) + 5 * w + 20 * xcd) * 2048 + 64 * t - 256) * 8 + lane * 16;
    PIN(qa); PIN(qb); PIN(ka); PIN(kb); PIN(va); PIN(kvsrc);
  };

  {
    const int P128 = 4 * (xcd & 3) + (t & 3), s = (xcd >> 2) * 8 + (t >> 2);
    if (s < 15) {
      LAS f32x2* st = (LAS f32x2*)(smem + 131072);
      LAS float* cs = (LAS float*)(smem + 132096);
      const int q4 = (lane & 15) >> 2, pp = lane & 3;
      const int fsw = q4 | ((g & 1) << 2);
      const int xlane = tok * 128 + ((g ^ (tok >> 1)) * 16);
      const int wlane = 16384 + (8 * g + q4) * 256 + pp * 8;
      const int cres = (3 * (s + P128)) % 10;
      const int rot = cres >= 5 ? 5 : 0, jres = cres - rot;
      if (w >= 4) {
        const int a = w - 4, lt = tid - 256;
        const char* xs; const char* wsrc;
        { const int row = lt >> 3, c = (lt & 7) ^ ((row >> 1) & 7); xs = p.x + (size_t)((P128 * 128 + row) * 1280 + c * 16); }
        { const int row = lt >> 4, f = (row & 3) | (((row >> 3) & 1) << 2), c = (lt & 15) ^ (f << 1); wsrc = p.wqkv + (size_t)(row * 3840 + s * 256 + c * 16); }
        auto stage = [&](int i) {
          int kc = i + rot; if (kc >= 10) kc -= 10;
          const unsigned d = lds0 + (i & 3) * 32768 + a * 1024;
          const char* xk = xs + kc * 128; const char* wk = wsrc + (size_t)(kc * (64 * 3840));
#pragma unroll
          for (int q = 0; q < 4; ++q) { glds16(wk + q * (16 * 3840), d + 16384 + q * 4096); glds16(xk + q * (32 * 1280), d + q * 4096); }
        };
        stage(0);
        att_consts();
        stage(1); stage(2);
        WAIT_BAR(16);
        {
          const v2u tv = *(const LAS v2u*)(smem + 132616);
          tag = uni64(tv.x, tv.y);
        }
        stage(3);
        v4u fx[3][2], fw[2][2];
#pragma unroll
        for (int k2 = 0; k2 < 2; ++k2) fx[0][k2] = fx[1][k2] = fx[2][k2] = fw[0][k2] = fw[1][k2] = v4u{0u, 0u, 0u, 0u};
#pragma unroll 1
        for (int i = 0; i < 7; ++i) {
          if (i == jres + 1) {
            const int r = tok & 3;
            f32x4 aC[2], aS[2], aG[2];
#pragma unroll
            for (int e = 0; e < 2; ++e) {
              aC[e] = mfma16(fw[e][1], ONES, mfma16(fw[e][0], ONES, f32x4{0, 0, 0, 0}));
              aS[e] = mfma16(ONES, fx[e][1], mfma16(ONES, fx[e][0], f32x4{0, 0, 0, 0}));
              aG[e] = mfma16(fx[2 * e][1], fx[e][1], mfma16(fx[2 * e][0], fx[e][0], f32x4{0, 0, 0, 0}));
            }
            const bool d1 = g == ((tok >> 2) ^ 1);
            const f32x4 G4 = d1 ? aG[1] : aG[0];
            const float G = r == 0 ? G4[0] : r == 1 ? G4[1] : r == 2 ? G4[2] : G4[3];
            gst64(rs, (s < 10 && (d1 || g == (tok >> 2))) ? WS_SP + ((P128 * 10 + cres) * 128 + (2 * a + (d1 ? 1 : 0)) * 16 + tok) * 8 : 0x7F000000, v2u{asu(d1 ? aS[1][0] : aS[0][0]), asu(G)});
            gst128(rs, (P128 < 10 && tok < 2) ? WS_CP + ((s * 10 + cres) * 128 + (2 * a + tok) * 16 + 4 * g) * 4 : 0x7F000000, __builtin_bit_cast(v4u, tok ? aC[1] : aC[0]));
          }
          if (i == jres) {
            lptr S = L + (i & 3) * 32768;
            const int xl2 = (tok ^ 4) * 128 + ((g ^ ((tok ^ 4) >> 1)) * 16);
#pragma unroll
            for (int k2 = 0; k2 < 2; ++k2) {
#pragma unroll
              for (int e = 0; e < 2; ++e) {
                fx[e][k2] = lds128(S + (2 * a + e) * 2048 + (xlane ^ (k2 * 64)));
                const int wo0 = wlane + (((2 * a + e) ^ fsw) * 32) + k2 * 8192;
                const v2u lo = ldstr(S + wo0), hi = ldstr(S + wo0 + 1024);
                fw[e][k2] = v4u{lo.x, lo.y, hi.x, hi.y};
              }
              fx[2][k2] = lds128(S + (2 * a + 1) * 2048 + (xl2 ^ (k2 * 64)));
            }
          }
          if (i < 6) WAITV(16); else WAITV(0);
          LBAR();
          if (i == (jres < 4 ? jres + 3 : 6) && tid == 256) set_flag(p.ws, WS_F0 + (P128 * 15 + s) * FL_STRIDE, tag);
          if (i + 4 < 10) stage(i + 4);
        }
        if (a < 2) {
          poll_flags(p.ws, WS_F0 + (a == 0 ? P128 * 15 + (lane < 10 ? lane : 0) : (lane < 10 ? lane : 0) * 15 + s) * FL_STRIDE, lane < 10, tag);
          if (a == 0) {
            v4u v[10];
#pragma unroll
            for (int c = 0; c < 10; ++c) v[c] = gld128(rs, WS_SP + ((P128 * 10 + c) * 128 + 2 * lane) * 8);
            float su = 0.f, sq = 0.f, tu = 0.f, tq = 0.f;
#pragma unroll
            for (int c = 0; c < 10; ++c) { su += asf(v[c].x); sq += asf(v[c].y); tu += asf(v[c].z); tq += asf(v[c].w); }
            const float m0 = su * (1.0f / DM), m1 = tu * (1.0f / DM);
            st[2 * lane] = f32x2{m0, 1.0f / sqrtf(fmaxf(sq * (1.0f / DM) - m0 * m0, 0.f) + LN_EPS)};
            st[2 * lane + 1] = f32x2{m1, 1.0f / sqrtf(fmaxf(tq * (1.0f / DM) - m1 * m1, 0.f) + LN_EPS)};
          } else {
            v2u v[10];
#pragma unroll
            for (int c = 0; c < 10; ++c) v[c] = gld64(rs, WS_CP + ((s * 10 + c) * 128 + 2 * lane) * 4);
            float su = 0.f, tu = 0.f;
#pragma unroll
            for (int c = 0; c < 10; ++c) { su += asf(v[c].x); tu += asf(v[c].y); }
            cs[2 * lane] = su; cs[2 * lane + 1] = tu;
          }
          if (a == 1) poll_early();
          LBAR();
        } else {
#pragma unroll 1
          for (int i = 0; i < 2; ++i) {
            const int pu = 375 + P128 * 15 + s + 240 * (2 * (a - 2) + i);
            const char *ps0, *ps1; int ppitch;
            pack_src(p, pu < 1320 ? pu : 0, lane, ps0, ps1, ppitch);
            pack_issue(ps0, ps1, ppitch, lds0 + 133120 + (2 * (a - 2) + i) * 4096);
          }
          LBAR();
          WAITV(0);
#pragma unroll 1
          for (int i = 0; i < 2; ++i) {
            const int pu = 375 + P128 * 15 + s + 240 * (2 * (a - 2) + i);
            if (pu < 1320) { int pd[4]; bool phalf; pack_dest(pu, pd, phalf); pack_finish(rs, L + 133120 + (2 * (a - 2) + i) * 4096, pd, phalf, lane); }
          }
          publish(4, 2u, WS_ARR + b * 8);
        }

      } else {
        const int mh = w >> 1, nh = w & 1;
        int xa[2], wq[4];
        xa[0] = mh * 8192 + xlane; xa[1] = xa[0] ^ 64;
#pragma unroll
        for (int n = 0; n < 4; ++n) wq[n] = wlane + (((4 * nh + n) ^ fsw) * 32);
        f32x4 acc[4][4];
#pragma unroll
        for (int m = 0; m < 4; ++m)
#pragma unroll
          for (int n = 0; n < 4; ++n) acc[m][n] = f32x4{0, 0, 0, 0};
        v4u xb[2][4], wa[2][4];
        auto rdfrag = [&](int k2) {
#pragma unroll
          for (int n = 0; n < 4; ++n) {
            const v2u lo = ldstr(L + wq[n] + k2 * 8192), hi = ldstr(L + wq[n] + k2 * 8192 + 1024);
            wa[k2][n] = v4u{lo.x, lo.y, hi.x, hi.y};
            xb[k2][n] = lds128(L + xa[k2] + n * 2048);
          }
        };
        auto mm = [&](int k2) {
#pragma unroll
          for (int m = 0; m < 4; ++m)
#pragma unroll
            for (int n = 0; n < 4; ++n) acc[m][n] = mfma16(wa[k2][n], xb[k2][m], acc[m][n]);
#pragma unroll
          for (int q = 0; q < 12; ++q) { __builtin_amdgcn_sched_group_barrier(0x008, 1, 0); __builtin_amdgcn_sched_group_barrier(0x100, 1, 0); }
          __builtin_amdgcn_sched_group_barrier(0x008, 4, 0);
          SBAR();
        };
        att_consts();
        if (w == 0) { const u64 tg = read_tag(); if (lane == 0) *(LAS u64*)(smem + 132616) = tg; }
        LBAR();
        {
          const v2u tv = *(const LAS v2u*)(smem + 132616);
          tag = uni64(tv.x, tv.y);
        }
        STAMP(16);
        rdfrag(0);
        SBAR();
#pragma unroll 1
        for (int i = 0; i < 10; ++i) {
          rdfrag(1);
          mm(0);
#pragma unroll
          for (int n = 0; n < 4; ++n) wq[n] = (wq[n] + 32768) & 0x1FFFF;
          xa[0] = (xa[0] + 32768) & 0x1FFFF; xa[1] = (xa[1] + 32768) & 0x1FFFF;
          if (i < 7) LBAR();
          SBAR();
          rdfrag(0);
          mm(1);
        }
        STAMP(1);
        LBAR();
        STAMP(17);
        const float qs = s < 5 ? QSCALE : 1.0f;
        f32x4 c4[4];
#pragma unroll
        for (int n = 0; n < 4; ++n) c4[n] = *(const LAS f32x4*)(cs + (4 * nh + n) * 16 + 4 * g);
        const bool odd = tok & 1;
#pragma unroll
        for (int m = 0; m < 4; ++m) {
          const int mt = 4 * mh + m;
          const f32x2 ms = st[mt * 16 + tok];
          const float sc = ms.y * qs;
#pragma unroll
          for (int n2 = 0; n2 < 2; ++n2) {
            unsigned pk[2][2];
#pragma unroll
            for (int e = 0; e < 2; ++e) {
              const f32x4 a4 = acc[m][2 * n2 + e], cc = c4[2 * n2 + e];
              pk[e][0] = pk2((a4[0] - ms.x * cc[0]) * sc, (a4[1] - ms.x * cc[1]) * sc);
              pk[e][1] = pk2((a4[2] - ms.x * cc[2]) * sc, (a4[3] - ms.x * cc[3]) * sc);
            }
            const unsigned s0 = odd ? pk[0][0] : pk[1][0], s1 = odd ? pk[0][1] : pk[1][1];
            const unsigned r0 = (unsigned)__builtin_amdgcn_mov_dpp((int)s0, 0xB1, 0xF, 0xF, true);
            const unsigned r1 = (unsigned)__builtin_amdgcn_mov_dpp((int)s1, 0xB1, 0xF, 0xF, true);
            const v4u v = odd ? v4u{r0, r1, pk[1][0], pk[1][1]} : v4u{pk[0][0], pk[0][1], r0, r1};
            const int quad = 32 * s + 4 * (4 * nh + 2 * n2 + (odd ? 1 : 0)) + g;
            gst128(rs, WS_QKV + (quad * 2048 + P128 * 128 + mt * 16 + (tok & 14)) * 8, v);
          }
        }
      }
      STAMP(18);
      if (w < 4) publish(0, 4u, WS_F1 + (P128 * 15 + s) * FL_STRIDE);
    } else {
      const int v = ((xcd - 4) * 4 + (t - 28)) * 8 + w;
#pragma unroll 1
      for (int i = 0; i < 3; ++i) {
        const char *ps0, *ps1; int ppitch;
        pack_src(p, v + 128 * i < 375 ? v + 128 * i : 0, lane, ps0, ps1, ppitch);
        pack_issue(ps0, ps1, ppitch, lds0 + (w * 3 + i) * 4096);
      }
      att_consts();
      tag = read_tag();
#pragma unroll 1
      for (int i = 0; i < 3; ++i) {
        const int u = v + 128 * i;
        if (u < 375) { int pd[4]; bool phalf; pack_dest(u, pd, phalf); pack_finish(rs, L + (w * 3 + i) * 4096, pd, phalf, lane); }
      }
      WAIT_BAR(0);
      if (tid == 0) set_flag(p.ws, WS_ARR + b * 8, tag);
      if (w == 6) poll_early();
    }
  }
  STAMP(2);

  const int pg = xcd * 4 + (t & 3), jg = t >> 2;
  v4u wo[5][3];
  v2u xr[3];
  const int ntile = jg < 6 ? 14 : 12, tbase = jg < 7 ? 14 * jg : 96;
  const bool gemm = 2 * w < ntile;
  constexpr int W1E = 6;
  constexpr int W1M = 13;
  constexpr int W2L = 7;
  constexpr int W2LOFF = 8 * 11776 + 1024;
  v4u w1f[20][2];
  const int w1base = gemm ? WS_W1P + (tbase + 2 * w) * 20480 + lane * 16 : 0x7F000000;
  const int w2base = WS_W2P + (jg * 8 + w) * 35840;
  {
    const int pn = t, j = xcd;
    const int tok0 = 64 * pn - 256;
    const int wobase = WS_WOP + (jg * 8 + w) * 12800;
    auto wo_loads = [&](int i) {
      if (i < 5) {
        wo[i][0] = gld128(rs, wobase + i * 2560 + lane * 16);
        wo[i][1] = gld128(rs, wobase + i * 2560 + 1024 + lane * 16);
        wo[i][2] = gld128(rs, wobase + i * 2560 + 2048 + lane * 8);
      } else {
        const char* src = p.x + (size_t)((64 * pg + 16 * (w >> 1) + tok) * 1280 + (80 * jg + 32 * (w & 1) + 4 * g) * 2);
        xr[0] = *(const GAS v2u*)src;
        xr[1] = *(const GAS v2u*)(src + 32);
        xr[2] = *(const GAS v2u*)(p.x + (size_t)((64 * pg + 16 * (w & 3) + tok) * 1280 + (80 * jg + 64 + 4 * g) * 2));
      }
      SBAR();
    };
    auto pre = [&](int i) {
      const int q = t * 8 + w;
      if (q < 216 && lane < 16) touch4(p.ws + (size_t)(WS_W1P + (q >> 1) * 20480 + (32 * i + 16 * (q & 1) + lane) * 128), lds0 + LDS_BYTES - 256);
    };
    auto wo_all = [&]() {
#pragma unroll
      for (int i = 0; i < 6; ++i) wo_loads(i);
    };
    LBAR();
    if (w != 7) wo_all();
    if (w == 7) {
      const int R = lane / 6, rem = lane % 6, pi = rem >> 1;
      const int q0 = 160 * R + 20 * j, sl = (rem & 1) ? (q0 + 19) >> 5 : q0 >> 5;
      const int P = (pn >> 1) - pi;
      const bool want = lane < 18 && P >= 0 && (R > 0 || pi == 0);
      poll_flags(p.ws, WS_F1 + ((want ? P : 0) * 15 + (want ? sl : 0)) * FL_STRIDE, want, tag);
    }
    LBAR();
    STAMP(3);
    {
      const char* src = p.ws + (size_t)kvsrc;
      const unsigned dst = lds0 + 5 * w * PL;
      if (pn >= 4) {
#pragma unroll
        for (int i = 0; i < 5; ++i) { glds16_sc1(src + i * 16384, dst + i * PL); glds16_sc1(src + i * 16384 + 1024, dst + i * PL + 1024); }
      } else {
#pragma unroll 1
        for (int i = 0; i < 10; ++i) if (tok0 + (i & 1) * 128 + lane * 2 >= 0) glds16_sc1(src + (i >> 1) * 16384 + (i & 1) * 1024, dst + (i >> 1) * PL + (i & 1) * 1024);
      }
      if (lane < 32) {
#pragma unroll
        for (int i = 0; i < 5; ++i) glds16_sc1(src + i * 16384 + 2048, dst + i * PL + 2048);
      }
    }
    if (w < 5) {
#pragma unroll
      for (int i = 0; i < 2; ++i) {
        const int pl = 4 * w + 2 * i + (lane >> 5);
        const char* src = p.ws + (size_t)(WS_QKV + ((20 * j + pl) * 2048 + 64 * pn) * 8 + (lane & 31) * 16);
        glds16_sc1(src, lds0 + QOFF + (4 * w + 2 * i) * 512);
      }
    }
    if (tid < 352) *(LAS v2u*)(smem + ONESOFF + tid * 8) = v2u{0x00003F80u, 0u};
    if (pn < 4) {
      const int n16 = -tok0 / 2;
#pragma unroll 1
      for (int i = tid; i < 40 * n16; i += 512) { const int pl = i / n16; *(LAS v4u*)(smem + pl * PL + (i - pl * n16) * 16) = v4u{0u, 0u, 0u, 0u}; }
    }
    if (w == 6) poll_arr(p.ws, tag, lane);
    WAIT_BAR(0);
    STAMP(4);
    if (w == 7) wo_all();
    pre(0);
    v4u qf0, qf1;
    {
      const v2u a = lds64(L + qa), c = lds64(L + qa + 512);
      qf0 = v4u{a.x, a.y, c.x, c.y};
      const v2u d = lds64(L + qb);
      qf1 = h ? v4u{0u, 0u, 0u, 0u} : v4u{d.x, d.y, 0x00003F80u, 0u};
    }
    lptr kp0 = L + ka, kp1 = L + kb;
    auto kfrag = [&](int blk, v4u (&kf)[2]) {
      const v2u a = lds64(kp0 + blk * 256), d = lds64(kp0 + PL + blk * 256), e = lds64(kp1 + blk * 256);
      kf[0] = v4u{a.x, a.y, d.x, d.y};
      kf[1] = v4u{e.x, e.y, blk >= bmin ? 0u : 0x0000F149u  , 0u};
    };
    auto scores = [&](const v4u (&kf)[2], f32x16 c) -> f32x16 { return mfma32(kf[1], qf1, mfma32(kf[0], qf0, c)); };
    f32x16 zero16;
#pragma unroll
    for (int r = 0; r < 16; ++r) zero16[r] = 0.f;
    lptr vp = L + va;
    f32x16 o = zero16;
    auto vfrag = [&](int blk, v4u (&vf)[2]) {
#pragma unroll
      for (int s2 = 0; s2 < 2; ++s2) {
        const v2u a = ldstr(vp + blk * 256 + s2 * 128), c = ldstr(vp + blk * 256 + s2 * 128 + 64);
        vf[s2] = v4u{a.x, a.y, c.x, c.y};
      }
    };
    auto pv = [&](const v4u (&vf)[2], const unsigned (&pk)[8]) {
      o = mfma32(vf[0], v4u{pk[0], pk[1], pk[2], pk[3]}, o);
      o = mfma32(vf[1], v4u{pk[4], pk[5], pk[6], pk[7]}, o);
    };
    auto xmax = [&](float v) {
      const unsigned u = asu(v);
      const auto sw = __builtin_amdgcn_permlane32_swap(u, u, false, false);
      return fmaxf(asf((unsigned)sw[0]), asf((unsigned)sw[1]));
    };
    {
      constexpr float THR = ATT_THR;
      v4u k8[2], k0[2], kn[2][2], v8[2], v0[2];
      kfrag(8, k8); kfrag(0, k0); kfrag(1, kn[1]); kfrag(2, kn[0]);
      vfrag(8, v8); vfrag(0, v0);
      SBAR();
      const f32x16 sa = scores(k8, zero16), sb = scores(k0, zero16);
      SBAR();
      float sm[16];
#pragma unroll
      for (int r = 0; r < 16; ++r) sm[r] = __builtin_amdgcn_inverse_ballot_w64(le_mask(r)) ? sa[r] : sb[r];
      float m = fmaxf(sm[0], sm[1]);
#pragma unroll
      for (int r = 2; r < 16; r += 2) m = fmaxf(fmaxf(m, sm[r]), sm[r + 1]);
      m = xmax(m);
      f32x16 negm;
#pragma unroll
      for (int r = 0; r < 16; ++r) negm[r] = -m;
      SBAR();
      f32x16 sn = scores(kn[1], negm);
      SBAR();
      unsigned p8[8], p0[8];
#pragma unroll
      for (int r = 0; r < 16; r += 2) {
        const unsigned pkd = pk2(__builtin_amdgcn_exp2f(sm[r] - m), __builtin_amdgcn_exp2f(sm[r + 1] - m));
        p8[r >> 1] = pkd & msk[r >> 1]; p0[r >> 1] = pkd & ~msk[r >> 1];
        PIN(p8[r >> 1]); PIN(p0[r >> 1]);
      }
      SBAR();
      pv(v8, p8);
      pv(v0, p0);
      STAMP(13);
#pragma unroll
      for (int blk = 1; blk < 8; ++blk) {
        f32x16 sc = sn;
        v4u vf[2];
        SBAR();
        if (blk < 7) sn = scores(kn[(blk + 1) & 1], negm);
        vfrag(blk, vf);
        if (blk < 6) kfrag(blk + 2, kn[blk & 1]);
        if (blk < 5) pre(blk);
        SBAR();
        float b0 = fmaxf(fmaxf(sc[0], sc[1]), sc[2]), b1 = fmaxf(fmaxf(sc[3], sc[4]), sc[5]);
#pragma unroll
        for (int r = 6; r < 16; r += 4) { b0 = fmaxf(fmaxf(b0, sc[r]), sc[r + 1]); if (r + 3 < 16) b1 = fmaxf(fmaxf(b1, sc[r + 2]), sc[r + 3]); }
        b0 = fmaxf(b0, b1);
        if (__builtin_expect(__any(b0 > THR), 0)) {
          const float d = fmaxf(xmax(b0), 0.f), f = __builtin_amdgcn_exp2f(-d);
#pragma unroll
          for (int r = 0; r < 16; ++r) { sc[r] -= d; sn[r] -= d; negm[r] -= d; o[r] *= f; }
        }
        SBAR();
        unsigned pk[8];
#pragma unroll
        for (int r = 0; r < 16; r += 2) { pk[r >> 1] = pk2(__builtin_amdgcn_exp2f(sc[r]), __builtin_amdgcn_exp2f(sc[r + 1])); PIN(pk[r >> 1]); }
        SBAR();
        pv(vf, pk);
      }
    }
    STAMP(14);
    {
      const unsigned du = asu(o[8]);
      const auto dsw = __builtin_amdgcn_permlane32_swap(du, du, false, false);
      const float inv = __builtin_amdgcn_rcpf(asf((unsigned)dsw[1]));
#pragma unroll
      for (int c = 0; c < 3; ++c) gst64(rs, oadr[c], v2u{pk2(o[4 * c] * inv, o[4 * c + 1] * inv), pk2(o[4 * c + 2] * inv, o[4 * c + 3] * inv)});
    }
    STAMP(19);
    publish(1, 8u, WS_F2 + (pn * 8 + j) * FL_STRIDE);
    LBAR();
  }
  const int pn = pg, j = jg;
  STAMP(5);

  auto red_write = [&](int lane, int hp, const f32x4 (&acc)[2][5]) {
#pragma unroll
    for (int ml = 0; ml < 2; ++ml) {
      LAS char* d = (LAS char*)smem + ((2 * hp + ml) * 8 + w) * 2560;
#pragma unroll
      for (int q = 0; q < 2; ++q) {
        const f32x4 a = acc[ml][2 * q], c = acc[ml][2 * q + 1];
        *(LAS v4u*)(d + q * 1024 + lane * 16) = v4u{pk2(a[0], a[1]), pk2(a[2], a[3]), pk2(c[0], c[1]), pk2(c[2], c[3])};
      }
      const f32x4 a = acc[ml][4];
      *(LAS v2u*)(d + 2048 + lane * 8) = v2u{pk2(a[0], a[1]), pk2(a[2], a[3])};
    }
  };
  auto red_sum = [&](int lane, f32x4& s0, f32x4& s1, f32x4& s2) {
    const int n = lane & 15, gg = lane >> 4;
    const unsigned one = (n >> 2) == gg ? ((n & 1) ? 0x3F800000u : 0x00003F80u) : 0u;
    const unsigned e01 = (n & 2) ? 0u : one, e23 = (n & 2) ? one : 0u;
    const v4u selA = {e01, e23, 0u, 0u}, selB = {0u, 0u, e01, e23};
    v4u r[8]; v2u r2[8];
#pragma unroll
    for (int ww = 0; ww < 8; ++ww) {
      r[ww] = lds128(L + ((w >> 1) * 8 + ww) * 2560 + (w & 1) * 1024 + lane * 16);
      r2[ww] = lds64(L + ((w & 3) * 8 + ww) * 2560 + 2048 + lane * 8);
    }
    SBAR();
#pragma unroll
    for (int ww = 0; ww < 8; ++ww) { s0 = mfma16(selA, r[ww], s0); s1 = mfma16(selB, r[ww], s1); }
    const v4u selC = {e01, e23, e01, e23};
#pragma unroll
    for (int ww = 0; ww < 8; ww += 2) s2 = mfma16(selC, v4u{r2[ww].x, r2[ww].y, r2[ww + 1].x, r2[ww + 1].y}, s2);
    SBAR();
  };
  {
#pragma unroll
    for (int ks = 0; ks < W1E; ++ks) { w1f[ks][0] = gld128(rs, w1base + ks * 1024); w1f[ks][1] = gld128(rs, w1base + (20 + ks) * 1024); }
    SBAR();
    poll_flags(p.ws, WS_F2 + (pn * 8 + w) * FL_STRIDE, lane == 0, tag);
    STAMP(6);
    const int obase = WS_O + (pn * 8 + w) * 10240;
    v4u ba[2][2][3];
    auto oload = [&](int hp) {
#pragma unroll
      for (int ml = 0; ml < 2; ++ml) {
        ba[hp][ml][0] = gld128(rs, obase + (2 * hp + ml) * 2560 + lane * 16);
        ba[hp][ml][1] = gld128(rs, obase + (2 * hp + ml) * 2560 + 1024 + lane * 16);
        const v2u c = gld64(rs, obase + (2 * hp + ml) * 2560 + 2048 + lane * 8);
        ba[hp][ml][2] = v4u{c.x, c.y, 0u, 0u};
      }
      SBAR();
    };
    oload(0); oload(1);
#pragma unroll
    for (int hp = 0; hp < 2; ++hp) {
      f32x4 acc[2][5];
#pragma unroll
      for (int ml = 0; ml < 2; ++ml)
#pragma unroll
        for (int nt = 0; nt < 5; ++nt) acc[ml][nt] = mfma16(wo[nt][0], ba[hp][ml][0], f32x4{0, 0, 0, 0});
#pragma unroll
      for (int k = 1; k < 3; ++k)
#pragma unroll
        for (int ml = 0; ml < 2; ++ml)
#pragma unroll
          for (int nt = 0; nt < 5; ++nt) acc[ml][nt] = mfma16(wo[nt][k], ba[hp][ml][k], acc[ml][nt]);
      red_write(lane, hp, acc);
      SBAR();
    }
    STAMP(27);
    LBAR();
    STAMP(20);
    {
      f32x4 s0 = {bflo(xr[0].x), bfhi(xr[0].x), bflo(xr[0].y), bfhi(xr[0].y)};
      f32x4 s1 = {bflo(xr[1].x), bfhi(xr[1].x), bflo(xr[1].y), bfhi(xr[1].y)};
      f32x4 s2 = {bflo(xr[2].x), bfhi(xr[2].x), bflo(xr[2].y), bfhi(xr[2].y)};
      red_sum(lane, s0, s1, s2);
      const v4u hres = v4u{pk2(s0[0], s0[1]), pk2(s0[2], s0[3]), pk2(s1[0], s1[1]), pk2(s1[2], s1[3])};
      const v4u hres2 = v4u{pk2(s2[0], s2[1]), pk2(s2[2], s2[3]), 0u, 0u};
      const int base = WS_H + (pn * 8 + j) * HB;
      gst128(rs, base + (w >> 1) * 2560 + (w & 1) * 1024 + lane * 16, hres);
      gst64(rs, w < 4 ? base + w * 2560 + 2048 + lane * 8 : 0x7F000000, v2u{hres2.x, hres2.y});
      const f32x4 z4 = {0, 0, 0, 0};
      const f32x4 su = mfma16(ONES, hres, z4), sq = mfma16(hres, hres, z4), tu = mfma16(ONES, hres2, z4), tq = mfma16(hres2, hres2, z4);
      const int r = tok & 3;
      const float q1 = r == 0 ? sq[0] : r == 1 ? sq[1] : r == 2 ? sq[2] : sq[3], q2 = r == 0 ? tq[0] : r == 1 ? tq[1] : r == 2 ? tq[2] : tq[3];
      const bool diag = g == (tok >> 2);
      gst64(rs, diag ? base + 10240 + (((w >> 1) * 3 + (w & 1)) * 16 + tok) * 8 : 0x7F000000, v2u{asu(su[0]), asu(q1)});
      gst64(rs, diag && w < 4 ? base + 10240 + ((w * 3 + 2) * 16 + tok) * 8 : 0x7F000000, v2u{asu(tu[0]), asu(q2)});
    }
    STAMP(21);
    publish(2, 8u, WS_F3 + (pn * 8 + j) * FL_STRIDE);
    STAMP(22);
    LBAR();
#pragma unroll
    for (int ks = W1E; ks < W1M; ++ks) { w1f[ks][0] = gld128(rs, w1base + ks * 1024); w1f[ks][1] = gld128(rs, w1base + (20 + ks) * 1024); }
    SBAR();
  }
  STAMP(7);

  v4u w2f[35];
  auto w2off = [&](int f) { return w2base + f * 1024; };
  {
    constexpr int HL = 11776;
    LAS f32x2* st = (LAS f32x2*)(smem + 8 * HL);
    f32x4 acc[4][2], accC[2];
    poll_flags(p.ws, WS_F3 + (pn * 8 + w) * FL_STRIDE, lane == 0, tag);
    STAMP(8);
    {
      const char* src = p.ws + (size_t)(WS_H + (pn * 8 + w) * HB + lane * 16);
#pragma unroll
      for (int i = 0; i < 11; ++i) glds16_sc1(src + i * 1024, lds0 + w * HL + i * 1024);
      if (lane < 32) glds16_sc1(src + 11 * 1024, lds0 + w * HL + 11 * 1024);
    }
    accC[0] = accC[1] = f32x4{0, 0, 0, 0};
#pragma unroll
    for (int ks = 0; ks < W1M; ++ks) { accC[0] = mfma16(w1f[ks][0], ONES, accC[0]); accC[1] = mfma16(w1f[ks][1], ONES, accC[1]); }
    SBAR();
    WAIT_BAR(0);
    STAMP(9);
    auto bfrag = [&](int ks, v4u (&bf)[4]) {
#pragma unroll
      for (int mt = 0; mt < 4; ++mt) {
        if (ks < 16) bf[mt] = lds128(L + (ks >> 1) * HL + mt * 2560 + (ks & 1) * 1024 + lane * 16);
        else {
          const v2u a = lds64(L + (2 * (ks - 16)) * HL + mt * 2560 + 2048 + lane * 8), c = lds64(L + (2 * (ks - 16) + 1) * HL + mt * 2560 + 2048 + lane * 8);
          bf[mt] = v4u{a.x, a.y, c.x, c.y};
        }
      }
      SBAR();
    };
    if (w == 7) {
      float su = 0.f, sq = 0.f;
#pragma unroll
      for (int q = 0; q < 8; ++q)
#pragma unroll
        for (int part = 0; part < 3; ++part) {
          const f32x2 v = *(const LAS f32x2*)(smem + q * HL + 10240 + ((g * 3 + part) * 16 + tok) * 8);
          su += v.x; sq += v.y;
        }
      const float mean = su * (1.0f / DM);
      const float var = fmaxf(sq * (1.0f / DM) - mean * mean, 0.f);
      st[lane] = f32x2{mean, 1.0f / sqrtf(var + LN_EPS)};
    }
#pragma unroll
    for (int i = 0; i < 4; ++i) { acc[i][0] = f32x4{0, 0, 0, 0}; acc[i][1] = f32x4{0, 0, 0, 0}; }
    v4u bf[2][4];
    bfrag(0, bf[0]);
#pragma unroll
    for (int ks = 0; ks < 20; ++ks) {
      if (ks + 1 < 20) bfrag(ks + 1, bf[(ks + 1) & 1]);
#pragma unroll
      for (int mt = 0; mt < 4; ++mt) {
        acc[mt][0] = mfma16(w1f[ks][0], bf[ks & 1][mt], acc[mt][0]);
        acc[mt][1] = mfma16(w1f[ks][1], bf[ks & 1][mt], acc[mt][1]);
      }
      if (ks >= W1M) { accC[0] = mfma16(w1f[ks][0], ONES, accC[0]); accC[1] = mfma16(w1f[ks][1], ONES, accC[1]); }
      SBAR();
      if (ks < 20 - W1M) { w1f[W1M + ks][0] = gld128(rs, w1base + (W1M + ks) * 1024); w1f[W1M + ks][1] = gld128(rs, w1base + (20 + W1M + ks) * 1024); SBAR(); }
      if (ks >= 6 && ks < 6 + W2L) glds16_sc1(p.ws + (size_t)(w2off(ks - 6) + lane * 16), lds0 + W2LOFF + (w * W2L + ks - 6) * 1024);
      if (ks >= 6 && ks < 19) {
#pragma unroll
        for (int e = 0; e < (ks >= 13 ? 2 : 1); ++e) { const int f = ks < 13 ? W2L + (ks - 6) : W2L + 7 + 2 * (ks - 13) + e; w2f[f] = gld128(rs, w2off(f) + lane * 16); }
        SBAR();
      }
    }
    STAMP(28);
    LBAR();
    STAMP(23);
    {
#pragma unroll
      for (int mt = 0; mt < 4; ++mt) {
        const f32x2 ms = st[mt * 16 + tok];
        float v[8];
#pragma unroll
        for (int e = 0; e < 2; ++e)
#pragma unroll
          for (int r = 0; r < 4; ++r) v[4 * e + r] = acc[mt][e][r] - ms.x * accC[e][r];
        gst128(rs, gemm ? WS_H1 + (((pn * 8 + j) * 7 + w) * 4 + mt) * 1024 + lane * 16 : 0x7F000000, v4u{relu2(pk2(v[0], v[1])), relu2(pk2(v[2], v[3])), relu2(pk2(v[4], v[5])), relu2(pk2(v[6], v[7]))});
      }
    }
    STAMP(24);
    publish(3, 8u, WS_F4 + (pn * 8 + j) * FL_STRIDE);
    LBAR();
  }
  STAMP(10);

  {
    const bool full = w < 6;
    int lz = lane; asm volatile("" : "+v"(lz));
    v4u hres; v2u hres2;
#pragma unroll
    for (int f = 26; f < 35; ++f) w2f[f] = gld128(rs, (f < 30 || full ? w2off(f) : 0x7F000000) + lane * 16);
    SBAR();
    poll_flags(p.ws, WS_F4 + (pn * 8 + w) * FL_STRIDE, lane == 0, tag);
    STAMP(11);
    const int bbase = WS_H1 + (pn * 8 + w) * 28672 + lane * 16;
    v4u bf[2][7][2];
#pragma unroll
    for (int q = 0; q < 7; ++q) {
      const int ks = (q + 2) % 7;
#pragma unroll
      for (int ml = 0; ml < 2; ++ml) bf[0][ks][ml] = gld128(rs, bbase + ((ks < 6 || full ? ks : 0) * 4 + ml) * 1024);
    }
    SBAR();
#pragma unroll
    for (int hp = 0; hp < 2; ++hp) {
      f32x4 acc[2][5];
#pragma unroll
      for (int ml = 0; ml < 2; ++ml)
#pragma unroll
        for (int nt = 0; nt < 5; ++nt) acc[ml][nt] = f32x4{0, 0, 0, 0};
#pragma unroll
      for (int q = 0; q < 7; ++q) {
        const int ks = (q + 2) % 7;
        if (q == 4 || q == 5) {
#pragma unroll
          for (int f = 5 * (q - 4); f < 5 * (q - 3); ++f) if (f < W2L) w2f[f] = lds128(L + W2LOFF + (w * W2L + f) * 1024 + lane * 16);
          SBAR();
        }
#pragma unroll
        for (int ml = 0; ml < 2; ++ml)
#pragma unroll
          for (int nt = 0; nt < 5; ++nt) acc[ml][nt] = mfma16(w2f[ks * 5 + nt], bf[hp][ks][ml], acc[ml][nt]);
        SBAR();
        if (hp == 0) {
#pragma unroll
          for (int ml = 0; ml < 2; ++ml) bf[1][ks][ml] = gld128(rs, bbase + ((ks < 6 || full ? ks : 0) * 4 + 2 + ml) * 1024);
          SBAR();
        }
      }
      red_write(lz, hp, acc);
      SBAR();
      if (hp == 0) {
        STAMP(15); asm volatile("" ::: "memory");
        const int base = WS_H + (pn * 8 + j) * HB;
        hres = gld128(rs, base + (w >> 1) * 2560 + (w & 1) * 1024 + lz * 16);
        hres2 = gld64(rs, base + (w & 3) * 2560 + 2048 + lz * 8);
        SBAR();
      }
    }
    STAMP(29);
    LBAR();
    STAMP(25);
    {
      f32x4 s0 = {0, 0, 0, 0}, s1 = s0, s2 = s0;
      red_sum(lz, s0, s1, s2);
      const LAS f32x2* st = (const LAS f32x2*)(smem + 8 * 11776);
      const float r1 = st[16 * (w >> 1) + (lz & 15)].y, r2 = st[16 * (w & 3) + (lz & 15)].y;
      char* dst = p.out + (size_t)((64 * pn + 16 * (w >> 1) + (lz & 15)) * 1280 + (80 * j + 32 * (w & 1) + 4 * (lz >> 4)) * 2);
      *(GAS v2u*)dst = v2u{pk2(fmaf(s0[0], r1, bflo(hres.x)), fmaf(s0[1], r1, bfhi(hres.x))), pk2(fmaf(s0[2], r1, bflo(hres.y)), fmaf(s0[3], r1, bfhi(hres.y)))};
      *(GAS v2u*)(dst + 32) = v2u{pk2(fmaf(s1[0], r1, bflo(hres.z)), fmaf(s1[1], r1, bfhi(hres.z))), pk2(fmaf(s1[2], r1, bflo(hres.w)), fmaf(s1[3], r1, bfhi(hres.w)))};
      if (w < 4) *(GAS v2u*)(p.out + (size_t)((64 * pn + 16 * w + (lz & 15)) * 1280 + (80 * j + 64 + 4 * (lz >> 4)) * 2)) =
          v2u{pk2(fmaf(s2[0], r2, bflo(hres2.x)), fmaf(s2[1], r2, bfhi(hres2.x))), pk2(fmaf(s2[2], r2, bflo(hres2.y)), fmaf(s2[3], r2, bfhi(hres2.y)))};
    }
  }
  STAMP(12);
  STAMP_DUMP();
  if (b == 0 && tid == 0) set_flag(p.ws, WS_EPOCH, tag);
  asm volatile("mk_code_end:");
}
}

extern "C" void kernel_launch(void* const* d_in, const int* in_sizes, int n_in,
                              void* d_out, int out_size, void* d_ws, size_t ws_size,
                              hipStream_t stream) {
  (void)in_sizes; (void)n_in; (void)out_size; (void)ws_size;
  Params p;
  p.x = (const char*)d_in[0]; p.wqkv = (const char*)d_in[1]; p.wo = (const char*)d_in[2];
  p.w1 = (const char*)d_in[4]; p.w2 = (const char*)d_in[5];
  p.out = (char*)d_out; p.ws = (char*)d_ws;
  (void)hipFuncSetAttribute((const void*)block_kernel, hipFuncAttributeMaxDynamicSharedMemorySize, LDS_BYTES);
  block_kernel<<<256, 512, LDS_BYTES, stream>>>(p);
}
```

```cpp
#include <hip/hip_runtime.h>
#include <hip/hip_bf16.h>

#define GAS __attribute__((address_space(1)))
#define LAS __attribute__((address_space(3)))
#define DI __device__ __forceinline__
typedef unsigned v4u __attribute__((ext_vector_type(4)));
typedef unsigned v2u __attribute__((ext_vector_type(2)));
typedef float f32x2 __attribute__((ext_vector_type(2)));
typedef float f32x4 __attribute__((ext_vector_type(4)));
typedef float f32x16 __attribute__((ext_vector_type(16)));
typedef short bf16x8 __attribute__((ext_vector_type(8)));
typedef short bf16x4 __attribute__((ext_vector_type(4)));
typedef __bf16 bfx2 __attribute__((ext_vector_type(2)));
typedef unsigned long long u64;
typedef GAS u64 gu64;
#define RLX_AGENT __ATOMIC_RELAXED, __HIP_MEMORY_SCOPE_AGENT

namespace {
constexpr int SEQ = 2048, DM = 640, NQKV = 1920, DFF = 1728;
constexpr float LN_EPS = 1e-5f;
constexpr float QSCALE = 0.22360679774997896f * 1.4426950408889634f;

constexpr int FL_STRIDE = 128;
constexpr int WS_EPOCH = 0, WS_TMO = 256, WS_ARR = 1024;
constexpr int WS_F1 = 4096;
constexpr int WS_F2 = WS_F1 + 256 * FL_STRIDE;
constexpr int WS_F3 = WS_F2 + 256 * FL_STRIDE;
constexpr int WS_F4 = WS_F3 + 256 * FL_STRIDE;
constexpr int WS_F0 = WS_F4 + 256 * FL_STRIDE;
constexpr int WS_SP = 512 * 1024;
constexpr int WS_CP = WS_SP + 16 * 10 * 1024;
constexpr int WS_QKV = 1 << 20;
constexpr int WS_O = WS_QKV + 480 * 2048 * 8;
constexpr int HB = 12288;
constexpr int WS_H = WS_O + 32 * 81920;
constexpr int WS_H1 = WS_H + 256 * HB;
constexpr int WS_WOP = WS_H1 + 32 * 8 * 28672;
constexpr int WS_W1P = WS_WOP + 64 * 12800;
constexpr int WS_W2P = WS_W1P + 108 * 20 * 1024;
constexpr int WS_END = WS_W2P + 64 * 35 * 1024;

constexpr int LDS_BYTES = 160 * 1024;
constexpr unsigned SPIN_CAP = 1u << 18;
constexpr int AUX_SC1 = 16;
constexpr float ATT_THR = 8.0f;

constexpr u64 le_mask(int r) {
  u64 m = 0;
  for (int l = 0; l < 64; ++l) if ((r & 3) + 8 * (r >> 2) + 4 * (l >> 5) <= (l & 31)) m |= 1ull << l;
  return m;
}

struct Params {
  const char* x; const char* wqkv; const char* wo; const char* w1; const char* w2;
  char* out; char* ws;
};

DI f32x4 mfma16(v4u a, v4u b, f32x4 c) {
  return __builtin_amdgcn_mfma_f32_16x16x32_bf16(__builtin_bit_cast(bf16x8, a), __builtin_bit_cast(bf16x8, b), c, 0, 0, 0);
}
DI f32x16 mfma32(v4u a, v4u b, f32x16 c) {
  return __builtin_amdgcn_mfma_f32_32x32x16_bf16(__builtin_bit_cast(bf16x8, a), __builtin_bit_cast(bf16x8, b), c, 0, 0, 0);
}
DI unsigned pk2(float lo, float hi) {
  f32x2 v = {lo, hi};
  return __builtin_bit_cast(unsigned, __builtin_convertvector(v, bfx2));
}
DI float asf(unsigned u) { return __builtin_bit_cast(float, u); }
DI unsigned asu(float f) { return __builtin_bit_cast(unsigned, f); }
typedef short s16x2 __attribute__((ext_vector_type(2)));
DI unsigned relu2(unsigned u) {
  const s16x2 v = __builtin_bit_cast(s16x2, u);
  return __builtin_bit_cast(unsigned, __builtin_elementwise_max(v, s16x2{0, 0}));
}
DI u64 uni64(unsigned lo, unsigned hi) {
  const unsigned l = (unsigned)__builtin_amdgcn_readfirstlane(lo), h = (unsigned)__builtin_amdgcn_readfirstlane(hi);
  return ((u64)h << 32) | (u64)l;
}
DI float bflo(unsigned u) { return __builtin_bit_cast(float, u << 16); }
DI float bfhi(unsigned u) { return __builtin_bit_cast(float, u & 0xffff0000u); }

DI void glds16(const char* g, unsigned lds_base) {
  unsigned sv;
  asm volatile("s_mov_b32 %0, m0\n\ts_mov_b32 m0, %2\n\ts_nop 0\n\tglobal_load_lds_dwordx4 %1, off\n\ts_mov_b32 m0, %0"
               : "=&s"(sv) : "v"(g), "s"(lds_base) : "memory");
}
DI void glds16_sc1(const char* g, unsigned lds_base) {
  unsigned sv;
  asm volatile("s_mov_b32 %0, m0\n\ts_mov_b32 m0, %2\n\ts_nop 0\n\tglobal_load_lds_dwordx4 %1, off sc1\n\ts_mov_b32 m0, %0"
               : "=&s"(sv) : "v"(g), "s"(lds_base) : "memory");
}
DI void touch4(const char* g, unsigned lds_base) {
  unsigned sv;
  asm volatile("s_mov_b32 %0, m0\n\ts_mov_b32 m0, %2\n\ts_nop 0\n\tglobal_load_lds_dword %1, off sc1\n\ts_mov_b32 m0, %0"
               : "=&s"(sv) : "v"(g), "s"(lds_base) : "memory");
}
#define WAITV(N) asm volatile("s_waitcnt vmcnt(" #N ")" ::: "memory")
#define WAIT_BAR(N) asm volatile("s_waitcnt vmcnt(" #N ") lgkmcnt(0)\n\ts_barrier" ::: "memory")
#define LBAR() asm volatile("s_waitcnt lgkmcnt(0)\n\ts_barrier" ::: "memory")
#define SBAR() __builtin_amdgcn_sched_barrier(0)
#define PIN(x) asm volatile("" : "+v"(x))

typedef LAS const char* lptr;
DI v4u lds128(lptr p) { return *(const LAS v4u*)p; }
DI v2u lds64(lptr p) { return *(const volatile LAS v2u*)p; }
DI v2u ldstr(lptr p) { return __builtin_bit_cast(v2u, __builtin_amdgcn_ds_read_tr16_b64_v4i16((LAS bf16x4*)p)); }

DI v4u gld128(__amdgpu_buffer_rsrc_t rs, int off) { return __builtin_amdgcn_raw_buffer_load_b128(rs, off, 0, AUX_SC1); }
DI v2u gld64(__amdgpu_buffer_rsrc_t rs, int off) { return __builtin_amdgcn_raw_buffer_load_b64(rs, off, 0, AUX_SC1); }
DI void gst128(__amdgpu_buffer_rsrc_t rs, int off, v4u v) { __builtin_amdgcn_raw_buffer_store_b128(v, rs, off, 0, AUX_SC1); }
DI void gst64(__amdgpu_buffer_rsrc_t rs, int off, v2u v) { __builtin_amdgcn_raw_buffer_store_b64(v, rs, off, 0, AUX_SC1); }

DI void poll_flags(char* ws, int off, bool want, u64 tag) {
  gu64* f = (gu64*)(ws + off);
  for (unsigned spins = 0;;) {
    u64 v = tag;
    if (want) v = __hip_atomic_load(f, RLX_AGENT);
    if (__all(v == tag)) break;
    if (++spins > SPIN_CAP) { __hip_atomic_store((gu64*)(ws + WS_TMO), tag, RLX_AGENT); break; }
    if ((spins & 1023u) == 0u && __builtin_amdgcn_readfirstlane((unsigned)(__hip_atomic_load((gu64*)(ws + WS_TMO), RLX_AGENT) == tag))) break;
  }
  __builtin_amdgcn_fence(__ATOMIC_ACQUIRE, "wavefront");
}
DI void poll_arr(char* ws, u64 tag, int lane) {
  gu64* f = (gu64*)(ws + WS_ARR) + lane;
  for (unsigned spins = 0;;) {
    const u64 a = __hip_atomic_load(f, RLX_AGENT), b = __hip_atomic_load(f + 64, RLX_AGENT), c = __hip_atomic_load(f + 128, RLX_AGENT), d = __hip_atomic_load(f + 192, RLX_AGENT);
    if (__all(a == tag && b == tag && c == tag && d == tag)) break;
    if (++spins > SPIN_CAP) { __hip_atomic_store((gu64*)(ws + WS_TMO), tag, RLX_AGENT); break; }
    if ((spins & 1023u) == 0u && __builtin_amdgcn_readfirstlane((unsigned)(__hip_atomic_load((gu64*)(ws + WS_TMO), RLX_AGENT) == tag))) break;
  }
  __builtin_amdgcn_fence(__ATOMIC_ACQUIRE, "wavefront");
}
DI void set_flag(char* ws, int off, u64 tag) { __hip_atomic_store((gu64*)(ws + off), tag, RLX_AGENT); }


DI void pack_src(const Params& p, int u, int lane, const char*& s0, const char*& s1, int& pitch) {
  const char* m; int r0, r1, cg;
  if (u < 240) {
    const int ru = u / 10, w = ru / 3, part = ru - 3 * w;
    cg = u - 10 * ru; m = p.wo; pitch = 1280; r0 = 80 * w + 32 * part; r1 = part == 2 ? r0 : r0 + 16;
  } else if (u < 780) {
    const int v = u - 240, ks = v / 27;
    cg = v - 27 * ks; m = p.w1; pitch = 3456;
    if (ks < 16) { r0 = 80 * (ks >> 1) + 32 * (ks & 1); r1 = r0 + 16; } else { r0 = 160 * (ks - 16) + 64; r1 = r0 + 80; }
  } else {
    const int v = u - 780, ks = v / 10;
    cg = v - 10 * ks; m = p.w2; pitch = 1280; r0 = 32 * ks; r1 = r0 + 16;
  }
  const int lr = lane >> 3, c = (lane & 7) ^ (((lr >> 1) & 3) << 1);
  s0 = m + (size_t)((r0 + lr) * pitch + cg * 128 + c * 16);
  s1 = m + (size_t)((r1 + lr) * pitch + cg * 128 + c * 16);
}
DI void pack_issue(const char* s0, const char* s1, int pitch, unsigned lds) {
  glds16(s0, lds); glds16(s0 + 8 * pitch, lds + 1024); glds16(s1, lds + 2048); glds16(s1 + 8 * pitch, lds + 3072);
}

DI void pack_dest(int u, int (&d)[4], bool& half) {
  int T0, base, sj, sn;
  half = false;
  if (u < 240) {
    const int ru = u / 10, w = ru / 3, part = ru - 3 * w;
    T0 = 4 * (u - 10 * ru); base = WS_WOP + w * 12800 + part * 1024; sj = 8 * 12800; sn = 2560; half = part == 2;
  } else if (u < 780) {
    const int v = u - 240, ks = v / 27;
    T0 = 4 * (v - 27 * ks); base = WS_W1P + ks * 1024; sj = 5 * 20480; sn = 20480;
  } else {
    const int v = u - 780, ru = v / 10;
    const int w = ru < 42 ? ru / 7 : 6 + (ru - 42) / 6, ks = ru < 42 ? ru - 7 * w : ru - 42 - 6 * (w - 6);
    T0 = 4 * (v - 10 * ru); base = WS_W2P + w * 35840 + ks * 5120; sj = 8 * 35840; sn = 1024;
  }
#pragma unroll
  for (int t = 0; t < 4; ++t) { const int T = T0 + t, jj = T / 5; d[t] = base + jj * sj + (T - 5 * jj) * sn; }
}
DI void pack_finish(__amdgpu_buffer_rsrc_t rs, lptr L, const int (&d)[4], bool half, int lane) {
  const int row = 4 * (lane >> 4) + ((lane & 15) >> 2);
  const int a0 = row * 128 + (lane & 3) * 8, sw = (row >> 1) & 3;
  v2u lo[4], hi[4];
#pragma unroll
  for (int t = 0; t < 4; ++t) { lo[t] = ldstr(L + a0 + ((t ^ sw) * 32)); hi[t] = ldstr(L + a0 + ((t ^ sw) * 32) + 2048); }
  if (half) {
#pragma unroll
    for (int t = 0; t < 4; ++t) gst64(rs, d[t] + lane * 8, lo[t]);
  } else {
#pragma unroll
    for (int t = 0; t < 4; ++t) gst128(rs, d[t] + lane * 16, v4u{lo[t].x, lo[t].y, hi[t].x, hi[t].y});
  }
}

__global__ void __launch_bounds__(512, 2) block_kernel(Params p) {
  extern __shared__ __attribute__((aligned(16))) char smem[];
  const int tid = threadIdx.x, lane = tid & 63, w = __builtin_amdgcn_readfirstlane(tid >> 6);
  const int tok = lane & 15, g = lane >> 4;
  const int b = blockIdx.x, xcd = b & 7, t = b >> 3;
  const unsigned lds0 = (unsigned)(size_t)smem;
  lptr L = (lptr)smem;
  const __amdgpu_buffer_rsrc_t rs = __builtin_amdgcn_make_buffer_rsrc(p.ws, 0, WS_END, 0x00020000);
  {
    unsigned len;
    asm volatile("mk_code_begin:\n\ts_mov_b32 %0, mk_code_end-mk_code_begin" : "=s"(len));
    const char* pc = (const char*)__builtin_amdgcn_s_getpc();
    if (w < 4) for (unsigned off = (unsigned)tid * 128u; off < len; off += 256u * 128u) touch4(pc + off, lds0 + LDS_BYTES - 256);
  }
  auto read_tag = [&]() -> u64 {
    const u64 e0 = __hip_atomic_load((gu64*)(p.ws + WS_EPOCH), RLX_AGENT);
    return uni64((unsigned)e0, (unsigned)(e0 >> 32)) + 0x9E3779B97F4A7C15ull;
  };
  const v4u ONES = {0x3F803F80u, 0x3F803F80u, 0x3F803F80u, 0x3F803F80u};
  u64 tag;
  LAS unsigned* cnt = (LAS unsigned*)(smem + LDS_BYTES - 1792);
  if (tid < 8) cnt[tid] = 0u;
  auto publish = [&](int which, unsigned n, int flag_off) {
    WAITV(0);
    unsigned old = 0;
    if (lane == 0) old = __hip_atomic_fetch_add(cnt + which, 1u, __ATOMIC_RELAXED, __HIP_MEMORY_SCOPE_WORKGROUP);
    if (lane == 0 && old == n - 1u) set_flag(p.ws, flag_off, tag);
  };

  auto poll_early = [&]() { poll_flags(p.ws, WS_ARR + ((28 + (lane & 3)) * 8 + 4 + ((lane >> 2) & 3)) * 8, lane < 16, tag); };

  constexpr int PL = 2592, QOFF = 40 * PL, ONESOFF = QOFF + 20 * 512;
  const int hl = w >> 1, qh = w & 1, r32 = lane & 31, h = lane >> 5;
  const int bmin = 8 - 2 * t - qh;
  int qa, qb, ka, kb, va, kvsrc, oadr[3];
  unsigned msk[8];
  auto att_consts = [&]() {
    qa = QOFF + (5 * hl + 2 * h) * 512 + (32 * qh + r32) * 8; qb = QOFF + (5 * hl + 4) * 512 + (32 * qh + r32) * 8;
    ka = (5 * hl + 2 * h) * PL + (32 * qh + r32) * 8; kb = (5 * hl + 4) * PL + (32 * qh + r32) * 8;
    const int blk16 = (lane >> 4) & 1, q4 = (lane & 15) >> 2, pp = lane & 3;
    va = ((blk16 == 0) ? (20 + 5 * hl + pp) * PL : (pp == 0 ? (24 + 5 * hl) * PL : ONESOFF + (((20 + 5 * hl) * 32 + 160) & 255))) + (32 * qh + 4 * h + q4) * 8;
#pragma unroll
    for (int c = 0; c < 3; ++c) {
      const int Qd = 5 * hl + (c < 2 ? 2 * c + h : 4), nt = Qd >> 2, gg = Qd & 3, tk = r32 & 15;
      const int off = nt < 4 ? (nt >> 1) * 1024 + (tk + 16 * gg) * 16 + (nt & 1) * 8 : 2048 + (tk + 16 * gg) * 8;
      oadr[c] = (c < 2 || h == 0) ? WS_O + ((t * 8 + xcd) * 4 + 2 * qh + (r32 >> 4)) * 2560 + off : 0x7F000000;
      PIN(oadr[c]);
    }
#pragma unroll
    for (int r = 0; r < 16; r += 2) {
      const int kk = (r & 3) + 8 * (r >> 2) + 4 * h;
      msk[r >> 1] = kk + 1 <= r32 ? 0xffffffffu : (kk <= r32 ? 0x0000ffffu : 0u);
      PIN(msk[r >> 1]);
    }
    kvsrc = WS_QKV + (((w < 4 ? 160 : 300) + 5 * w + 20 * xcd) * 2048 + 64 * t - 256) * 8 + lane * 16;
    PIN(qa); PIN(qb); PIN(ka); PIN(kb); PIN(va); PIN(kvsrc);
  };

  {
    const int P128 = 4 * (xcd & 3) + (t & 3), s = (xcd >> 2) * 8 + (t >> 2);
    if (s < 15) {
      LAS f32x2* st = (LAS f32x2*)(smem + 131072);
      LAS float* cs = (LAS float*)(smem + 132096);
      const int q4 = (lane & 15) >> 2, pp = lane & 3;
      const int fsw = q4 | ((g & 1) << 2);
      const int xlane = tok * 128 + ((g ^ (tok >> 1)) * 16);
      const int wlane = 16384 + (8 * g + q4) * 256 + pp * 8;
      const int cres = (3 * (s + P128)) % 10;
      const int rot = cres >= 5 ? 5 : 0, jres = cres - rot;
      if (w >= 4) {
        const int a = w - 4, lt = tid - 256;
        const char* xs; const char* wsrc;
        { const int row = lt >> 3, c = (lt & 7) ^ ((row >> 1) & 7); xs = p.x + (size_t)((P128 * 128 + row) * 1280 + c * 16); }
        { const int row = lt >> 4, f = (row & 3) | (((row >> 3) & 1) << 2), c = (lt & 15) ^ (f << 1); wsrc = p.wqkv + (size_t)(row * 3840 + s * 256 + c * 16); }
        auto stage = [&](int i) {
          int kc = i + rot; if (kc >= 10) kc -= 10;
          const unsigned d = lds0 + (i & 3) * 32768 + a * 1024;
          const char* xk = xs + kc * 128; const char* wk = wsrc + (size_t)(kc * (64 * 3840));
#pragma unroll
          for (int q = 0; q < 4; ++q) { glds16(wk + q * (16 * 3840), d + 16384 + q * 4096); glds16(xk + q * (32 * 1280), d + q * 4096); }
        };
        stage(0);
        att_consts();
        stage(1); stage(2);
        WAIT_BAR(16);
        {
          const v2u tv = *(const LAS v2u*)(smem + 132616);
          tag = uni64(tv.x, tv.y);
        }
        stage(3);
        v4u fx[3][2], fw[2][2];
#pragma unroll
        for (int k2 = 0; k2 < 2; ++k2) fx[0][k2] = fx[1][k2] = fx[2][k2] = fw[0][k2] = fw[1][k2] = v4u{0u, 0u, 0u, 0u};
#pragma unroll 1
        for (int i = 0; i < 7; ++i) {
          if (i == jres + 1) {
            const int r = tok & 3;
            f32x4 aC[2], aS[2], aG[2];
#pragma unroll
            for (int e = 0; e < 2; ++e) {
              aC[e] = mfma16(fw[e][1], ONES, mfma16(fw[e][0], ONES, f32x4{0, 0, 0, 0}));
              aS[e] = mfma16(ONES, fx[e][1], mfma16(ONES, fx[e][0], f32x4{0, 0, 0, 0}));
              aG[e] = mfma16(fx[2 * e][1], fx[e][1], mfma16(fx[2 * e][0], fx[e][0], f32x4{0, 0, 0, 0}));
            }
            const bool d1 = g == ((tok >> 2) ^ 1);
            const f32x4 G4 = d1 ? aG[1] : aG[0];
            const float G = r == 0 ? G4[0] : r == 1 ? G4[1] : r == 2 ? G4[2] : G4[3];
            gst64(rs, (s < 10 && (d1 || g == (tok >> 2))) ? WS_SP + ((P128 * 10 + cres) * 128 + (2 * a + (d1 ? 1 : 0)) * 16 + tok) * 8 : 0x7F000000, v2u{asu(d1 ? aS[1][0] : aS[0][0]), asu(G)});
            gst128(rs, (P128 < 10 && tok < 2) ? WS_CP + ((s * 10 + cres) * 128 + (2 * a + tok) * 16 + 4 * g) * 4 : 0x7F000000, __builtin_bit_cast(v4u, tok ? aC[1] : aC[0]));
          }
          if (i == jres) {
            lptr S = L + (i & 3) * 32768;
            const int xl2 = (tok ^ 4) * 128 + ((g ^ ((tok ^ 4) >> 1)) * 16);
#pragma unroll
            for (int k2 = 0; k2 < 2; ++k2) {
#pragma unroll
              for (int e = 0; e < 2; ++e) {
                fx[e][k2] = lds128(S + (2 * a + e) * 2048 + (xlane ^ (k2 * 64)));
                const int wo0 = wlane + (((2 * a + e) ^ fsw) * 32) + k2 * 8192;
                const v2u lo = ldstr(S + wo0), hi = ldstr(S + wo0 + 1024);
                fw[e][k2] = v4u{lo.x, lo.y, hi.x, hi.y};
              }
              fx[2][k2] = lds128(S + (2 * a + 1) * 2048 + (xl2 ^ (k2 * 64)));
            }
          }
          if (i < 6) WAITV(16); else WAITV(0);
          LBAR();
          if (i == (jres < 4 ? jres + 3 : 6) && tid == 256) set_flag(p.ws, WS_F0 + (P128 * 15 + s) * FL_STRIDE, tag);
          if (i + 4 < 10) stage(i + 4);
        }
        if (a < 2) {
          poll_flags(p.ws, WS_F0 + (a == 0 ? P128 * 15 + (lane < 10 ? lane : 0) : (lane < 10 ? lane : 0) * 15 + s) * FL_STRIDE, lane < 10, tag);
          if (a == 0) {
            v4u v[10];
#pragma unroll
            for (int c = 0; c < 10; ++c) v[c] = gld128(rs, WS_SP + ((P128 * 10 + c) * 128 + 2 * lane) * 8);
            float su = 0.f, sq = 0.f, tu = 0.f, tq = 0.f;
#pragma unroll
            for (int c = 0; c < 10; ++c) { su += asf(v[c].x); sq += asf(v[c].y); tu += asf(v[c].z); tq += asf(v[c].w); }
            const float m0 = su * (1.0f / DM), m1 = tu * (1.0f / DM);
            st[2 * lane] = f32x2{m0, 1.0f / sqrtf(fmaxf(sq * (1.0f / DM) - m0 * m0, 0.f) + LN_EPS)};
            st[2 * lane + 1] = f32x2{m1, 1.0f / sqrtf(fmaxf(tq * (1.0f / DM) - m1 * m1, 0.f) + LN_EPS)};
          } else {
            v2u v[10];
#pragma unroll
            for (int c = 0; c < 10; ++c) v[c] = gld64(rs, WS_CP + ((s * 10 + c) * 128 + 2 * lane) * 4);
            float su = 0.f, tu = 0.f;
#pragma unroll
            for (int c = 0; c < 10; ++c) { su += asf(v[c].x); tu += asf(v[c].y); }
            cs[2 * lane] = su; cs[2 * lane + 1] = tu;
          }
          if (a == 1) poll_early();
          LBAR();
        } else {
#pragma unroll 1
          for (int i = 0; i < 2; ++i) {
            const int pu = 375 + P128 * 15 + s + 240 * (2 * (a - 2) + i);
            const char *ps0, *ps1; int ppitch;
            pack_src(p, pu < 1320 ? pu : 0, lane, ps0, ps1, ppitch);
            pack_issue(ps0, ps1, ppitch, lds0 + 133120 + (2 * (a - 2) + i) * 4096);
          }
          LBAR();
          WAITV(0);
#pragma unroll 1
          for (int i = 0; i < 2; ++i) {
            const int pu = 375 + P128 * 15 + s + 240 * (2 * (a - 2) + i);
            if (pu < 1320) { int pd[4]; bool phalf; pack_dest(pu, pd, phalf); pack_finish(rs, L + 133120 + (2 * (a - 2) + i) * 4096, pd, phalf, lane); }
          }
          publish(4, 2u, WS_ARR + b * 8);
        }

      } else {
        const int mh = w >> 1, nh = w & 1;
        int xa[2], wq[4];
        xa[0] = mh * 8192 + xlane; xa[1] = xa[0] ^ 64;
#pragma unroll
        for (int n = 0; n < 4; ++n) wq[n] = wlane + (((4 * nh + n) ^ fsw) * 32);
        f32x4 acc[4][4];
#pragma unroll
        for (int m = 0; m < 4; ++m)
#pragma unroll
          for (int n = 0; n < 4; ++n) acc[m][n] = f32x4{0, 0, 0, 0};
        v4u xb[2][4], wa[2][4];
        auto rdfrag = [&](int k2) {
#pragma unroll
          for (int n = 0; n < 4; ++n) {
            const v2u lo = ldstr(L + wq[n] + k2 * 8192), hi = ldstr(L + wq[n] + k2 * 8192 + 1024);
            wa[k2][n] = v4u{lo.x, lo.y, hi.x, hi.y};
            xb[k2][n] = lds128(L + xa[k2] + n * 2048);
          }
        };
        auto mm = [&](int k2) {
#pragma unroll
          for (int m = 0; m < 4; ++m)
#pragma unroll
            for (int n = 0; n < 4; ++n) acc[m][n] = mfma16(wa[k2][n], xb[k2][m], acc[m][n]);
#pragma unroll
          for (int q = 0; q < 12; ++q) { __builtin_amdgcn_sched_group_barrier(0x008, 1, 0); __builtin_amdgcn_sched_group_barrier(0x100, 1, 0); }
          __builtin_amdgcn_sched_group_barrier(0x008, 4, 0);
          SBAR();
        };
        att_consts();
        if (w == 0) { const u64 tg = read_tag(); if (lane == 0) *(LAS u64*)(smem + 132616) = tg; }
        LBAR();
        {
          const v2u tv = *(const LAS v2u*)(smem + 132616);
          tag = uni64(tv.x, tv.y);
        }
        rdfrag(0);
        SBAR();
#pragma unroll 1
        for (int i = 0; i < 10; ++i) {
          rdfrag(1);
          mm(0);
#pragma unroll
          for (int n = 0; n < 4; ++n) wq[n] = (wq[n] + 32768) & 0x1FFFF;
          xa[0] = (xa[0] + 32768) & 0x1FFFF; xa[1] = (xa[1] + 32768) & 0x1FFFF;
          if (i < 7) LBAR();
          SBAR();
          rdfrag(0);
          mm(1);
        }
        LBAR();
        const float qs = s < 5 ? QSCALE : 1.0f;
        f32x4 c4[4];
#pragma unroll
        for (int n = 0; n < 4; ++n) c4[n] = *(const LAS f32x4*)(cs + (4 * nh + n) * 16 + 4 * g);
        const bool odd = tok & 1;
#pragma unroll
        for (int m = 0; m < 4; ++m) {
          const int mt = 4 * mh + m;
          const f32x2 ms = st[mt * 16 + tok];
          const float sc = ms.y * qs;
#pragma unroll
          for (int n2 = 0; n2 < 2; ++n2) {
            unsigned pk[2][2];
#pragma unroll
            for (int e = 0; e < 2; ++e) {
              const f32x4 a4 = acc[m][2 * n2 + e], cc = c4[2 * n2 + e];
              pk[e][0] = pk2((a4[0] - ms.x * cc[0]) * sc, (a4[1] - ms.x * cc[1]) * sc);
              pk[e][1] = pk2((a4[2] - ms.x * cc[2]) * sc, (a4[3] - ms.x * cc[3]) * sc);
            }
            const unsigned s0 = odd ? pk[0][0] : pk[1][0], s1 = odd ? pk[0][1] : pk[1][1];
            const unsigned r0 = (unsigned)__builtin_amdgcn_mov_dpp((int)s0, 0xB1, 0xF, 0xF, true);
            const unsigned r1 = (unsigned)__builtin_amdgcn_mov_dpp((int)s1, 0xB1, 0xF, 0xF, true);
            const v4u v = odd ? v4u{r0, r1, pk[1][0], pk[1][1]} : v4u{pk[0][0], pk[0][1], r0, r1};
            const int quad = 32 * s + 4 * (4 * nh + 2 * n2 + (odd ? 1 : 0)) + g;
            gst128(rs, WS_QKV + (quad * 2048 + P128 * 128 + mt * 16 + (tok & 14)) * 8, v);
          }
        }
      }
      if (w < 4) publish(0, 4u, WS_F1 + (P128 * 15 + s) * FL_STRIDE);
    } else {
      const int v = ((xcd - 4) * 4 + (t - 28)) * 8 + w;
#pragma unroll 1
      for (int i = 0; i < 3; ++i) {
        const char *ps0, *ps1; int ppitch;
        pack_src(p, v + 128 * i < 375 ? v + 128 * i : 0, lane, ps0, ps1, ppitch);
        pack_issue(ps0, ps1, ppitch, lds0 + (w * 3 + i) * 4096);
      }
      att_consts();
      tag = read_tag();
#pragma unroll 1
      for (int i = 0; i < 3; ++i) {
        const int u = v + 128 * i;
        if (u < 375) { int pd[4]; bool phalf; pack_dest(u, pd, phalf); pack_finish(rs, L + (w * 3 + i) * 4096, pd, phalf, lane); }
      }
      WAIT_BAR(0);
      if (tid == 0) set_flag(p.ws, WS_ARR + b * 8, tag);
      if (w == 6) poll_early();
    }
  }

  const int pg = xcd * 4 + (t & 3), jg = t >> 2;
  v4u wo[5][3];
  v2u xr[3];
  const int ntile = jg < 6 ? 14 : 12, tbase = jg < 7 ? 14 * jg : 96;
  const bool gemm = 2 * w < ntile;
  constexpr int W1E = 8;
  constexpr int W1M = 14;
  constexpr int W2L = 7;
  constexpr int W2LOFF = 8 * 11776 + 1024;
  v4u w1f[20][2];
  const int w1base = gemm ? WS_W1P + (tbase + 2 * w) * 20480 + lane * 16 : 0x7F000000;
  const int w2base = WS_W2P + (jg * 8 + w) * 35840;
  {
    const int pn = t, j = xcd;
    const int tok0 = 64 * pn - 256;
    const int wobase = WS_WOP + (jg * 8 + w) * 12800;
    auto wo_loads = [&](int i) {
      if (i < 5) {
        wo[i][0] = gld128(rs, wobase + i * 2560 + lane * 16);
        wo[i][1] = gld128(rs, wobase + i * 2560 + 1024 + lane * 16);
        wo[i][2] = gld128(rs, wobase + i * 2560 + 2048 + lane * 8);
      } else {
        const char* src = p.x + (size_t)((64 * pg + 16 * (w >> 1) + tok) * 1280 + (80 * jg + 32 * (w & 1) + 4 * g) * 2);
        xr[0] = *(const GAS v2u*)src;
        xr[1] = *(const GAS v2u*)(src + 32);
        xr[2] = *(const GAS v2u*)(p.x + (size_t)((64 * pg + 16 * (w & 3) + tok) * 1280 + (80 * jg + 64 + 4 * g) * 2));
      }
      SBAR();
    };
    auto pre = [&](int i) {
      const int q = t * 8 + w;
      if (q < 216 && lane < 16) touch4(p.ws + (size_t)(WS_W1P + (q >> 1) * 20480 + (32 * i + 16 * (q & 1) + lane) * 128), lds0 + LDS_BYTES - 256);
    };
    auto wo_all = [&]() {
#pragma unroll
      for (int i = 0; i < 6; ++i) wo_loads(i);
    };
    LBAR();
    if (w != 7) wo_all();
    if (w == 7) {
      const int R = lane / 6, rem = lane % 6, pi = rem >> 1;
      const int q0 = 160 * R + 20 * j, sl = (rem & 1) ? (q0 + 19) >> 5 : q0 >> 5;
      const int P = (pn >> 1) - pi;
      const bool want = lane < 18 && P >= 0 && (R > 0 || pi == 0);
      poll_flags(p.ws, WS_F1 + ((want ? P : 0) * 15 + (want ? sl : 0)) * FL_STRIDE, want, tag);
    }
    LBAR();
    {
      const char* src = p.ws + (size_t)kvsrc;
      const unsigned dst = lds0 + 5 * w * PL;
      if (pn >= 4) {
#pragma unroll
        for (int i = 0; i < 5; ++i) { glds16_sc1(src + i * 16384, dst + i * PL); glds16_sc1(src + i * 16384 + 1024, dst + i * PL + 1024); }
      } else {
#pragma unroll 1
        for (int i = 0; i < 10; ++i) if (tok0 + (i & 1) * 128 + lane * 2 >= 0) glds16_sc1(src + (i >> 1) * 16384 + (i & 1) * 1024, dst + (i >> 1) * PL + (i & 1) * 1024);
      }
      if (lane < 32) {
#pragma unroll
        for (int i = 0; i < 5; ++i) glds16_sc1(src + i * 16384 + 2048, dst + i * PL + 2048);
      }
    }
    if (w < 5) {
#pragma unroll
      for (int i = 0; i < 2; ++i) {
        const int pl = 4 * w + 2 * i + (lane >> 5);
        const char* src = p.ws + (size_t)(WS_QKV + ((20 * j + pl) * 2048 + 64 * pn) * 8 + (lane & 31) * 16);
        glds16_sc1(src, lds0 + QOFF + (4 * w + 2 * i) * 512);
      }
    }
    if (tid < 352) *(LAS v2u*)(smem + ONESOFF + tid * 8) = v2u{0x00003F80u, 0u};
    if (pn < 4) {
      const int n16 = -tok0 / 2;
#pragma unroll 1
      for (int i = tid; i < 40 * n16; i += 512) { const int pl = i / n16; *(LAS v4u*)(smem + pl * PL + (i - pl * n16) * 16) = v4u{0u, 0u, 0u, 0u}; }
    }
    if (w == 6) poll_arr(p.ws, tag, lane);
    WAIT_BAR(0);
    if (w == 7) wo_all();
    pre(0);
    v4u qf0, qf1;
    {
      const v2u a = lds64(L + qa), c = lds64(L + qa + 512);
      qf0 = v4u{a.x, a.y, c.x, c.y};
      const v2u d = lds64(L + qb);
      qf1 = h ? v4u{0u, 0u, 0u, 0u} : v4u{d.x, d.y, 0x00003F80u, 0u};
    }
    lptr kp0 = L + ka, kp1 = L + kb;
    auto kfrag = [&](int blk, v4u (&kf)[2]) {
      const v2u a = lds64(kp0 + blk * 256), d = lds64(kp0 + PL + blk * 256), e = lds64(kp1 + blk * 256);
      kf[0] = v4u{a.x, a.y, d.x, d.y};
      kf[1] = v4u{e.x, e.y, blk >= bmin ? 0u : 0x0000F149u  , 0u};
    };
    auto scores = [&](const v4u (&kf)[2], f32x16 c) -> f32x16 { return mfma32(kf[1], qf1, mfma32(kf[0], qf0, c)); };
    f32x16 zero16;
#pragma unroll
    for (int r = 0; r < 16; ++r) zero16[r] = 0.f;
    lptr vp = L + va;
    f32x16 o = zero16;
    auto vfrag = [&](int blk, v4u (&vf)[2]) {
#pragma unroll
      for (int s2 = 0; s2 < 2; ++s2) {
        const v2u a = ldstr(vp + blk * 256 + s2 * 128), c = ldstr(vp + blk * 256 + s2 * 128 + 64);
        vf[s2] = v4u{a.x, a.y, c.x, c.y};
      }
    };
    auto pv = [&](const v4u (&vf)[2], const unsigned (&pk)[8]) {
      o = mfma32(vf[0], v4u{pk[0], pk[1], pk[2], pk[3]}, o);
      o = mfma32(vf[1], v4u{pk[4], pk[5], pk[6], pk[7]}, o);
    };
    auto xmax = [&](float v) {
      const unsigned u = asu(v);
      const auto sw = __builtin_amdgcn_permlane32_swap(u, u, false, false);
      return fmaxf(asf((unsigned)sw[0]), asf((unsigned)sw[1]));
    };
    {
      constexpr float THR = ATT_THR;
      v4u k8[2], k0[2], kn[2][2], v8[2], v0[2];
      kfrag(8, k8); kfrag(0, k0); kfrag(1, kn[1]); kfrag(2, kn[0]);
      vfrag(8, v8); vfrag(0, v0);
      SBAR();
      const f32x16 sa = scores(k8, zero16), sb = scores(k0, zero16);
      SBAR();
      float sm[16];
#pragma unroll
      for (int r = 0; r < 16; ++r) sm[r] = __builtin_amdgcn_inverse_ballot_w64(le_mask(r)) ? sa[r] : sb[r];
      float m = fmaxf(sm[0], sm[1]);
#pragma unroll
      for (int r = 2; r < 16; r += 2) m = fmaxf(fmaxf(m, sm[r]), sm[r + 1]);
      m = xmax(m);
      f32x16 negm;
#pragma unroll
      for (int r = 0; r < 16; ++r) negm[r] = -m;
      SBAR();
      f32x16 sn = scores(kn[1], negm);
      SBAR();
      unsigned p8[8], p0[8];
#pragma unroll
      for (int r = 0; r < 16; r += 2) {
        const unsigned pkd = pk2(__builtin_amdgcn_exp2f(sm[r] - m), __builtin_amdgcn_exp2f(sm[r + 1] - m));
        p8[r >> 1] = pkd & msk[r >> 1]; p0[r >> 1] = pkd & ~msk[r >> 1];
        PIN(p8[r >> 1]); PIN(p0[r >> 1]);
      }
      SBAR();
      pv(v8, p8);
      pv(v0, p0);
#pragma unroll
      for (int blk = 1; blk < 8; ++blk) {
        f32x16 sc = sn;
        v4u vf[2];
        SBAR();
        if (blk < 7) sn = scores(kn[(blk + 1) & 1], negm);
        vfrag(blk, vf);
        if (blk < 6) kfrag(blk + 2, kn[blk & 1]);
        if (blk < 5) pre(blk);
        SBAR();
        float b0 = fmaxf(fmaxf(sc[0], sc[1]), sc[2]), b1 = fmaxf(fmaxf(sc[3], sc[4]), sc[5]);
#pragma unroll
        for (int r = 6; r < 16; r += 4) { b0 = fmaxf(fmaxf(b0, sc[r]), sc[r + 1]); if (r + 3 < 16) b1 = fmaxf(fmaxf(b1, sc[r + 2]), sc[r + 3]); }
        b0 = fmaxf(b0, b1);
        if (__builtin_expect(__any(b0 > THR), 0)) {
          const float d = fmaxf(xmax(b0), 0.f), f = __builtin_amdgcn_exp2f(-d);
#pragma unroll
          for (int r = 0; r < 16; ++r) { sc[r] -= d; sn[r] -= d; negm[r] -= d; o[r] *= f; }
        }
        SBAR();
        unsigned pk[8];
#pragma unroll
        for (int r = 0; r < 16; r += 2) { pk[r >> 1] = pk2(__builtin_amdgcn_exp2f(sc[r]), __builtin_amdgcn_exp2f(sc[r + 1])); PIN(pk[r >> 1]); }
        SBAR();
        pv(vf, pk);
      }
    }
    {
      const unsigned du = asu(o[8]);
      const auto dsw = __builtin_amdgcn_permlane32_swap(du, du, false, false);
      const float inv = __builtin_amdgcn_rcpf(asf((unsigned)dsw[1]));
#pragma unroll
      for (int c = 0; c < 3; ++c) gst64(rs, oadr[c], v2u{pk2(o[4 * c] * inv, o[4 * c + 1] * inv), pk2(o[4 * c + 2] * inv, o[4 * c + 3] * inv)});
    }
    publish(1, 8u, WS_F2 + (pn * 8 + j) * FL_STRIDE);
    LBAR();
  }
  const int pn = pg, j = jg;

  auto red_write = [&](int lane, int hp, const f32x4 (&acc)[2][5]) {
#pragma unroll
    for (int ml = 0; ml < 2; ++ml) {
      LAS char* d = (LAS char*)smem + ((2 * hp + ml) * 8 + w) * 2560;
#pragma unroll
      for (int q = 0; q < 2; ++q) {
        const f32x4 a = acc[ml][2 * q], c = acc[ml][2 * q + 1];
        *(LAS v4u*)(d + q * 1024 + lane * 16) = v4u{pk2(a[0], a[1]), pk2(a[2], a[3]), pk2(c[0], c[1]), pk2(c[2], c[3])};
      }
      const f32x4 a = acc[ml][4];
      *(LAS v2u*)(d + 2048 + lane * 8) = v2u{pk2(a[0], a[1]), pk2(a[2], a[3])};
    }
  };
  auto red_sum = [&](int lane, f32x4& s0, f32x4& s1, f32x4& s2) {
    const int n = lane & 15, gg = lane >> 4;
    const unsigned one = (n >> 2) == gg ? ((n & 1) ? 0x3F800000u : 0x00003F80u) : 0u;
    const unsigned e01 = (n & 2) ? 0u : one, e23 = (n & 2) ? one : 0u;
    const v4u selA = {e01, e23, 0u, 0u}, selB = {0u, 0u, e01, e23};
    v4u r[8]; v2u r2[8];
#pragma unroll
    for (int ww = 0; ww < 8; ++ww) {
      r[ww] = lds128(L + ((w >> 1) * 8 + ww) * 2560 + (w & 1) * 1024 + lane * 16);
      r2[ww] = lds64(L + ((w & 3) * 8 + ww) * 2560 + 2048 + lane * 8);
    }
    SBAR();
#pragma unroll
    for (int ww = 0; ww < 8; ++ww) { s0 = mfma16(selA, r[ww], s0); s1 = mfma16(selB, r[ww], s1); }
    const v4u selC = {e01, e23, e01, e23};
#pragma unroll
    for (int ww = 0; ww < 8; ww += 2) s2 = mfma16(selC, v4u{r2[ww].x, r2[ww].y, r2[ww + 1].x, r2[ww + 1].y}, s2);
    SBAR();
  };
  {
#pragma unroll
    for (int ks = 0; ks < W1E; ++ks) { w1f[ks][0] = gld128(rs, w1base + ks * 1024); w1f[ks][1] = gld128(rs, w1base + (20 + ks) * 1024); }
    SBAR();
    poll_flags(p.ws, WS_F2 + (pn * 8 + w) * FL_STRIDE, lane == 0, tag);
    const int obase = WS_O + (pn * 8 + w) * 10240;
    v4u ba[2][2][3];
    auto oload = [&](int hp) {
#pragma unroll
      for (int ml = 0; ml < 2; ++ml) {
        ba[hp][ml][0] = gld128(rs, obase + (2 * hp + ml) * 2560 + lane * 16);
        ba[hp][ml][1] = gld128(rs, obase + (2 * hp + ml) * 2560 + 1024 + lane * 16);
        const v2u c = gld64(rs, obase + (2 * hp + ml) * 2560 + 2048 + lane * 8);
        ba[hp][ml][2] = v4u{c.x, c.y, 0u, 0u};
      }
      SBAR();
    };
    oload(0); oload(1);
#pragma unroll
    for (int hp = 0; hp < 2; ++hp) {
      f32x4 acc[2][5];
#pragma unroll
      for (int ml = 0; ml < 2; ++ml)
#pragma unroll
        for (int nt = 0; nt < 5; ++nt) acc[ml][nt] = mfma16(wo[nt][0], ba[hp][ml][0], f32x4{0, 0, 0, 0});
#pragma unroll
      for (int k = 1; k < 3; ++k)
#pragma unroll
        for (int ml = 0; ml < 2; ++ml)
#pragma unroll
          for (int nt = 0; nt < 5; ++nt) acc[ml][nt] = mfma16(wo[nt][k], ba[hp][ml][k], acc[ml][nt]);
      red_write(lane, hp, acc);
      SBAR();
    }
    LBAR();
    {
      f32x4 s0 = {bflo(xr[0].x), bfhi(xr[0].x), bflo(xr[0].y), bfhi(xr[0].y)};
      f32x4 s1 = {bflo(xr[1].x), bfhi(xr[1].x), bflo(xr[1].y), bfhi(xr[1].y)};
      f32x4 s2 = {bflo(xr[2].x), bfhi(xr[2].x), bflo(xr[2].y), bfhi(xr[2].y)};
      red_sum(lane, s0, s1, s2);
      const v4u hres = v4u{pk2(s0[0], s0[1]), pk2(s0[2], s0[3]), pk2(s1[0], s1[1]), pk2(s1[2], s1[3])};
      const v4u hres2 = v4u{pk2(s2[0], s2[1]), pk2(s2[2], s2[3]), 0u, 0u};
      const int base = WS_H + (pn * 8 + j) * HB;
      gst128(rs, base + (w >> 1) * 2560 + (w & 1) * 1024 + lane * 16, hres);
      gst64(rs, w < 4 ? base + w * 2560 + 2048 + lane * 8 : 0x7F000000, v2u{hres2.x, hres2.y});
      const f32x4 z4 = {0, 0, 0, 0};
      const f32x4 su = mfma16(ONES, hres, z4), sq = mfma16(hres, hres, z4), tu = mfma16(ONES, hres2, z4), tq = mfma16(hres2, hres2, z4);
      const int r = tok & 3;
      const float q1 = r == 0 ? sq[0] : r == 1 ? sq[1] : r == 2 ? sq[2] : sq[3], q2 = r == 0 ? tq[0] : r == 1 ? tq[1] : r == 2 ? tq[2] : tq[3];
      const bool diag = g == (tok >> 2);
      gst64(rs, diag ? base + 10240 + (((w >> 1) * 3 + (w & 1)) * 16 + tok) * 8 : 0x7F000000, v2u{asu(su[0]), asu(q1)});
      gst64(rs, diag && w < 4 ? base + 10240 + ((w * 3 + 2) * 16 + tok) * 8 : 0x7F000000, v2u{asu(tu[0]), asu(q2)});
    }
    publish(2, 8u, WS_F3 + (pn * 8 + j) * FL_STRIDE);
    LBAR();
#pragma unroll
    for (int ks = W1E; ks < W1M; ++ks) { w1f[ks][0] = gld128(rs, w1base + ks * 1024); w1f[ks][1] = gld128(rs, w1base + (20 + ks) * 1024); }
    SBAR();
  }

  v4u w2f[35];
  auto w2off = [&](int f) { return w2base + f * 1024; };
  {
    constexpr int HL = 11776;
    LAS f32x2* st = (LAS f32x2*)(smem + 8 * HL);
    f32x4 acc[4][2], accC[2];
    poll_flags(p.ws, WS_F3 + (pn * 8 + w) * FL_STRIDE, lane == 0, tag);
    {
      const char* src = p.ws + (size_t)(WS_H + (pn * 8 + w) * HB + lane * 16);
#pragma unroll
      for (int i = 0; i < 11; ++i) glds16_sc1(src + i * 1024, lds0 + w * HL + i * 1024);
      if (lane < 32) glds16_sc1(src + 11 * 1024, lds0 + w * HL + 11 * 1024);
    }
    accC[0] = accC[1] = f32x4{0, 0, 0, 0};
#pragma unroll
    for (int ks = 0; ks < W1M; ++ks) { accC[0] = mfma16(w1f[ks][0], ONES, accC[0]); accC[1] = mfma16(w1f[ks][1], ONES, accC[1]); }
    SBAR();
    WAIT_BAR(0);
    auto bfrag = [&](int ks, v4u (&bf)[4]) {
#pragma unroll
      for (int mt = 0; mt < 4; ++mt) {
        if (ks < 16) bf[mt] = lds128(L + (ks >> 1) * HL + mt * 2560 + (ks & 1) * 1024 + lane * 16);
        else {
          const v2u a = lds64(L + (2 * (ks - 16)) * HL + mt * 2560 + 2048 + lane * 8), c = lds64(L + (2 * (ks - 16) + 1) * HL + mt * 2560 + 2048 + lane * 8);
          bf[mt] = v4u{a.x, a.y, c.x, c.y};
        }
      }
      SBAR();
    };
    if (w == 7) {
      float su = 0.f, sq = 0.f;
#pragma unroll
      for (int q = 0; q < 8; ++q)
#pragma unroll
        for (int part = 0; part < 3; ++part) {
          const f32x2 v = *(const LAS f32x2*)(smem + q * HL + 10240 + ((g * 3 + part) * 16 + tok) * 8);
          su += v.x; sq += v.y;
        }
      const float mean = su * (1.0f / DM);
      const float var = fmaxf(sq * (1.0f / DM) - mean * mean, 0.f);
      st[lane] = f32x2{mean, 1.0f / sqrtf(var + LN_EPS)};
    }
#pragma unroll
    for (int i = 0; i < 4; ++i) { acc[i][0] = f32x4{0, 0, 0, 0}; acc[i][1] = f32x4{0, 0, 0, 0}; }
    v4u bf[2][4];
    bfrag(0, bf[0]);
#pragma unroll
    for (int ks = 0; ks < 20; ++ks) {
      if (ks + 1 < 20) bfrag(ks + 1, bf[(ks + 1) & 1]);
#pragma unroll
      for (int mt = 0; mt < 4; ++mt) {
        acc[mt][0] = mfma16(w1f[ks][0], bf[ks & 1][mt], acc[mt][0]);
        acc[mt][1] = mfma16(w1f[ks][1], bf[ks & 1][mt], acc[mt][1]);
      }
      if (ks >= W1M) { accC[0] = mfma16(w1f[ks][0], ONES, accC[0]); accC[1] = mfma16(w1f[ks][1], ONES, accC[1]); }
      SBAR();
      if (ks < 20 - W1M) { w1f[W1M + ks][0] = gld128(rs, w1base + (W1M + ks) * 1024); w1f[W1M + ks][1] = gld128(rs, w1base + (20 + W1M + ks) * 1024); SBAR(); }
      if (ks >= 6 && ks < 6 + W2L) glds16_sc1(p.ws + (size_t)(w2off(ks - 6) + lane * 16), lds0 + W2LOFF + (w * W2L + ks - 6) * 1024);
      if (ks >= 6 && ks < 19) {
#pragma unroll
        for (int e = 0; e < (ks >= 13 ? 2 : 1); ++e) { const int f = ks < 13 ? W2L + (ks - 6) : W2L + 7 + 2 * (ks - 13) + e; w2f[f] = gld128(rs, w2off(f) + lane * 16); }
        SBAR();
      }
    }
    LBAR();
    {
#pragma unroll
      for (int mt = 0; mt < 4; ++mt) {
        const f32x2 ms = st[mt * 16 + tok];
        float v[8];
#pragma unroll
        for (int e = 0; e < 2; ++e)
#pragma unroll
          for (int r = 0; r < 4; ++r) v[4 * e + r] = acc[mt][e][r] - ms.x * accC[e][r];
        gst128(rs, gemm ? WS_H1 + (((pn * 8 + j) * 7 + w) * 4 + mt) * 1024 + lane * 16 : 0x7F000000, v4u{relu2(pk2(v[0], v[1])), relu2(pk2(v[2], v[3])), relu2(pk2(v[4], v[5])), relu2(pk2(v[6], v[7]))});
      }
    }
    publish(3, 8u, WS_F4 + (pn * 8 + j) * FL_STRIDE);
    LBAR();
  }

  {
    const bool full = w < 6;
    int lz = lane; asm volatile("" : "+v"(lz));
    v4u hres; v2u hres2;
#pragma unroll
    for (int f = 26; f < 35; ++f) w2f[f] = gld128(rs, (f < 30 || full ? w2off(f) : 0x7F000000) + lane * 16);
    SBAR();
    poll_flags(p.ws, WS_F4 + (pn * 8 + w) * FL_STRIDE, lane == 0, tag);
    const int bbase = WS_H1 + (pn * 8 + w) * 28672 + lane * 16;
    v4u bf[2][7][2];
#pragma unroll
    for (int q = 0; q < 7; ++q) {
      const int ks = (q + 2) % 7;
#pragma unroll
      for (int ml = 0; ml < 2; ++ml) bf[0][ks][ml] = gld128(rs, bbase + ((ks < 6 || full ? ks : 0) * 4 + ml) * 1024);
    }
    SBAR();
#pragma unroll
    for (int hp = 0; hp < 2; ++hp) {
      f32x4 acc[2][5];
#pragma unroll
      for (int ml = 0; ml < 2; ++ml)
#pragma unroll
        for (int nt = 0; nt < 5; ++nt) acc[ml][nt] = f32x4{0, 0, 0, 0};
#pragma unroll
      for (int q = 0; q < 7; ++q) {
        const int ks = (q + 2) % 7;
        if (q == 4 || q == 5) {
#pragma unroll
          for (int f = 5 * (q - 4); f < 5 * (q - 3); ++f) if (f < W2L) w2f[f] = lds128(L + W2LOFF + (w * W2L + f) * 1024 + lane * 16);
          SBAR();
        }
#pragma unroll
        for (int ml = 0; ml < 2; ++ml)
#pragma unroll
          for (int nt = 0; nt < 5; ++nt) acc[ml][nt] = mfma16(w2f[ks * 5 + nt], bf[hp][ks][ml], acc[ml][nt]);
        SBAR();
        if (hp == 0) {
#pragma unroll
          for (int ml = 0; ml < 2; ++ml) bf[1][ks][ml] = gld128(rs, bbase + ((ks < 6 || full ? ks : 0) * 4 + 2 + ml) * 1024);
          SBAR();
        }
      }
      red_write(lz, hp, acc);
      SBAR();
      if (hp == 0) {
        asm volatile("" ::: "memory");
        const int base = WS_H + (pn * 8 + j) * HB;
        hres = gld128(rs, base + (w >> 1) * 2560 + (w & 1) * 1024 + lz * 16);
        hres2 = gld64(rs, base + (w & 3) * 2560 + 2048 + lz * 8);
        SBAR();
      }
    }
    LBAR();
    {
      f32x4 s0 = {0, 0, 0, 0}, s1 = s0, s2 = s0;
      red_sum(lz, s0, s1, s2);
      const LAS f32x2* st = (const LAS f32x2*)(smem + 8 * 11776);
      const float r1 = st[16 * (w >> 1) + (lz & 15)].y, r2 = st[16 * (w & 3) + (lz & 15)].y;
      char* dst = p.out + (size_t)((64 * pn + 16 * (w >> 1) + (lz & 15)) * 1280 + (80 * j + 32 * (w & 1) + 4 * (lz >> 4)) * 2);
      *(GAS v2u*)dst = v2u{pk2(fmaf(s0[0], r1, bflo(hres.x)), fmaf(s0[1], r1, bfhi(hres.x))), pk2(fmaf(s0[2], r1, bflo(hres.y)), fmaf(s0[3], r1, bfhi(hres.y)))};
      *(GAS v2u*)(dst + 32) = v2u{pk2(fmaf(s1[0], r1, bflo(hres.z)), fmaf(s1[1], r1, bfhi(hres.z))), pk2(fmaf(s1[2], r1, bflo(hres.w)), fmaf(s1[3], r1, bfhi(hres.w)))};
      if (w < 4) *(GAS v2u*)(p.out + (size_t)((64 * pn + 16 * w + (lz & 15)) * 1280 + (80 * j + 64 + 4 * (lz >> 4)) * 2)) =
          v2u{pk2(fmaf(s2[0], r2, bflo(hres2.x)), fmaf(s2[1], r2, bfhi(hres2.x))), pk2(fmaf(s2[2], r2, bflo(hres2.y)), fmaf(s2[3], r2, bfhi(hres2.y)))};
    }
  }
  if (b == 0 && tid == 0) set_flag(p.ws, WS_EPOCH, tag);
  asm volatile("mk_code_end:");
}
}

extern "C" void kernel_launch(void* const* d_in, const int* in_sizes, int n_in,
                              void* d_out, int out_size, void* d_ws, size_t ws_size,
                              hipStream_t stream) {
  (void)in_sizes; (void)n_in; (void)out_size; (void)ws_size;
  Params p;
  p.x = (const char*)d_in[0]; p.wqkv = (const char*)d_in[1]; p.wo = (const char*)d_in[2];
  p.w1 = (const char*)d_in[4]; p.w2 = (const char*)d_in[5];
  p.out = (char*)d_out; p.ws = (char*)d_ws;
  (void)hipFuncSetAttribute((const void*)block_kernel, hipFuncAttributeMaxDynamicSharedMemorySize, LDS_BYTES);
  block_kernel<<<256, 512, LDS_BYTES, stream>>>(p);
}
```

```cpp
#define STAMPS 1
#include <hip/hip_runtime.h>
#include <hip/hip_bf16.h>

#define GAS __attribute__((address_space(1)))
#define LAS __attribute__((address_space(3)))
#define DI __device__ __forceinline__
typedef unsigned v4u __attribute__((ext_vector_type(4)));
typedef unsigned v2u __attribute__((ext_vector_type(2)));
typedef float f32x2 __attribute__((ext_vector_type(2)));
typedef float f32x4 __attribute__((ext_vector_type(4)));
typedef float f32x16 __attribute__((ext_vector_type(16)));
typedef short bf16x8 __attribute__((ext_vector_type(8)));
typedef short bf16x4 __attribute__((ext_vector_type(4)));
typedef __bf16 bfx2 __attribute__((ext_vector_type(2)));
typedef unsigned long long u64;
typedef GAS u64 gu64;
#define RLX_AGENT __ATOMIC_RELAXED, __HIP_MEMORY_SCOPE_AGENT

namespace {
constexpr int SEQ = 2048, DM = 640, NQKV = 1920, DFF = 1728;
constexpr float LN_EPS = 1e-5f;
constexpr float QSCALE = 0.22360679774997896f * 1.4426950408889634f;

constexpr int FL_STRIDE = 128;
constexpr int WS_EPOCH = 0, WS_TMO = 256, WS_ARR = 1024;
constexpr int WS_F1 = 4096;
constexpr int WS_F2 = WS_F1 + 256 * FL_STRIDE;
constexpr int WS_F3 = WS_F2 + 256 * FL_STRIDE;
constexpr int WS_F4 = WS_F3 + 256 * FL_STRIDE;
constexpr int WS_F0 = WS_F4 + 256 * FL_STRIDE;
constexpr int WS_STAMP = WS_F0 + 256 * FL_STRIDE;
constexpr int WS_SP = 512 * 1024;
constexpr int WS_CP = WS_SP + 16 * 10 * 1024;
constexpr int WS_QKV = 1 << 20;
constexpr int WS_O = WS_QKV + 480 * 2048 * 8;
constexpr int HB = 12288;
constexpr int WS_H = WS_O + 32 * 81920;
constexpr int WS_H1 = WS_H + 256 * HB;
constexpr int WS_WOP = WS_H1 + 32 * 8 * 28672;
constexpr int WS_W1P = WS_WOP + 64 * 12800;
constexpr int WS_W2P = WS_W1P + 108 * 20 * 1024;
constexpr int WS_END = WS_W2P + 64 * 35 * 1024;

constexpr int LDS_BYTES = 160 * 1024;
constexpr unsigned SPIN_CAP = 1u << 18;
constexpr int AUX_SC1 = 16;
#define ATT_THR 8.0f

constexpr u64 le_mask(int r) {
  u64 m = 0;
  for (int l = 0; l < 64; ++l) if ((r & 3) + 8 * (r >> 2) + 4 * (l >> 5) <= (l & 31)) m |= 1ull << l;
  return m;
}

struct Params {
  const char* x; const char* wqkv; const char* wo; const char* w1; const char* w2;
  char* out; char* ws;
};

DI f32x4 mfma16(v4u a, v4u b, f32x4 c) {
  return __builtin_amdgcn_mfma_f32_16x16x32_bf16(__builtin_bit_cast(bf16x8, a), __builtin_bit_cast(bf16x8, b), c, 0, 0, 0);
}
DI f32x16 mfma32(v4u a, v4u b, f32x16 c) {
  return __builtin_amdgcn_mfma_f32_32x32x16_bf16(__builtin_bit_cast(bf16x8, a), __builtin_bit_cast(bf16x8, b), c, 0, 0, 0);
}
DI unsigned pk2(float lo, float hi) {
  f32x2 v = {lo, hi};
  return __builtin_bit_cast(unsigned, __builtin_convertvector(v, bfx2));
}
DI float asf(unsigned u) { return __builtin_bit_cast(float, u); }
DI unsigned asu(float f) { return __builtin_bit_cast(unsigned, f); }
typedef short s16x2 __attribute__((ext_vector_type(2)));
DI unsigned relu2(unsigned u) {
  const s16x2 v = __builtin_bit_cast(s16x2, u);
  return __builtin_bit_cast(unsigned, __builtin_elementwise_max(v, s16x2{0, 0}));
}
DI u64 uni64(unsigned lo, unsigned hi) {
  const unsigned l = (unsigned)__builtin_amdgcn_readfirstlane(lo), h = (unsigned)__builtin_amdgcn_readfirstlane(hi);
  return ((u64)h << 32) | (u64)l;
}
DI float bflo(unsigned u) { return __builtin_bit_cast(float, u << 16); }
DI float bfhi(unsigned u) { return __builtin_bit_cast(float, u & 0xffff0000u); }

DI void glds16(const char* g, unsigned lds_base) {
  unsigned sv;
  asm volatile("s_mov_b32 %0, m0\n\ts_mov_b32 m0, %2\n\ts_nop 0\n\tglobal_load_lds_dwordx4 %1, off\n\ts_mov_b32 m0, %0"
               : "=&s"(sv) : "v"(g), "s"(lds_base) : "memory");
}
DI void glds16_sc1(const char* g, unsigned lds_base) {
  unsigned sv;
  asm volatile("s_mov_b32 %0, m0\n\ts_mov_b32 m0, %2\n\ts_nop 0\n\tglobal_load_lds_dwordx4 %1, off sc1\n\ts_mov_b32 m0, %0"
               : "=&s"(sv) : "v"(g), "s"(lds_base) : "memory");
}
DI void touch4(const char* g, unsigned lds_base) {
  unsigned sv;
  asm volatile("s_mov_b32 %0, m0\n\ts_mov_b32 m0, %2\n\ts_nop 0\n\tglobal_load_lds_dword %1, off sc1\n\ts_mov_b32 m0, %0"
               : "=&s"(sv) : "v"(g), "s"(lds_base) : "memory");
}
#define WAITV(N) asm volatile("s_waitcnt vmcnt(" #N ")" ::: "memory")
#define WAIT_BAR(N) asm volatile("s_waitcnt vmcnt(" #N ") lgkmcnt(0)\n\ts_barrier" ::: "memory")
#define LBAR() asm volatile("s_waitcnt lgkmcnt(0)\n\ts_barrier" ::: "memory")
#define SBAR() __builtin_amdgcn_sched_barrier(0)
#define PIN(x) asm volatile("" : "+v"(x))

typedef LAS const char* lptr;
DI v4u lds128(lptr p) { return *(const LAS v4u*)p; }
DI v2u lds64(lptr p) { return *(const volatile LAS v2u*)p; }
DI v2u ldstr(lptr p) { return __builtin_bit_cast(v2u, __builtin_amdgcn_ds_read_tr16_b64_v4i16((LAS bf16x4*)p)); }

DI v4u gld128(__amdgpu_buffer_rsrc_t rs, int off) { return __builtin_amdgcn_raw_buffer_load_b128(rs, off, 0, AUX_SC1); }
DI v2u gld64(__amdgpu_buffer_rsrc_t rs, int off) { return __builtin_amdgcn_raw_buffer_load_b64(rs, off, 0, AUX_SC1); }
DI void gst128(__amdgpu_buffer_rsrc_t rs, int off, v4u v) { __builtin_amdgcn_raw_buffer_store_b128(v, rs, off, 0, AUX_SC1); }
DI void gst64(__amdgpu_buffer_rsrc_t rs, int off, v2u v) { __builtin_amdgcn_raw_buffer_store_b64(v, rs, off, 0, AUX_SC1); }

DI void poll_flags(char* ws, int off, bool want, u64 tag) {
  gu64* f = (gu64*)(ws + off);
  for (unsigned spins = 0;;) {
    u64 v = tag;
    if (want) v = __hip_atomic_load(f, RLX_AGENT);
    if (__all(v == tag)) break;
    if (++spins > SPIN_CAP) { __hip_atomic_store((gu64*)(ws + WS_TMO), tag, RLX_AGENT); break; }
    if ((spins & 1023u) == 0u && __builtin_amdgcn_readfirstlane((unsigned)(__hip_atomic_load((gu64*)(ws + WS_TMO), RLX_AGENT) == tag))) break;
  }
  __builtin_amdgcn_fence(__ATOMIC_ACQUIRE, "wavefront");
}
DI void poll_arr(char* ws, u64 tag, int lane) {
  gu64* f = (gu64*)(ws + WS_ARR) + lane;
  for (unsigned spins = 0;;) {
    const u64 a = __hip_atomic_load(f, RLX_AGENT), b = __hip_atomic_load(f + 64, RLX_AGENT), c = __hip_atomic_load(f + 128, RLX_AGENT), d = __hip_atomic_load(f + 192, RLX_AGENT);
    if (__all(a == tag && b == tag && c == tag && d == tag)) break;
    if (++spins > SPIN_CAP) { __hip_atomic_store((gu64*)(ws + WS_TMO), tag, RLX_AGENT); break; }
    if ((spins & 1023u) == 0u && __builtin_amdgcn_readfirstlane((unsigned)(__hip_atomic_load((gu64*)(ws + WS_TMO), RLX_AGENT) == tag))) break;
  }
  __builtin_amdgcn_fence(__ATOMIC_ACQUIRE, "wavefront");
}
DI void set_flag(char* ws, int off, u64 tag) { __hip_atomic_store((gu64*)(ws + off), tag, RLX_AGENT); }

#define STAMP(i) do { if ((threadIdx.x & 255) == 0) { ((LAS u64*)(smem + LDS_BYTES - 1536))[(threadIdx.x ? 32 : 0) + (i)] = __builtin_amdgcn_s_memrealtime(); if (threadIdx.x == 0) ((LAS u64*)(smem + LDS_BYTES - 1536))[64 + (i)] = __builtin_amdgcn_s_memtime(); } } while (0)
#define STAMPC(i) do { if (threadIdx.x == 0) ((LAS u64*)(smem + LDS_BYTES - 1536))[64 + (i)] = __builtin_amdgcn_s_memtime(); } while (0)
#define STAMPL(i) do { if (threadIdx.x == 0) ((LAS u64*)(smem + LDS_BYTES - 1536))[96 + (i)] = __builtin_amdgcn_s_memtime(); } while (0)
#define STAMP_DUMP() do { __syncthreads(); if (threadIdx.x < 160) ((gu64*)(p.ws + WS_STAMP))[(blockIdx.x + (threadIdx.x >> 5) * 256) * 32 + (threadIdx.x & 31)] = ((LAS u64*)(smem + LDS_BYTES - 1536))[threadIdx.x]; } while (0)

DI void pack_src(const Params& p, int u, int lane, const char*& s0, const char*& s1, int& pitch) {
  const char* m; int r0, r1, cg;
  if (u < 240) {
    const int ru = u / 10, w = ru / 3, part = ru - 3 * w;
    cg = u - 10 * ru; m = p.wo; pitch = 1280; r0 = 80 * w + 32 * part; r1 = part == 2 ? r0 : r0 + 16;
  } else if (u < 780) {
    const int v = u - 240, ks = v / 27;
    cg = v - 27 * ks; m = p.w1; pitch = 3456;
    if (ks < 16) { r0 = 80 * (ks >> 1) + 32 * (ks & 1); r1 = r0 + 16; } else { r0 = 160 * (ks - 16) + 64; r1 = r0 + 80; }
  } else {
    const int v = u - 780, ks = v / 10;
    cg = v - 10 * ks; m = p.w2; pitch = 1280; r0 = 32 * ks; r1 = r0 + 16;
  }
  const int lr = lane >> 3, c = (lane & 7) ^ (((lr >> 1) & 3) << 1);
  s0 = m + (size_t)((r0 + lr) * pitch + cg * 128 + c * 16);
  s1 = m + (size_t)((r1 + lr) * pitch + cg * 128 + c * 16);
}
DI void pack_issue(const char* s0, const char* s1, int pitch, unsigned lds) {
  glds16(s0, lds); glds16(s0 + 8 * pitch, lds + 1024); glds16(s1, lds + 2048); glds16(s1 + 8 * pitch, lds + 3072);
}

DI void pack_dest(int u, int (&d)[4], bool& half) {
  int T0, base, sj, sn;
  half = false;
  if (u < 240) {
    const int ru = u / 10, w = ru / 3, part = ru - 3 * w;
    T0 = 4 * (u - 10 * ru); base = WS_WOP + w * 12800 + part * 1024; sj = 8 * 12800; sn = 2560; half = part == 2;
  } else if (u < 780) {
    const int v = u - 240, ks = v / 27;
    T0 = 4 * (v - 27 * ks); base = WS_W1P + ks * 1024; sj = 5 * 20480; sn = 20480;
  } else {
    const int v = u - 780, ru = v / 10;
    const int w = ru < 42 ? ru / 7 : 6 + (ru - 42) / 6, ks = ru < 42 ? ru - 7 * w : ru - 42 - 6 * (w - 6);
    T0 = 4 * (v - 10 * ru); base = WS_W2P + w * 35840 + ks * 5120; sj = 8 * 35840; sn = 1024;
  }
#pragma unroll
  for (int t = 0; t < 4; ++t) { const int T = T0 + t, jj = T / 5; d[t] = base + jj * sj + (T - 5 * jj) * sn; }
}
DI void pack_finish(__amdgpu_buffer_rsrc_t rs, lptr L, const int (&d)[4], bool half, int lane) {
  const int row = 4 * (lane >> 4) + ((lane & 15) >> 2);
  const int a0 = row * 128 + (lane & 3) * 8, sw = (row >> 1) & 3;
  v2u lo[4], hi[4];
#pragma unroll
  for (int t = 0; t < 4; ++t) { lo[t] = ldstr(L + a0 + ((t ^ sw) * 32)); hi[t] = ldstr(L + a0 + ((t ^ sw) * 32) + 2048); }
  if (half) {
#pragma unroll
    for (int t = 0; t < 4; ++t) gst64(rs, d[t] + lane * 8, lo[t]);
  } else {
#pragma unroll
    for (int t = 0; t < 4; ++t) gst128(rs, d[t] + lane * 16, v4u{lo[t].x, lo[t].y, hi[t].x, hi[t].y});
  }
}

__global__ void __launch_bounds__(512, 2) block_kernel(Params p) {
  extern __shared__ __attribute__((aligned(16))) char smem[];
  const int tid = threadIdx.x, lane = tid & 63, w = __builtin_amdgcn_readfirstlane(tid >> 6);
  const int tok = lane & 15, g = lane >> 4;
  const int b = blockIdx.x, xcd = b & 7, t = b >> 3;
  const unsigned lds0 = (unsigned)(size_t)smem;
  lptr L = (lptr)smem;
  const __amdgpu_buffer_rsrc_t rs = __builtin_amdgcn_make_buffer_rsrc(p.ws, 0, WS_END, 0x00020000);
  STAMP(0);
  if (lane == 0) ((LAS unsigned char*)(smem + LDS_BYTES - 1536 + 30 * 8))[w] = (unsigned char)((__builtin_amdgcn_s_getreg(4 | (31 << 11)) >> 4) & 3);
  {
    unsigned len;
    asm volatile("mk_code_begin:\n\ts_mov_b32 %0, mk_code_end-mk_code_begin" : "=s"(len));
    const char* pc = (const char*)__builtin_amdgcn_s_getpc();
    if (w < 4) for (unsigned off = (unsigned)tid * 128u; off < len; off += 256u * 128u) touch4(pc + off, lds0 + LDS_BYTES - 256);
  }
  auto read_tag = [&]() -> u64 {
    const u64 e0 = __hip_atomic_load((gu64*)(p.ws + WS_EPOCH), RLX_AGENT);
    return uni64((unsigned)e0, (unsigned)(e0 >> 32)) + 0x9E3779B97F4A7C15ull;
  };
  const v4u ONES = {0x3F803F80u, 0x3F803F80u, 0x3F803F80u, 0x3F803F80u};
  u64 tag;
  LAS unsigned* cnt = (LAS unsigned*)(smem + LDS_BYTES - 1792);
  if (tid < 8) cnt[tid] = 0u;
  auto publish = [&](int which, unsigned n, int flag_off) {
    WAITV(0);
    unsigned old = 0;
    if (lane == 0) old = __hip_atomic_fetch_add(cnt + which, 1u, __ATOMIC_RELAXED, __HIP_MEMORY_SCOPE_WORKGROUP);
    if (lane == 0 && old == n - 1u) set_flag(p.ws, flag_off, tag);
  };

  auto poll_early = [&]() { poll_flags(p.ws, WS_ARR + ((28 + (lane & 3)) * 8 + 4 + ((lane >> 2) & 3)) * 8, lane < 16, tag); };

  constexpr int PL = 2592, QOFF = 40 * PL, ONESOFF = QOFF + 20 * 512;
  const int hl = w >> 1, qh = w & 1, r32 = lane & 31, h = lane >> 5;
  const int bmin = 8 - 2 * t - qh;
  int qa, qb, ka, kb, va, kvsrc, oadr[3];
  unsigned msk[8];
  auto att_consts = [&]() {
    qa = QOFF + (5 * hl + 2 * h) * 512 + (32 * qh + r32) * 8; qb = QOFF + (5 * hl + 4) * 512 + (32 * qh + r32) * 8;
    ka = (5 * hl + 2 * h) * PL + (32 * qh + r32) * 8; kb = (5 * hl + 4) * PL + (32 * qh + r32) * 8;
    const int blk16 = (lane >> 4) & 1, q4 = (lane & 15) >> 2, pp = lane & 3;
    va = ((blk16 == 0) ? (20 + 5 * hl + pp) * PL : (pp == 0 ? (24 + 5 * hl) * PL : ONESOFF + (((20 + 5 * hl) * 32 + 160) & 255))) + (32 * qh + 4 * h + q4) * 8;
#pragma unroll
    for (int c = 0; c < 3; ++c) {
      const int Qd = 5 * hl + (c < 2 ? 2 * c + h : 4), nt = Qd >> 2, gg = Qd & 3, tk = r32 & 15;
      const int off = nt < 4 ? (nt >> 1) * 1024 + (tk + 16 * gg) * 16 + (nt & 1) * 8 : 2048 + (tk + 16 * gg) * 8;
      oadr[c] = (c < 2 || h == 0) ? WS_O + ((t * 8 + xcd) * 4 + 2 * qh + (r32 >> 4)) * 2560 + off : 0x7F000000;
      PIN(oadr[c]);
    }
#pragma unroll
    for (int r = 0; r < 16; r += 2) {
      const int kk = (r & 3) + 8 * (r >> 2) + 4 * h;
      msk[r >> 1] = kk + 1 <= r32 ? 0xffffffffu : (kk <= r32 ? 0x0000ffffu : 0u);
      PIN(msk[r >> 1]);
    }
    kvsrc = WS_QKV + (((w < 4 ? 160 : 300) + 5 * w + 20 * xcd) * 2048 + 64 * t - 256) * 8 + lane * 16;
    PIN(qa); PIN(qb); PIN(ka); PIN(kb); PIN(va); PIN(kvsrc);
  };

  {
    const int P128 = 4 * (xcd & 3) + (t & 3), s = (xcd >> 2) * 8 + (t >> 2);
    if (s < 15) {
      LAS f32x2* st = (LAS f32x2*)(smem + 131072);
      LAS float* cs = (LAS float*)(smem + 132096);
      const int q4 = (lane & 15) >> 2, pp = lane & 3;
      const int fsw = q4 | ((g & 1) << 2);
      const int xlane = tok * 128 + ((g ^ (tok >> 1)) * 16);
      const int wlane = 16384 + (8 * g + q4) * 256 + pp * 8;
      const int cres = (3 * (s + P128)) % 10;
      const int rot = cres >= 5 ? 5 : 0, jres = cres - rot;
      if (w >= 4) {
        const int a = w - 4, lt = tid - 256;
        const char* xs; const char* wsrc;
        { const int row = lt >> 3, c = (lt & 7) ^ ((row >> 1) & 7); xs = p.x + (size_t)((P128 * 128 + row) * 1280 + c * 16); }
        { const int row = lt >> 4, f = (row & 3) | (((row >> 3) & 1) << 2), c = (lt & 15) ^ (f << 1); wsrc = p.wqkv + (size_t)(row * 3840 + s * 256 + c * 16); }
        auto stage = [&](int i) {
          int kc = i + rot; if (kc >= 10) kc -= 10;
          const unsigned d = lds0 + (i & 3) * 32768 + a * 1024;
          const char* xk = xs + kc * 128; const char* wk = wsrc + (size_t)(kc * (64 * 3840));
#pragma unroll
          for (int q = 0; q < 4; ++q) { glds16(wk + q * (16 * 3840), d + 16384 + q * 4096); glds16(xk + q * (32 * 1280), d + q * 4096); }
        };
        stage(0);
        att_consts();
        stage(1); stage(2);
        WAIT_BAR(16);
        {
          const v2u tv = *(const LAS v2u*)(smem + 132616);
          tag = uni64(tv.x, tv.y);
        }
        stage(3);
        v4u fx[3][2], fw[2][2];
#pragma unroll
        for (int k2 = 0; k2 < 2; ++k2) fx[0][k2] = fx[1][k2] = fx[2][k2] = fw[0][k2] = fw[1][k2] = v4u{0u, 0u, 0u, 0u};
#pragma unroll 1
        for (int i = 0; i < 7; ++i) {
          if (i == jres + 1) {
            const int r = tok & 3;
            f32x4 aC[2], aS[2], aG[2];
#pragma unroll
            for (int e = 0; e < 2; ++e) {
              aC[e] = mfma16(fw[e][1], ONES, mfma16(fw[e][0], ONES, f32x4{0, 0, 0, 0}));
              aS[e] = mfma16(ONES, fx[e][1], mfma16(ONES, fx[e][0], f32x4{0, 0, 0, 0}));
              aG[e] = mfma16(fx[2 * e][1], fx[e][1], mfma16(fx[2 * e][0], fx[e][0], f32x4{0, 0, 0, 0}));
            }
            const bool d1 = g == ((tok >> 2) ^ 1);
            const f32x4 G4 = d1 ? aG[1] : aG[0];
            const float G = r == 0 ? G4[0] : r == 1 ? G4[1] : r == 2 ? G4[2] : G4[3];
            gst64(rs, (s < 10 && (d1 || g == (tok >> 2))) ? WS_SP + ((P128 * 10 + cres) * 128 + (2 * a + (d1 ? 1 : 0)) * 16 + tok) * 8 : 0x7F000000, v2u{asu(d1 ? aS[1][0] : aS[0][0]), asu(G)});
            gst128(rs, (P128 < 10 && tok < 2) ? WS_CP + ((s * 10 + cres) * 128 + (2 * a + tok) * 16 + 4 * g) * 4 : 0x7F000000, __builtin_bit_cast(v4u, tok ? aC[1] : aC[0]));
          }
          if (i == jres) {
            lptr S = L + (i & 3) * 32768;
            const int xl2 = (tok ^ 4) * 128 + ((g ^ ((tok ^ 4) >> 1)) * 16);
#pragma unroll
            for (int k2 = 0; k2 < 2; ++k2) {
#pragma unroll
              for (int e = 0; e < 2; ++e) {
                fx[e][k2] = lds128(S + (2 * a + e) * 2048 + (xlane ^ (k2 * 64)));
                const int wo0 = wlane + (((2 * a + e) ^ fsw) * 32) + k2 * 8192;
                const v2u lo = ldstr(S + wo0), hi = ldstr(S + wo0 + 1024);
                fw[e][k2] = v4u{lo.x, lo.y, hi.x, hi.y};
              }
              fx[2][k2] = lds128(S + (2 * a + 1) * 2048 + (xl2 ^ (k2 * 64)));
            }
          }
          if (i < 6) WAITV(16); else WAITV(0);
          LBAR();
          if (i == (jres < 4 ? jres + 3 : 6) && tid == 256) set_flag(p.ws, WS_F0 + (P128 * 15 + s) * FL_STRIDE, tag);
          if (i + 4 < 10) stage(i + 4);
        }
        if (a < 2) {
          poll_flags(p.ws, WS_F0 + (a == 0 ? P128 * 15 + (lane < 10 ? lane : 0) : (lane < 10 ? lane : 0) * 15 + s) * FL_STRIDE, lane < 10, tag);
          if (a == 0) {
            v4u v[10];
#pragma unroll
            for (int c = 0; c < 10; ++c) v[c] = gld128(rs, WS_SP + ((P128 * 10 + c) * 128 + 2 * lane) * 8);
            float su = 0.f, sq = 0.f, tu = 0.f, tq = 0.f;
#pragma unroll
            for (int c = 0; c < 10; ++c) { su += asf(v[c].x); sq += asf(v[c].y); tu += asf(v[c].z); tq += asf(v[c].w); }
            const float m0 = su * (1.0f / DM), m1 = tu * (1.0f / DM);
            st[2 * lane] = f32x2{m0, 1.0f / sqrtf(fmaxf(sq * (1.0f / DM) - m0 * m0, 0.f) + LN_EPS)};
            st[2 * lane + 1] = f32x2{m1, 1.0f / sqrtf(fmaxf(tq * (1.0f / DM) - m1 * m1, 0.f) + LN_EPS)};
          } else {
            v2u v[10];
#pragma unroll
            for (int c = 0; c < 10; ++c) v[c] = gld64(rs, WS_CP + ((s * 10 + c) * 128 + 2 * lane) * 4);
            float su = 0.f, tu = 0.f;
#pragma unroll
            for (int c = 0; c < 10; ++c) { su += asf(v[c].x); tu += asf(v[c].y); }
            cs[2 * lane] = su; cs[2 * lane + 1] = tu;
          }
          if (a == 1) poll_early();
          LBAR();
        } else {
#pragma unroll 1
          for (int i = 0; i < 2; ++i) {
            const int pu = 375 + P128 * 15 + s + 240 * (2 * (a - 2) + i);
            const char *ps0, *ps1; int ppitch;
            pack_src(p, pu < 1320 ? pu : 0, lane, ps0, ps1, ppitch);
            pack_issue(ps0, ps1, ppitch, lds0 + 133120 + (2 * (a - 2) + i) * 4096);
          }
          LBAR();
          WAITV(0);
#pragma unroll 1
          for (int i = 0; i < 2; ++i) {
            const int pu = 375 + P128 * 15 + s + 240 * (2 * (a - 2) + i);
            if (pu < 1320) { int pd[4]; bool phalf; pack_dest(pu, pd, phalf); pack_finish(rs, L + 133120 + (2 * (a - 2) + i) * 4096, pd, phalf, lane); }
          }
          publish(4, 2u, WS_ARR + b * 8);
        }

      } else {
        const int mh = w >> 1, nh = w & 1;
        int xa[2], wq[4];
        xa[0] = mh * 8192 + xlane; xa[1] = xa[0] ^ 64;
#pragma unroll
        for (int n = 0; n < 4; ++n) wq[n] = wlane + (((4 * nh + n) ^ fsw) * 32);
        f32x4 acc[4][4];
#pragma unroll
        for (int m = 0; m < 4; ++m)
#pragma unroll
          for (int n = 0; n < 4; ++n) acc[m][n] = f32x4{0, 0, 0, 0};
        v4u xb[2][4], wa[2][4];
        auto rdfrag = [&](int k2) {
#pragma unroll
          for (int n = 0; n < 4; ++n) {
            const v2u lo = ldstr(L + wq[n] + k2 * 8192), hi = ldstr(L + wq[n] + k2 * 8192 + 1024);
            wa[k2][n] = v4u{lo.x, lo.y, hi.x, hi.y};
            xb[k2][n] = lds128(L + xa[k2] + n * 2048);
          }
        };
        auto mm = [&](int k2) {
#pragma unroll
          for (int m = 0; m < 4; ++m)
#pragma unroll
            for (int n = 0; n < 4; ++n) acc[m][n] = mfma16(wa[k2][n], xb[k2][m], acc[m][n]);
#pragma unroll
          for (int q = 0; q < 12; ++q) { __builtin_amdgcn_sched_group_barrier(0x008, 1, 0); __builtin_amdgcn_sched_group_barrier(0x100, 1, 0); }
          __builtin_amdgcn_sched_group_barrier(0x008, 4, 0);
          SBAR();
        };
        att_consts();
        if (w == 0) { const u64 tg = read_tag(); if (lane == 0) *(LAS u64*)(smem + 132616) = tg; }
        LBAR();
        {
          const v2u tv = *(const LAS v2u*)(smem + 132616);
          tag = uni64(tv.x, tv.y);
        }
        STAMP(16);
        rdfrag(0);
        SBAR();
#pragma unroll 1
        for (int i = 0; i < 10; ++i) {
          rdfrag(1);
          mm(0);
#pragma unroll
          for (int n = 0; n < 4; ++n) wq[n] = (wq[n] + 32768) & 0x1FFFF;
          xa[0] = (xa[0] + 32768) & 0x1FFFF; xa[1] = (xa[1] + 32768) & 0x1FFFF;
          if (i < 7) LBAR();
          SBAR();
          rdfrag(0);
          mm(1);
        }
        STAMP(1);
        LBAR();
        STAMP(17);
        const float qs = s < 5 ? QSCALE : 1.0f;
        f32x4 c4[4];
#pragma unroll
        for (int n = 0; n < 4; ++n) c4[n] = *(const LAS f32x4*)(cs + (4 * nh + n) * 16 + 4 * g);
        const bool odd = tok & 1;
#pragma unroll
        for (int m = 0; m < 4; ++m) {
          const int mt = 4 * mh + m;
          const f32x2 ms = st[mt * 16 + tok];
          const float sc = ms.y * qs;
#pragma unroll
          for (int n2 = 0; n2 < 2; ++n2) {
            unsigned pk[2][2];
#pragma unroll
            for (int e = 0; e < 2; ++e) {
              const f32x4 a4 = acc[m][2 * n2 + e], cc = c4[2 * n2 + e];
              pk[e][0] = pk2((a4[0] - ms.x * cc[0]) * sc, (a4[1] - ms.x * cc[1]) * sc);
              pk[e][1] = pk2((a4[2] - ms.x * cc[2]) * sc, (a4[3] - ms.x * cc[3]) * sc);
            }
            const unsigned s0 = odd ? pk[0][0] : pk[1][0], s1 = odd ? pk[0][1] : pk[1][1];
            const unsigned r0 = (unsigned)__builtin_amdgcn_mov_dpp((int)s0, 0xB1, 0xF, 0xF, true);
            const unsigned r1 = (unsigned)__builtin_amdgcn_mov_dpp((int)s1, 0xB1, 0xF, 0xF, true);
            const v4u v = odd ? v4u{r0, r1, pk[1][0], pk[1][1]} : v4u{pk[0][0], pk[0][1], r0, r1};
            const int quad = 32 * s + 4 * (4 * nh + 2 * n2 + (odd ? 1 : 0)) + g;
            gst128(rs, WS_QKV + (quad * 2048 + P128 * 128 + mt * 16 + (tok & 14)) * 8, v);
          }
        }
      }
      STAMP(18);
      if (w < 4) publish(0, 4u, WS_F1 + (P128 * 15 + s) * FL_STRIDE);
    } else {
      const int v = ((xcd - 4) * 4 + (t - 28)) * 8 + w;
#pragma unroll 1
      for (int i = 0; i < 3; ++i) {
        const char *ps0, *ps1; int ppitch;
        pack_src(p, v + 128 * i < 375 ? v + 128 * i : 0, lane, ps0, ps1, ppitch);
        pack_issue(ps0, ps1, ppitch, lds0 + (w * 3 + i) * 4096);
      }
      att_consts();
      tag = read_tag();
#pragma unroll 1
      for (int i = 0; i < 3; ++i) {
        const int u = v + 128 * i;
        if (u < 375) { int pd[4]; bool phalf; pack_dest(u, pd, phalf); pack_finish(rs, L + (w * 3 + i) * 4096, pd, phalf, lane); }
      }
      WAIT_BAR(0);
      if (tid == 0) set_flag(p.ws, WS_ARR + b * 8, tag);
      if (w == 6) poll_early();
    }
  }
  STAMP(2);

  const int pg = xcd * 4 + (t & 3), jg = t >> 2;
  v4u wo[5][3];
  v2u xr[3];
  const int ntile = jg < 6 ? 14 : 12, tbase = jg < 7 ? 14 * jg : 96;
  const bool gemm = 2 * w < ntile;
  constexpr int W1E = 8;
  constexpr int W1M = 14;
  constexpr int W2L = 7;
  constexpr int W2LOFF = 8 * 11776 + 1024;
  v4u w1f[20][2];
  const int w1base = gemm ? WS_W1P + (tbase + 2 * w) * 20480 + lane * 16 : 0x7F000000;
  const int w2base = WS_W2P + (jg * 8 + w) * 35840;
  {
    const int pn = t, j = xcd;
    const int tok0 = 64 * pn - 256;
    const int wobase = WS_WOP + (jg * 8 + w) * 12800;
    auto wo_loads = [&](int i) {
      if (i < 5) {
        wo[i][0] = gld128(rs, wobase + i * 2560 + lane * 16);
        wo[i][1] = gld128(rs, wobase + i * 2560 + 1024 + lane * 16);
        wo[i][2] = gld128(rs, wobase + i * 2560 + 2048 + lane * 8);
      } else {
        const char* src = p.x + (size_t)((64 * pg + 16 * (w >> 1) + tok) * 1280 + (80 * jg + 32 * (w & 1) + 4 * g) * 2);
        xr[0] = *(const GAS v2u*)src;
        xr[1] = *(const GAS v2u*)(src + 32);
        xr[2] = *(const GAS v2u*)(p.x + (size_t)((64 * pg + 16 * (w & 3) + tok) * 1280 + (80 * jg + 64 + 4 * g) * 2));
      }
      SBAR();
    };
    auto pre = [&](int i) {
      const int q = t * 8 + w;
      if (q < 216 && lane < 16) touch4(p.ws + (size_t)(WS_W1P + (q >> 1) * 20480 + (32 * i + 16 * (q & 1) + lane) * 128), lds0 + LDS_BYTES - 256);
    };
    auto wo_all = [&]() {
#pragma unroll
      for (int i = 0; i < 6; ++i) wo_loads(i);
    };
    LBAR();
    if (w != 7) wo_all();
    if (w == 7) {
      const int R = lane / 6, rem = lane % 6, pi = rem >> 1;
      const int q0 = 160 * R + 20 * j, sl = (rem & 1) ? (q0 + 19) >> 5 : q0 >> 5;
      const int P = (pn >> 1) - pi;
      const bool want = lane < 18 && P >= 0 && (R > 0 || pi == 0);
      poll_flags(p.ws, WS_F1 + ((want ? P : 0) * 15 + (want ? sl : 0)) * FL_STRIDE, want, tag);
    }
    LBAR();
    STAMP(3);
    {
      const char* src = p.ws + (size_t)kvsrc;
      const unsigned dst = lds0 + 5 * w * PL;
      if (pn >= 4) {
#pragma unroll
        for (int i = 0; i < 5; ++i) { glds16_sc1(src + i * 16384, dst + i * PL); glds16_sc1(src + i * 16384 + 1024, dst + i * PL + 1024); }
      } else {
#pragma unroll 1
        for (int i = 0; i < 10; ++i) if (tok0 + (i & 1) * 128 + lane * 2 >= 0) glds16_sc1(src + (i >> 1) * 16384 + (i & 1) * 1024, dst + (i >> 1) * PL + (i & 1) * 1024);
      }
      if (lane < 32) {
#pragma unroll
        for (int i = 0; i < 5; ++i) glds16_sc1(src + i * 16384 + 2048, dst + i * PL + 2048);
      }
    }
    if (w < 5) {
#pragma unroll
      for (int i = 0; i < 2; ++i) {
        const int pl = 4 * w + 2 * i + (lane >> 5);
        const char* src = p.ws + (size_t)(WS_QKV + ((20 * j + pl) * 2048 + 64 * pn) * 8 + (lane & 31) * 16);
        glds16_sc1(src, lds0 + QOFF + (4 * w + 2 * i) * 512);
      }
    }
    if (tid < 352) *(LAS v2u*)(smem + ONESOFF + tid * 8) = v2u{0x00003F80u, 0u};
    if (pn < 4) {
      const int n16 = -tok0 / 2;
#pragma unroll 1
      for (int i = tid; i < 40 * n16; i += 512) { const int pl = i / n16; *(LAS v4u*)(smem + pl * PL + (i - pl * n16) * 16) = v4u{0u, 0u, 0u, 0u}; }
    }
    if (w == 6) poll_arr(p.ws, tag, lane);
    WAIT_BAR(0);
    STAMP(4);
    if (w == 7) wo_all();
    pre(0);
    v4u qf0, qf1;
    {
      const v2u a = lds64(L + qa), c = lds64(L + qa + 512);
      qf0 = v4u{a.x, a.y, c.x, c.y};
      const v2u d = lds64(L + qb);
      qf1 = h ? v4u{0u, 0u, 0u, 0u} : v4u{d.x, d.y, 0x00003F80u, 0u};
    }
    lptr kp0 = L + ka, kp1 = L + kb;
    auto kfrag = [&](int blk, v4u (&kf)[2]) {
      const v2u a = lds64(kp0 + blk * 256), d = lds64(kp0 + PL + blk * 256), e = lds64(kp1 + blk * 256);
      kf[0] = v4u{a.x, a.y, d.x, d.y};
      kf[1] = v4u{e.x, e.y, blk >= bmin ? 0u : 0x0000F149u  , 0u};
    };
    auto scores = [&](const v4u (&kf)[2], f32x16 c) -> f32x16 { return mfma32(kf[1], qf1, mfma32(kf[0], qf0, c)); };
    f32x16 zero16;
#pragma unroll
    for (int r = 0; r < 16; ++r) zero16[r] = 0.f;
    lptr vp = L + va;
    f32x16 o = zero16;
    auto vfrag = [&](int blk, v4u (&vf)[2]) {
#pragma unroll
      for (int s2 = 0; s2 < 2; ++s2) {
        const v2u a = ldstr(vp + blk * 256 + s2 * 128), c = ldstr(vp + blk * 256 + s2 * 128 + 64);
        vf[s2] = v4u{a.x, a.y, c.x, c.y};
      }
    };
    auto pv = [&](const v4u (&vf)[2], const unsigned (&pk)[8]) {
      o = mfma32(vf[0], v4u{pk[0], pk[1], pk[2], pk[3]}, o);
      o = mfma32(vf[1], v4u{pk[4], pk[5], pk[6], pk[7]}, o);
    };
    auto xmax = [&](float v) {
      const unsigned u = asu(v);
      const auto sw = __builtin_amdgcn_permlane32_swap(u, u, false, false);
      return fmaxf(asf((unsigned)sw[0]), asf((unsigned)sw[1]));
    };
    {
      constexpr float THR = ATT_THR;
      v4u k8[2], k0[2], kn[2][2], v8[2], v0[2];
      kfrag(8, k8); kfrag(0, k0); kfrag(1, kn[1]); kfrag(2, kn[0]);
      vfrag(8, v8); vfrag(0, v0);
      SBAR();
      const f32x16 sa = scores(k8, zero16), sb = scores(k0, zero16);
      SBAR();
      float sm[16];
#pragma unroll
      for (int r = 0; r < 16; ++r) sm[r] = __builtin_amdgcn_inverse_ballot_w64(le_mask(r)) ? sa[r] : sb[r];
      float m = fmaxf(sm[0], sm[1]);
#pragma unroll
      for (int r = 2; r < 16; r += 2) m = fmaxf(fmaxf(m, sm[r]), sm[r + 1]);
      m = xmax(m);
      f32x16 negm;
#pragma unroll
      for (int r = 0; r < 16; ++r) negm[r] = -m;
      SBAR();
      f32x16 sn = scores(kn[1], negm);
      SBAR();
      unsigned p8[8], p0[8];
#pragma unroll
      for (int r = 0; r < 16; r += 2) {
        const unsigned pkd = pk2(__builtin_amdgcn_exp2f(sm[r] - m), __builtin_amdgcn_exp2f(sm[r + 1] - m));
        p8[r >> 1] = pkd & msk[r >> 1]; p0[r >> 1] = pkd & ~msk[r >> 1];
        PIN(p8[r >> 1]); PIN(p0[r >> 1]);
      }
      SBAR();
      pv(v8, p8);
      pv(v0, p0);
      STAMP(13);
#pragma unroll
      for (int blk = 1; blk < 8; ++blk) {
        f32x16 sc = sn;
        v4u vf[2];
        SBAR();
        if (blk < 7) sn = scores(kn[(blk + 1) & 1], negm);
        vfrag(blk, vf);
        if (blk < 6) kfrag(blk + 2, kn[blk & 1]);
        if (blk < 5) pre(blk);
        SBAR();
        float b0 = fmaxf(fmaxf(sc[0], sc[1]), sc[2]), b1 = fmaxf(fmaxf(sc[3], sc[4]), sc[5]);
#pragma unroll
        for (int r = 6; r < 16; r += 4) { b0 = fmaxf(fmaxf(b0, sc[r]), sc[r + 1]); if (r + 3 < 16) b1 = fmaxf(fmaxf(b1, sc[r + 2]), sc[r + 3]); }
        b0 = fmaxf(b0, b1);
        if (__builtin_expect(__any(b0 > THR), 0)) {
          const float d = fmaxf(xmax(b0), 0.f), f = __builtin_amdgcn_exp2f(-d);
#pragma unroll
          for (int r = 0; r < 16; ++r) { sc[r] -= d; sn[r] -= d; negm[r] -= d; o[r] *= f; }
        }
        SBAR();
        unsigned pk[8];
#pragma unroll
        for (int r = 0; r < 16; r += 2) { pk[r >> 1] = pk2(__builtin_amdgcn_exp2f(sc[r]), __builtin_amdgcn_exp2f(sc[r + 1])); PIN(pk[r >> 1]); }
        SBAR();
        pv(vf, pk);
      }
    }
    STAMP(14);
    {
      const unsigned du = asu(o[8]);
      const auto dsw = __builtin_amdgcn_permlane32_swap(du, du, false, false);
      const float inv = __builtin_amdgcn_rcpf(asf((unsigned)dsw[1]));
#pragma unroll
      for (int c = 0; c < 3; ++c) gst64(rs, oadr[c], v2u{pk2(o[4 * c] * inv, o[4 * c + 1] * inv), pk2(o[4 * c + 2] * inv, o[4 * c + 3] * inv)});
    }
    STAMP(19);
    publish(1, 8u, WS_F2 + (pn * 8 + j) * FL_STRIDE);
    LBAR();
  }
  const int pn = pg, j = jg;
  STAMP(5);

  auto red_write = [&](int lane, int hp, const f32x4 (&acc)[2][5]) {
#pragma unroll
    for (int ml = 0; ml < 2; ++ml) {
      LAS char* d = (LAS char*)smem + ((2 * hp + ml) * 8 + w) * 2560;
#pragma unroll
      for (int q = 0; q < 2; ++q) {
        const f32x4 a = acc[ml][2 * q], c = acc[ml][2 * q + 1];
        *(LAS v4u*)(d + q * 1024 + lane * 16) = v4u{pk2(a[0], a[1]), pk2(a[2], a[3]), pk2(c[0], c[1]), pk2(c[2], c[3])};
      }
      const f32x4 a = acc[ml][4];
      *(LAS v2u*)(d + 2048 + lane * 8) = v2u{pk2(a[0], a[1]), pk2(a[2], a[3])};
    }
  };
  auto red_sum = [&](int lane, f32x4& s0, f32x4& s1, f32x4& s2) {
    const int n = lane & 15, gg = lane >> 4;
    const unsigned one = (n >> 2) == gg ? ((n & 1) ? 0x3F800000u : 0x00003F80u) : 0u;
    const unsigned e01 = (n & 2) ? 0u : one, e23 = (n & 2) ? one : 0u;
    const v4u selA = {e01, e23, 0u, 0u}, selB = {0u, 0u, e01, e23};
    v4u r[8]; v2u r2[8];
#pragma unroll
    for (int ww = 0; ww < 8; ++ww) {
      r[ww] = lds128(L + ((w >> 1) * 8 + ww) * 2560 + (w & 1) * 1024 + lane * 16);
      r2[ww] = lds64(L + ((w & 3) * 8 + ww) * 2560 + 2048 + lane * 8);
    }
    SBAR();
#pragma unroll
    for (int ww = 0; ww < 8; ++ww) { s0 = mfma16(selA, r[ww], s0); s1 = mfma16(selB, r[ww], s1); }
    const v4u selC = {e01, e23, e01, e23};
#pragma unroll
    for (int ww = 0; ww < 8; ww += 2) s2 = mfma16(selC, v4u{r2[ww].x, r2[ww].y, r2[ww + 1].x, r2[ww + 1].y}, s2);
    SBAR();
  };
  {
#pragma unroll
    for (int ks = 0; ks < W1E; ++ks) { w1f[ks][0] = gld128(rs, w1base + ks * 1024); w1f[ks][1] = gld128(rs, w1base + (20 + ks) * 1024); }
    SBAR();
    poll_flags(p.ws, WS_F2 + (pn * 8 + w) * FL_STRIDE, lane == 0, tag);
    STAMP(6);
    const int obase = WS_O + (pn * 8 + w) * 10240;
    v4u ba[2][2][3];
    auto oload = [&](int hp) {
#pragma unroll
      for (int ml = 0; ml < 2; ++ml) {
        ba[hp][ml][0] = gld128(rs, obase + (2 * hp + ml) * 2560 + lane * 16);
        ba[hp][ml][1] = gld128(rs, obase + (2 * hp + ml) * 2560 + 1024 + lane * 16);
        const v2u c = gld64(rs, obase + (2 * hp + ml) * 2560 + 2048 + lane * 8);
        ba[hp][ml][2] = v4u{c.x, c.y, 0u, 0u};
      }
      SBAR();
    };
    oload(0); oload(1);
#pragma unroll
    for (int hp = 0; hp < 2; ++hp) {
      f32x4 acc[2][5];
#pragma unroll
      for (int ml = 0; ml < 2; ++ml)
#pragma unroll
        for (int nt = 0; nt < 5; ++nt) acc[ml][nt] = mfma16(wo[nt][0], ba[hp][ml][0], f32x4{0, 0, 0, 0});
#pragma unroll
      for (int k = 1; k < 3; ++k)
#pragma unroll
        for (int ml = 0; ml < 2; ++ml)
#pragma unroll
          for (int nt = 0; nt < 5; ++nt) acc[ml][nt] = mfma16(wo[nt][k], ba[hp][ml][k], acc[ml][nt]);
      red_write(lane, hp, acc);
      SBAR();
    }
    STAMP(27);
    LBAR();
    STAMP(20);
    {
      f32x4 s0 = {bflo(xr[0].x), bfhi(xr[0].x), bflo(xr[0].y), bfhi(xr[0].y)};
      f32x4 s1 = {bflo(xr[1].x), bfhi(xr[1].x), bflo(xr[1].y), bfhi(xr[1].y)};
      f32x4 s2 = {bflo(xr[2].x), bfhi(xr[2].x), bflo(xr[2].y), bfhi(xr[2].y)};
      red_sum(lane, s0, s1, s2);
      const v4u hres = v4u{pk2(s0[0], s0[1]), pk2(s0[2], s0[3]), pk2(s1[0], s1[1]), pk2(s1[2], s1[3])};
      const v4u hres2 = v4u{pk2(s2[0], s2[1]), pk2(s2[2], s2[3]), 0u, 0u};
      const int base = WS_H + (pn * 8 + j) * HB;
      gst128(rs, base + (w >> 1) * 2560 + (w & 1) * 1024 + lane * 16, hres);
      gst64(rs, w < 4 ? base + w * 2560 + 2048 + lane * 8 : 0x7F000000, v2u{hres2.x, hres2.y});
      const f32x4 z4 = {0, 0, 0, 0};
      const f32x4 su = mfma16(ONES, hres, z4), sq = mfma16(hres, hres, z4), tu = mfma16(ONES, hres2, z4), tq = mfma16(hres2, hres2, z4);
      const int r = tok & 3;
      const float q1 = r == 0 ? sq[0] : r == 1 ? sq[1] : r == 2 ? sq[2] : sq[3], q2 = r == 0 ? tq[0] : r == 1 ? tq[1] : r == 2 ? tq[2] : tq[3];
      const bool diag = g == (tok >> 2);
      gst64(rs, diag ? base + 10240 + (((w >> 1) * 3 + (w & 1)) * 16 + tok) * 8 : 0x7F000000, v2u{asu(su[0]), asu(q1)});
      gst64(rs, diag && w < 4 ? base + 10240 + ((w * 3 + 2) * 16 + tok) * 8 : 0x7F000000, v2u{asu(tu[0]), asu(q2)});
    }
    STAMP(21);
    publish(2, 8u, WS_F3 + (pn * 8 + j) * FL_STRIDE);
    STAMP(22);
    LBAR();
#pragma unroll
    for (int ks = W1E; ks < W1M; ++ks) { w1f[ks][0] = gld128(rs, w1base + ks * 1024); w1f[ks][1] = gld128(rs, w1base + (20 + ks) * 1024); }
    SBAR();
  }
  STAMP(7);

  v4u w2f[35];
  auto w2off = [&](int f) { return w2base + f * 1024; };
  {
    constexpr int HL = 11776;
    LAS f32x2* st = (LAS f32x2*)(smem + 8 * HL);
    f32x4 acc[4][2], accC[2];
    poll_flags(p.ws, WS_F3 + (pn * 8 + w) * FL_STRIDE, lane == 0, tag);
    STAMP(8);
    {
      const char* src = p.ws + (size_t)(WS_H + (pn * 8 + w) * HB + lane * 16);
#pragma unroll
      for (int i = 0; i < 11; ++i) glds16_sc1(src + i * 1024, lds0 + w * HL + i * 1024);
      if (lane < 32) glds16_sc1(src + 11 * 1024, lds0 + w * HL + 11 * 1024);
    }
    accC[0] = accC[1] = f32x4{0, 0, 0, 0};
#pragma unroll
    for (int ks = 0; ks < W1M; ++ks) { accC[0] = mfma16(w1f[ks][0], ONES, accC[0]); accC[1] = mfma16(w1f[ks][1], ONES, accC[1]); }
    SBAR();
    WAIT_BAR(0);
    STAMP(9);
    auto bfrag = [&](int ks, v4u (&bf)[4]) {
#pragma unroll
      for (int mt = 0; mt < 4; ++mt) {
        if (ks < 16) bf[mt] = lds128(L + (ks >> 1) * HL + mt * 2560 + (ks & 1) * 1024 + lane * 16);
        else {
          const v2u a = lds64(L + (2 * (ks - 16)) * HL + mt * 2560 + 2048 + lane * 8), c = lds64(L + (2 * (ks - 16) + 1) * HL + mt * 2560 + 2048 + lane * 8);
          bf[mt] = v4u{a.x, a.y, c.x, c.y};
        }
      }
      SBAR();
    };
    if (w == 7) {
      float su = 0.f, sq = 0.f;
#pragma unroll
      for (int q = 0; q < 8; ++q)
#pragma unroll
        for (int part = 0; part < 3; ++part) {
          const f32x2 v = *(const LAS f32x2*)(smem + q * HL + 10240 + ((g * 3 + part) * 16 + tok) * 8);
          su += v.x; sq += v.y;
        }
      const float mean = su * (1.0f / DM);
      const float var = fmaxf(sq * (1.0f / DM) - mean * mean, 0.f);
      st[lane] = f32x2{mean, 1.0f / sqrtf(var + LN_EPS)};
    }
#pragma unroll
    for (int i = 0; i < 4; ++i) { acc[i][0] = f32x4{0, 0, 0, 0}; acc[i][1] = f32x4{0, 0, 0, 0}; }
    v4u bf[2][4];
    bfrag(0, bf[0]);
#pragma unroll
    for (int ks = 0; ks < 20; ++ks) {
      if (ks + 1 < 20) bfrag(ks + 1, bf[(ks + 1) & 1]);
#pragma unroll
      for (int mt = 0; mt < 4; ++mt) {
        acc[mt][0] = mfma16(w1f[ks][0], bf[ks & 1][mt], acc[mt][0]);
        acc[mt][1] = mfma16(w1f[ks][1], bf[ks & 1][mt], acc[mt][1]);
      }
      if (ks >= W1M) { accC[0] = mfma16(w1f[ks][0], ONES, accC[0]); accC[1] = mfma16(w1f[ks][1], ONES, accC[1]); }
      SBAR();
      if (ks < 20 - W1M) { w1f[W1M + ks][0] = gld128(rs, w1base + (W1M + ks) * 1024); w1f[W1M + ks][1] = gld128(rs, w1base + (20 + W1M + ks) * 1024); SBAR(); }
      if (ks >= 6 && ks < 6 + W2L) glds16_sc1(p.ws + (size_t)(w2off(ks - 6) + lane * 16), lds0 + W2LOFF + (w * W2L + ks - 6) * 1024);
      if (ks >= 6 && ks < 19) {
#pragma unroll
        for (int e = 0; e < (ks >= 13 ? 2 : 1); ++e) { const int f = ks < 13 ? W2L + (ks - 6) : W2L + 7 + 2 * (ks - 13) + e; w2f[f] = gld128(rs, w2off(f) + lane * 16); }
        SBAR();
      }
    }
    STAMP(28);
    LBAR();
    STAMP(23);
    {
#pragma unroll
      for (int mt = 0; mt < 4; ++mt) {
        const f32x2 ms = st[mt * 16 + tok];
        float v[8];
#pragma unroll
        for (int e = 0; e < 2; ++e)
#pragma unroll
          for (int r = 0; r < 4; ++r) v[4 * e + r] = acc[mt][e][r] - ms.x * accC[e][r];
        gst128(rs, gemm ? WS_H1 + (((pn * 8 + j) * 7 + w) * 4 + mt) * 1024 + lane * 16 : 0x7F000000, v4u{relu2(pk2(v[0], v[1])), relu2(pk2(v[2], v[3])), relu2(pk2(v[4], v[5])), relu2(pk2(v[6], v[7]))});
      }
    }
    STAMP(24);
    publish(3, 8u, WS_F4 + (pn * 8 + j) * FL_STRIDE);
    LBAR();
  }
  STAMP(10);

  {
    const bool full = w < 6;
    int lz = lane; asm volatile("" : "+v"(lz));
    v4u hres; v2u hres2;
#pragma unroll
    for (int f = 26; f < 35; ++f) w2f[f] = gld128(rs, (f < 30 || full ? w2off(f) : 0x7F000000) + lane * 16);
    SBAR();
    poll_flags(p.ws, WS_F4 + (pn * 8 + w) * FL_STRIDE, lane == 0, tag);
    STAMP(11);
    const int bbase = WS_H1 + (pn * 8 + w) * 28672 + lane * 16;
    v4u bf[2][7][2];
#pragma unroll
    for (int q = 0; q < 7; ++q) {
      const int ks = (q + 2) % 7;
#pragma unroll
      for (int ml = 0; ml < 2; ++ml) bf[0][ks][ml] = gld128(rs, bbase + ((ks < 6 || full ? ks : 0) * 4 + ml) * 1024);
    }
    SBAR();
#pragma unroll
    for (int hp = 0; hp < 2; ++hp) {
      f32x4 acc[2][5];
#pragma unroll
      for (int ml = 0; ml < 2; ++ml)
#pragma unroll
        for (int nt = 0; nt < 5; ++nt) acc[ml][nt] = f32x4{0, 0, 0, 0};
#pragma unroll
      for (int q = 0; q < 7; ++q) {
        const int ks = (q + 2) % 7;
        if (q == 4 || q == 5) {
#pragma unroll
          for (int f = 5 * (q - 4); f < 5 * (q - 3); ++f) if (f < W2L) w2f[f] = lds128(L + W2LOFF + (w * W2L + f) * 1024 + lane * 16);
          SBAR();
        }
#pragma unroll
        for (int ml = 0; ml < 2; ++ml)
#pragma unroll
          for (int nt = 0; nt < 5; ++nt) acc[ml][nt] = mfma16(w2f[ks * 5 + nt], bf[hp][ks][ml], acc[ml][nt]);
        SBAR();
        if (hp == 0) {
#pragma unroll
          for (int ml = 0; ml < 2; ++ml) bf[1][ks][ml] = gld128(rs, bbase + ((ks < 6 || full ? ks : 0) * 4 + 2 + ml) * 1024);
          SBAR();
        }
      }
      red_write(lz, hp, acc);
      SBAR();
      if (hp == 0) {
        STAMP(15); asm volatile("" ::: "memory");
        const int base = WS_H + (pn * 8 + j) * HB;
        hres = gld128(rs, base + (w >> 1) * 2560 + (w & 1) * 1024 + lz * 16);
        hres2 = gld64(rs, base + (w & 3) * 2560 + 2048 + lz * 8);
        SBAR();
      }
    }
    STAMP(29);
    LBAR();
    STAMP(25);
    {
      f32x4 s0 = {0, 0, 0, 0}, s1 = s0, s2 = s0;
      red_sum(lz, s0, s1, s2);
      const LAS f32x2* st = (const LAS f32x2*)(smem + 8 * 11776);
      const float r1 = st[16 * (w >> 1) + (lz & 15)].y, r2 = st[16 * (w & 3) + (lz & 15)].y;
      char* dst = p.out + (size_t)((64 * pn + 16 * (w >> 1) + (lz & 15)) * 1280 + (80 * j + 32 * (w & 1) + 4 * (lz >> 4)) * 2);
      *(GAS v2u*)dst = v2u{pk2(fmaf(s0[0], r1, bflo(hres.x)), fmaf(s0[1], r1, bfhi(hres.x))), pk2(fmaf(s0[2], r1, bflo(hres.y)), fmaf(s0[3], r1, bfhi(hres.y)))};
      *(GAS v2u*)(dst + 32) = v2u{pk2(fmaf(s1[0], r1, bflo(hres.z)), fmaf(s1[1], r1, bfhi(hres.z))), pk2(fmaf(s1[2], r1, bflo(hres.w)), fmaf(s1[3], r1, bfhi(hres.w)))};
      if (w < 4) *(GAS v2u*)(p.out + (size_t)((64 * pn + 16 * w + (lz & 15)) * 1280 + (80 * j + 64 + 4 * (lz >> 4)) * 2)) =
          v2u{pk2(fmaf(s2[0], r2, bflo(hres2.x)), fmaf(s2[1], r2, bfhi(hres2.x))), pk2(fmaf(s2[2], r2, bflo(hres2.y)), fmaf(s2[3], r2, bfhi(hres2.y)))};
    }
  }
  STAMP(12);
  STAMP_DUMP();
  if (threadIdx.x == 0 && (blockIdx.x % 37) == 3) {
    LAS u64* sm = (LAS u64*)(smem + LDS_BYTES - 1536);
    const u64 t0 = sm[0];
    printf("WG%d abs0 %llu: 16:%d 1:%d 18:%d 2:%d 3:%d 4:%d 14:%d 5:%d 6:%d 27:%d 22:%d 8:%d 9:%d 28:%d 10:%d 11:%d 29:%d 12:%d clk %d\n", (int)blockIdx.x, t0 % 100000000ull,
      (int)(sm[16]-t0), (int)(sm[1]-t0), (int)(sm[18]-t0), (int)(sm[2]-t0), (int)(sm[3]-t0), (int)(sm[4]-t0), (int)(sm[14]-t0), (int)(sm[5]-t0), (int)(sm[6]-t0), (int)(sm[27]-t0), (int)(sm[22]-t0), (int)(sm[8]-t0), (int)(sm[9]-t0), (int)(sm[28]-t0), (int)(sm[10]-t0), (int)(sm[11]-t0), (int)(sm[29]-t0), (int)(sm[12]-t0), (int)(sm[64+12]-sm[64]));
  }
  if (b == 0 && tid == 0) set_flag(p.ws, WS_EPOCH, tag);
  asm volatile("mk_code_end:");
}
}

extern "C" void kernel_launch(void* const* d_in, const int* in_sizes, int n_in,
                              void* d_out, int out_size, void* d_ws, size_t ws_size,
                              hipStream_t stream) {
  (void)in_sizes; (void)n_in; (void)out_size; (void)ws_size;
  Params p;
  p.x = (const char*)d_in[0]; p.wqkv = (const char*)d_in[1]; p.wo = (const char*)d_in[2];
  p.w1 = (const char*)d_in[4]; p.w2 = (const char*)d_in[5];
  p.out = (char*)d_out; p.ws = (char*)d_ws;
  (void)hipFuncSetAttribute((const void*)block_kernel, hipFuncAttributeMaxDynamicSharedMemorySize, LDS_BYTES);
  block_kernel<<<256, 512, LDS_BYTES, stream>>>(p);
}
```

```cpp
#include <hip/hip_runtime.h>
#include <hip/hip_bf16.h>

#define GAS __attribute__((address_space(1)))
#define LAS __attribute__((address_space(3)))
#define DI __device__ __forceinline__
typedef unsigned v4u __attribute__((ext_vector_type(4)));
typedef unsigned v2u __attribute__((ext_vector_type(2)));
typedef float f32x2 __attribute__((ext_vector_type(2)));
typedef float f32x4 __attribute__((ext_vector_type(4)));
typedef float f32x16 __attribute__((ext_vector_type(16)));
typedef short bf16x8 __attribute__((ext_vector_type(8)));
typedef short bf16x4 __attribute__((ext_vector_type(4)));
typedef __bf16 bfx2 __attribute__((ext_vector_type(2)));
typedef unsigned long long u64;
typedef GAS u64 gu64;
#define RLX_AGENT __ATOMIC_RELAXED, __HIP_MEMORY_SCOPE_AGENT

namespace {
constexpr int SEQ = 2048, DM = 640, NQKV = 1920, DFF = 1728;
constexpr float LN_EPS = 1e-5f;
constexpr float QSCALE = 0.22360679774997896f * 1.4426950408889634f;

constexpr int FL_STRIDE = 128;
constexpr int WS_EPOCH = 0, WS_TMO = 256, WS_ARR = 1024;
constexpr int WS_F1 = 4096;
constexpr int WS_F2 = WS_F1 + 256 * FL_STRIDE;
constexpr int WS_F3 = WS_F2 + 256 * FL_STRIDE;
constexpr int WS_F4 = WS_F3 + 256 * FL_STRIDE;
constexpr int WS_F0 = WS_F4 + 256 * FL_STRIDE;
constexpr int WS_SP = 512 * 1024;
constexpr int WS_CP = WS_SP + 16 * 10 * 1024;
constexpr int WS_QKV = 1 << 20;
constexpr int WS_O = WS_QKV + 480 * 2048 * 8;
constexpr int HB = 12288;
constexpr int WS_H = WS_O + 32 * 81920;
constexpr int WS_H1 = WS_H + 256 * HB;
constexpr int WS_WOP = WS_H1 + 32 * 8 * 28672;
constexpr int WS_W1P = WS_WOP + 64 * 12800;
constexpr int WS_W2P = WS_W1P + 108 * 20 * 1024;
constexpr int WS_END = WS_W2P + 64 * 35 * 1024;

constexpr int LDS_BYTES = 160 * 1024;
constexpr unsigned SPIN_CAP = 1u << 18;
constexpr int AUX_SC1 = 16;
constexpr float ATT_THR = 8.0f;

constexpr u64 le_mask(int r) {
  u64 m = 0;
  for (int l = 0; l < 64; ++l) if ((r & 3) + 8 * (r >> 2) + 4 * (l >> 5) <= (l & 31)) m |= 1ull << l;
  return m;
}

struct Params {
  const char* x; const char* wqkv; const char* wo; const char* w1; const char* w2;
  char* out; char* ws;
};

DI f32x4 mfma16(v4u a, v4u b, f32x4 c) {
  return __builtin_amdgcn_mfma_f32_16x16x32_bf16(__builtin_bit_cast(bf16x8, a), __builtin_bit_cast(bf16x8, b), c, 0, 0, 0);
}
DI f32x16 mfma32(v4u a, v4u b, f32x16 c) {
  return __builtin_amdgcn_mfma_f32_32x32x16_bf16(__builtin_bit_cast(bf16x8, a), __builtin_bit_cast(bf16x8, b), c, 0, 0, 0);
}
DI unsigned pk2(float lo, float hi) {
  f32x2 v = {lo, hi};
  return __builtin_bit_cast(unsigned, __builtin_convertvector(v, bfx2));
}
DI float asf(unsigned u) { return __builtin_bit_cast(float, u); }
DI unsigned asu(float f) { return __builtin_bit_cast(unsigned, f); }
typedef short s16x2 __attribute__((ext_vector_type(2)));
DI unsigned relu2(unsigned u) {
  const s16x2 v = __builtin_bit_cast(s16x2, u);
  return __builtin_bit_cast(unsigned, __builtin_elementwise_max(v, s16x2{0, 0}));
}
DI u64 uni64(unsigned lo, unsigned hi) {
  const unsigned l = (unsigned)__builtin_amdgcn_readfirstlane(lo), h = (unsigned)__builtin_amdgcn_readfirstlane(hi);
  return ((u64)h << 32) | (u64)l;
}
DI float bflo(unsigned u) { return __builtin_bit_cast(float, u << 16); }
DI float bfhi(unsigned u) { return __builtin_bit_cast(float, u & 0xffff0000u); }

DI void glds16(const char* g, unsigned lds_base) {
  unsigned sv;
  asm volatile("s_mov_b32 %0, m0\n\ts_mov_b32 m0, %2\n\ts_nop 0\n\tglobal_load_lds_dwordx4 %1, off\n\ts_mov_b32 m0, %0"
               : "=&s"(sv) : "v"(g), "s"(lds_base) : "memory");
}
DI void glds16_sc1(const char* g, unsigned lds_base) {
  unsigned sv;
  asm volatile("s_mov_b32 %0, m0\n\ts_mov_b32 m0, %2\n\ts_nop 0\n\tglobal_load_lds_dwordx4 %1, off sc1\n\ts_mov_b32 m0, %0"
               : "=&s"(sv) : "v"(g), "s"(lds_base) : "memory");
}
DI void touch4(const char* g, unsigned lds_base) {
  unsigned sv;
  asm volatile("s_mov_b32 %0, m0\n\ts_mov_b32 m0, %2\n\ts_nop 0\n\tglobal_load_lds_dword %1, off sc1\n\ts_mov_b32 m0, %0"
               : "=&s"(sv) : "v"(g), "s"(lds_base) : "memory");
}
#define WAITV(N) asm volatile("s_waitcnt vmcnt(" #N ")" ::: "memory")
#define WAIT_BAR(N) asm volatile("s_waitcnt vmcnt(" #N ") lgkmcnt(0)\n\ts_barrier" ::: "memory")
#define LBAR() asm volatile("s_waitcnt lgkmcnt(0)\n\ts_barrier" ::: "memory")
#define SBAR() __builtin_amdgcn_sched_barrier(0)
#define PIN(x) asm volatile("" : "+v"(x))

typedef LAS const char* lptr;
DI v4u lds128(lptr p) { return *(const LAS v4u*)p; }
DI v2u lds64(lptr p) { return *(const volatile LAS v2u*)p; }
DI v2u ldstr(lptr p) { return __builtin_bit_cast(v2u, __builtin_amdgcn_ds_read_tr16_b64_v4i16((LAS bf16x4*)p)); }

DI v4u gld128(__amdgpu_buffer_rsrc_t rs, int off) { return __builtin_amdgcn_raw_buffer_load_b128(rs, off, 0, AUX_SC1); }
DI v2u gld64(__amdgpu_buffer_rsrc_t rs, int off) { return __builtin_amdgcn_raw_buffer_load_b64(rs, off, 0, AUX_SC1); }
DI void gst128(__amdgpu_buffer_rsrc_t rs, int off, v4u v) { __builtin_amdgcn_raw_buffer_store_b128(v, rs, off, 0, AUX_SC1); }
DI void gst64(__amdgpu_buffer_rsrc_t rs, int off, v2u v) { __builtin_amdgcn_raw_buffer_store_b64(v, rs, off, 0, AUX_SC1); }

DI void poll_flags(char* ws, int off, bool want, u64 tag) {
  gu64* f = (gu64*)(ws + off);
  for (unsigned spins = 0;;) {
    u64 v = tag;
    if (want) v = __hip_atomic_load(f, RLX_AGENT);
    if (__all(v == tag)) break;
    if (++spins > SPIN_CAP) { __hip_atomic_store((gu64*)(ws + WS_TMO), tag, RLX_AGENT); break; }
    if ((spins & 1023u) == 0u && __builtin_amdgcn_readfirstlane((unsigned)(__hip_atomic_load((gu64*)(ws + WS_TMO), RLX_AGENT) == tag))) break;
  }
  __builtin_amdgcn_fence(__ATOMIC_ACQUIRE, "wavefront");
}
DI void poll_arr(char* ws, u64 tag, int lane) {
  gu64* f = (gu64*)(ws + WS_ARR) + lane;
  for (unsigned spins = 0;;) {
    const u64 a = __hip_atomic_load(f, RLX_AGENT), b = __hip_atomic_load(f + 64, RLX_AGENT), c = __hip_atomic_load(f + 128, RLX_AGENT), d = __hip_atomic_load(f + 192, RLX_AGENT);
    if (__all(a == tag && b == tag && c == tag && d == tag)) break;
    if (++spins > SPIN_CAP) { __hip_atomic_store((gu64*)(ws + WS_TMO), tag, RLX_AGENT); break; }
    if ((spins & 1023u) == 0u && __builtin_amdgcn_readfirstlane((unsigned)(__hip_atomic_load((gu64*)(ws + WS_TMO), RLX_AGENT) == tag))) break;
  }
  __builtin_amdgcn_fence(__ATOMIC_ACQUIRE, "wavefront");
}
DI void set_flag(char* ws, int off, u64 tag) { __hip_atomic_store((gu64*)(ws + off), tag, RLX_AGENT); }


DI void pack_src(const Params& p, int u, int lane, const char*& s0, const char*& s1, int& pitch) {
  const char* m; int r0, r1, cg;
  if (u < 240) {
    const int ru = u / 10, w = ru / 3, part = ru - 3 * w;
    cg = u - 10 * ru; m = p.wo; pitch = 1280; r0 = 80 * w + 32 * part; r1 = part == 2 ? r0 : r0 + 16;
  } else if (u < 780) {
    const int v = u - 240, ks = v / 27;
    cg = v - 27 * ks; m = p.w1; pitch = 3456;
    if (ks < 16) { r0 = 80 * (ks >> 1) + 32 * (ks & 1); r1 = r0 + 16; } else { r0 = 160 * (ks - 16) + 64; r1 = r0 + 80; }
  } else {
    const int v = u - 780, ks = v / 10;
    cg = v - 10 * ks; m = p.w2; pitch = 1280; r0 = 32 * ks; r1 = r0 + 16;
  }
  const int lr = lane >> 3, c = (lane & 7) ^ (((lr >> 1) & 3) << 1);
  s0 = m + (size_t)((r0 + lr) * pitch + cg * 128 + c * 16);
  s1 = m + (size_t)((r1 + lr) * pitch + cg * 128 + c * 16);
}
DI void pack_issue(const char* s0, const char* s1, int pitch, unsigned lds) {
  glds16(s0, lds); glds16(s0 + 8 * pitch, lds + 1024); glds16(s1, lds + 2048); glds16(s1 + 8 * pitch, lds + 3072);
}

DI void pack_dest(int u, int (&d)[4], bool& half) {
  int T0, base, sj, sn;
  half = false;
  if (u < 240) {
    const int ru = u / 10, w = ru / 3, part = ru - 3 * w;
    T0 = 4 * (u - 10 * ru); base = WS_WOP + w * 12800 + part * 1024; sj = 8 * 12800; sn = 2560; half = part == 2;
  } else if (u < 780) {
    const int v = u - 240, ks = v / 27;
    T0 = 4 * (v - 27 * ks); base = WS_W1P + ks * 1024; sj = 5 * 20480; sn = 20480;
  } else {
    const int v = u - 780, ru = v / 10;
    const int w = ru < 42 ? ru / 7 : 6 + (ru - 42) / 6, ks = ru < 42 ? ru - 7 * w : ru - 42 - 6 * (w - 6);
    T0 = 4 * (v - 10 * ru); base = WS_W2P + w * 35840 + ks * 5120; sj = 8 * 35840; sn = 1024;
  }
#pragma unroll
  for (int t = 0; t < 4; ++t) { const int T = T0 + t, jj = T / 5; d[t] = base + jj * sj + (T - 5 * jj) * sn; }
}
DI void pack_finish(__amdgpu_buffer_rsrc_t rs, lptr L, const int (&d)[4], bool half, int lane) {
  const int row = 4 * (lane >> 4) + ((lane & 15) >> 2);
  const int a0 = row * 128 + (lane & 3) * 8, sw = (row >> 1) & 3;
  v2u lo[4], hi[4];
#pragma unroll
  for (int t = 0; t < 4; ++t) { lo[t] = ldstr(L + a0 + ((t ^ sw) * 32)); hi[t] = ldstr(L + a0 + ((t ^ sw) * 32) + 2048); }
  if (half) {
#pragma unroll
    for (int t = 0; t < 4; ++t) gst64(rs, d[t] + lane * 8, lo[t]);
  } else {
#pragma unroll
    for (int t = 0; t < 4; ++t) gst128(rs, d[t] + lane * 16, v4u{lo[t].x, lo[t].y, hi[t].x, hi[t].y});
  }
}

__global__ void __launch_bounds__(512, 2) block_kernel(Params p) {
  extern __shared__ __attribute__((aligned(16))) char smem[];
  const int tid = threadIdx.x, lane = tid & 63, w = __builtin_amdgcn_readfirstlane(tid >> 6);
  const int tok = lane & 15, g = lane >> 4;
  const int b = blockIdx.x, xcd = b & 7, t = b >> 3;
  const unsigned lds0 = (unsigned)(size_t)smem;
  lptr L = (lptr)smem;
  const __amdgpu_buffer_rsrc_t rs = __builtin_amdgcn_make_buffer_rsrc(p.ws, 0, WS_END, 0x00020000);
  unsigned code_len;
  asm volatile("mk_code_begin:\n\ts_mov_b32 %0, mk_code_end-mk_code_begin" : "=s"(code_len));
  const char* code_pc = (const char*)__builtin_amdgcn_s_getpc();
  auto code_warm = [&](unsigned idx) { if (idx * 128u < code_len) touch4(code_pc + idx * 128u, lds0 + LDS_BYTES - 256); };
  if (w < 4) { code_warm((unsigned)tid); code_warm((unsigned)tid + 256u); }
  auto read_tag = [&]() -> u64 {
    const u64 e0 = __hip_atomic_load((gu64*)(p.ws + WS_EPOCH), RLX_AGENT);
    return uni64((unsigned)e0, (unsigned)(e0 >> 32)) + 0x9E3779B97F4A7C15ull;
  };
  const v4u ONES = {0x3F803F80u, 0x3F803F80u, 0x3F803F80u, 0x3F803F80u};
  u64 tag;
  LAS unsigned* cnt = (LAS unsigned*)(smem + LDS_BYTES - 1792);
  if (tid < 8) cnt[tid] = 0u;
  auto publish = [&](int which, unsigned n, int flag_off) {
    WAITV(0);
    unsigned old = 0;
    if (lane == 0) old = __hip_atomic_fetch_add(cnt + which, 1u, __ATOMIC_RELAXED, __HIP_MEMORY_SCOPE_WORKGROUP);
    if (lane == 0 && old == n - 1u) set_flag(p.ws, flag_off, tag);
  };

  auto poll_early = [&]() { poll_flags(p.ws, WS_ARR + ((28 + (lane & 3)) * 8 + 4 + ((lane >> 2) & 3)) * 8, lane < 16, tag); };

  constexpr int PL = 2592, QOFF = 40 * PL, ONESOFF = QOFF + 20 * 512;
  const int hl = w >> 1, qh = w & 1, r32 = lane & 31, h = lane >> 5;
  const int bmin = 8 - 2 * t - qh;
  int qa, qb, ka, kb, va, kvsrc, oadr[3];
  unsigned msk[8];
  auto att_consts = [&]() {
    qa = QOFF + (5 * hl + 2 * h) * 512 + (32 * qh + r32) * 8; qb = QOFF + (5 * hl + 4) * 512 + (32 * qh + r32) * 8;
    ka = (5 * hl + 2 * h) * PL + (32 * qh + r32) * 8; kb = (5 * hl + 4) * PL + (32 * qh + r32) * 8;
    const int blk16 = (lane >> 4) & 1, q4 = (lane & 15) >> 2, pp = lane & 3;
    va = ((blk16 == 0) ? (20 + 5 * hl + pp) * PL : (pp == 0 ? (24 + 5 * hl) * PL : ONESOFF + (((20 + 5 * hl) * 32 + 160) & 255))) + (32 * qh + 4 * h + q4) * 8;
#pragma unroll
    for (int c = 0; c < 3; ++c) {
      const int Qd = 5 * hl + (c < 2 ? 2 * c + h : 4), nt = Qd >> 2, gg = Qd & 3, tk = r32 & 15;
      const int off = nt < 4 ? (nt >> 1) * 1024 + (tk + 16 * gg) * 16 + (nt & 1) * 8 : 2048 + (tk + 16 * gg) * 8;
      oadr[c] = (c < 2 || h == 0) ? WS_O + ((t * 8 + xcd) * 4 + 2 * qh + (r32 >> 4)) * 2560 + off : 0x7F000000;
      PIN(oadr[c]);
    }
#pragma unroll
    for (int r = 0; r < 16; r += 2) {
      const int kk = (r & 3) + 8 * (r >> 2) + 4 * h;
      msk[r >> 1] = kk + 1 <= r32 ? 0xffffffffu : (kk <= r32 ? 0x0000ffffu : 0u);
      PIN(msk[r >> 1]);
    }
    kvsrc = WS_QKV + (((w < 4 ? 160 : 300) + 5 * w + 20 * xcd) * 2048 + 64 * t - 256) * 8 + lane * 16;
    PIN(qa); PIN(qb); PIN(ka); PIN(kb); PIN(va); PIN(kvsrc);
  };

  {
    const int P128 = 4 * (xcd & 3) + (t & 3), s = (xcd >> 2) * 8 + (t >> 2);
    if (s < 15) {
      LAS f32x2* st = (LAS f32x2*)(smem + 131072);
      LAS float* cs = (LAS float*)(smem + 132096);
      const int q4 = (lane & 15) >> 2, pp = lane & 3;
      const int fsw = q4 | ((g & 1) << 2);
      const int xlane = tok * 128 + ((g ^ (tok >> 1)) * 16);
      const int wlane = 16384 + (8 * g + q4) * 256 + pp * 8;
      const int cres = (3 * (s + P128)) % 10;
      const int rot = cres >= 5 ? 5 : 0, jres = cres - rot;
      if (w >= 4) {
        const int a = w - 4, lt = tid - 256;
        const char* xs; const char* wsrc;
        { const int row = lt >> 3, c = (lt & 7) ^ ((row >> 1) & 7); xs = p.x + (size_t)((P128 * 128 + row) * 1280 + c * 16); }
        { const int row = lt >> 4, f = (row & 3) | (((row >> 3) & 1) << 2), c = (lt & 15) ^ (f << 1); wsrc = p.wqkv + (size_t)(row * 3840 + s * 256 + c * 16); }
        auto stage = [&](int i) {
          int kc = i + rot; if (kc >= 10) kc -= 10;
          const unsigned d = lds0 + (i & 3) * 32768 + a * 1024;
          const char* xk = xs + kc * 128; const char* wk = wsrc + (size_t)(kc * (64 * 3840));
#pragma unroll
          for (int q = 0; q < 4; ++q) { glds16(wk + q * (16 * 3840), d + 16384 + q * 4096); glds16(xk + q * (32 * 1280), d + q * 4096); }
        };
        stage(0);
        att_consts();
        stage(1); stage(2);
        WAIT_BAR(16);
        {
          const v2u tv = *(const LAS v2u*)(smem + 132616);
          tag = uni64(tv.x, tv.y);
        }
        stage(3);
        v4u fx[3][2], fw[2][2];
#pragma unroll
        for (int k2 = 0; k2 < 2; ++k2) fx[0][k2] = fx[1][k2] = fx[2][k2] = fw[0][k2] = fw[1][k2] = v4u{0u, 0u, 0u, 0u};
#pragma unroll 1
        for (int i = 0; i < 7; ++i) {
          if (i == jres + 1) {
            const int r = tok & 3;
            f32x4 aC[2], aS[2], aG[2];
#pragma unroll
            for (int e = 0; e < 2; ++e) {
              aC[e] = mfma16(fw[e][1], ONES, mfma16(fw[e][0], ONES, f32x4{0, 0, 0, 0}));
              aS[e] = mfma16(ONES, fx[e][1], mfma16(ONES, fx[e][0], f32x4{0, 0, 0, 0}));
              aG[e] = mfma16(fx[2 * e][1], fx[e][1], mfma16(fx[2 * e][0], fx[e][0], f32x4{0, 0, 0, 0}));
            }
            const bool d1 = g == ((tok >> 2) ^ 1);
            const f32x4 G4 = d1 ? aG[1] : aG[0];
            const float G = r == 0 ? G4[0] : r == 1 ? G4[1] : r == 2 ? G4[2] : G4[3];
            gst64(rs, (s < 10 && (d1 || g == (tok >> 2))) ? WS_SP + ((P128 * 10 + cres) * 128 + (2 * a + (d1 ? 1 : 0)) * 16 + tok) * 8 : 0x7F000000, v2u{asu(d1 ? aS[1][0] : aS[0][0]), asu(G)});
            gst128(rs, (P128 < 10 && tok < 2) ? WS_CP + ((s * 10 + cres) * 128 + (2 * a + tok) * 16 + 4 * g) * 4 : 0x7F000000, __builtin_bit_cast(v4u, tok ? aC[1] : aC[0]));
          }
          if (i == jres) {
            lptr S = L + (i & 3) * 32768;
            const int xl2 = (tok ^ 4) * 128 + ((g ^ ((tok ^ 4) >> 1)) * 16);
#pragma unroll
            for (int k2 = 0; k2 < 2; ++k2) {
#pragma unroll
              for (int e = 0; e < 2; ++e) {
                fx[e][k2] = lds128(S + (2 * a + e) * 2048 + (xlane ^ (k2 * 64)));
                const int wo0 = wlane + (((2 * a + e) ^ fsw) * 32) + k2 * 8192;
                const v2u lo = ldstr(S + wo0), hi = ldstr(S + wo0 + 1024);
                fw[e][k2] = v4u{lo.x, lo.y, hi.x, hi.y};
              }
              fx[2][k2] = lds128(S + (2 * a + 1) * 2048 + (xl2 ^ (k2 * 64)));
            }
          }
          if (i < 6) WAITV(16); else WAITV(0);
          LBAR();
          if (i == (jres < 4 ? jres + 3 : 6) && tid == 256) set_flag(p.ws, WS_F0 + (P128 * 15 + s) * FL_STRIDE, tag);
          if (i + 4 < 10) stage(i + 4);
        }
        if (a < 2) {
          poll_flags(p.ws, WS_F0 + (a == 0 ? P128 * 15 + (lane < 10 ? lane : 0) : (lane < 10 ? lane : 0) * 15 + s) * FL_STRIDE, lane < 10, tag);
          if (a == 0) {
            v4u v[10];
#pragma unroll
            for (int c = 0; c < 10; ++c) v[c] = gld128(rs, WS_SP + ((P128 * 10 + c) * 128 + 2 * lane) * 8);
            float su = 0.f, sq = 0.f, tu = 0.f, tq = 0.f;
#pragma unroll
            for (int c = 0; c < 10; ++c) { su += asf(v[c].x); sq += asf(v[c].y); tu += asf(v[c].z); tq += asf(v[c].w); }
            const float m0 = su * (1.0f / DM), m1 = tu * (1.0f / DM);
            st[2 * lane] = f32x2{m0, 1.0f / sqrtf(fmaxf(sq * (1.0f / DM) - m0 * m0, 0.f) + LN_EPS)};
            st[2 * lane + 1] = f32x2{m1, 1.0f / sqrtf(fmaxf(tq * (1.0f / DM) - m1 * m1, 0.f) + LN_EPS)};
          } else {
            v2u v[10];
#pragma unroll
            for (int c = 0; c < 10; ++c) v[c] = gld64(rs, WS_CP + ((s * 10 + c) * 128 + 2 * lane) * 4);
            float su = 0.f, tu = 0.f;
#pragma unroll
            for (int c = 0; c < 10; ++c) { su += asf(v[c].x); tu += asf(v[c].y); }
            cs[2 * lane] = su; cs[2 * lane + 1] = tu;
          }
          if (a == 1) poll_early();
          LBAR();
        } else {
#pragma unroll 1
          for (int i = 0; i < 2; ++i) {
            const int pu = 375 + P128 * 15 + s + 240 * (2 * (a - 2) + i);
            const char *ps0, *ps1; int ppitch;
            pack_src(p, pu < 1320 ? pu : 0, lane, ps0, ps1, ppitch);
            pack_issue(ps0, ps1, ppitch, lds0 + 133120 + (2 * (a - 2) + i) * 4096);
          }
          LBAR();
          WAITV(0);
#pragma unroll 1
          for (int i = 0; i < 2; ++i) {
            const int pu = 375 + P128 * 15 + s + 240 * (2 * (a - 2) + i);
            if (pu < 1320) { int pd[4]; bool phalf; pack_dest(pu, pd, phalf); pack_finish(rs, L + 133120 + (2 * (a - 2) + i) * 4096, pd, phalf, lane); }
          }
          publish(4, 2u, WS_ARR + b * 8);
        }

      } else {
        const int mh = w >> 1, nh = w & 1;
        int xa[2], wq[4];
        xa[0] = mh * 8192 + xlane; xa[1] = xa[0] ^ 64;
#pragma unroll
        for (int n = 0; n < 4; ++n) wq[n] = wlane + (((4 * nh + n) ^ fsw) * 32);
        f32x4 acc[4][4];
#pragma unroll
        for (int m = 0; m < 4; ++m)
#pragma unroll
          for (int n = 0; n < 4; ++n) acc[m][n] = f32x4{0, 0, 0, 0};
        v4u xb[2][4], wa[2][4];
        auto rdfrag = [&](int k2) {
#pragma unroll
          for (int n = 0; n < 4; ++n) {
            const v2u lo = ldstr(L + wq[n] + k2 * 8192), hi = ldstr(L + wq[n] + k2 * 8192 + 1024);
            wa[k2][n] = v4u{lo.x, lo.y, hi.x, hi.y};
            xb[k2][n] = lds128(L + xa[k2] + n * 2048);
          }
        };
        auto mm = [&](int k2) {
#pragma unroll
          for (int m = 0; m < 4; ++m)
#pragma unroll
            for (int n = 0; n < 4; ++n) acc[m][n] = mfma16(wa[k2][n], xb[k2][m], acc[m][n]);
#pragma unroll
          for (int q = 0; q < 12; ++q) { __builtin_amdgcn_sched_group_barrier(0x008, 1, 0); __builtin_amdgcn_sched_group_barrier(0x100, 1, 0); }
          __builtin_amdgcn_sched_group_barrier(0x008, 4, 0);
          SBAR();
        };
        att_consts();
        if (w == 0) { const u64 tg = read_tag(); if (lane == 0) *(LAS u64*)(smem + 132616) = tg; }
        LBAR();
        {
          const v2u tv = *(const LAS v2u*)(smem + 132616);
          tag = uni64(tv.x, tv.y);
        }
        rdfrag(0);
        SBAR();
#pragma unroll 1
        for (int i = 0; i < 10; ++i) {
          rdfrag(1);
          mm(0);
#pragma unroll
          for (int n = 0; n < 4; ++n) wq[n] = (wq[n] + 32768) & 0x1FFFF;
          xa[0] = (xa[0] + 32768) & 0x1FFFF; xa[1] = (xa[1] + 32768) & 0x1FFFF;
          if (i < 7) LBAR();
          SBAR();
          rdfrag(0);
          mm(1);
        }
        LBAR();
        const float qs = s < 5 ? QSCALE : 1.0f;
        f32x4 c4[4];
#pragma unroll
        for (int n = 0; n < 4; ++n) c4[n] = *(const LAS f32x4*)(cs + (4 * nh + n) * 16 + 4 * g);
        const bool odd = tok & 1;
#pragma unroll
        for (int m = 0; m < 4; ++m) {
          const int mt = 4 * mh + m;
          const f32x2 ms = st[mt * 16 + tok];
          const float sc = ms.y * qs;
#pragma unroll
          for (int n2 = 0; n2 < 2; ++n2) {
            unsigned pk[2][2];
#pragma unroll
            for (int e = 0; e < 2; ++e) {
              const f32x4 a4 = acc[m][2 * n2 + e], cc = c4[2 * n2 + e];
              pk[e][0] = pk2((a4[0] - ms.x * cc[0]) * sc, (a4[1] - ms.x * cc[1]) * sc);
              pk[e][1] = pk2((a4[2] - ms.x * cc[2]) * sc, (a4[3] - ms.x * cc[3]) * sc);
            }
            const unsigned s0 = odd ? pk[0][0] : pk[1][0], s1 = odd ? pk[0][1] : pk[1][1];
            const unsigned r0 = (unsigned)__builtin_amdgcn_mov_dpp((int)s0, 0xB1, 0xF, 0xF, true);
            const unsigned r1 = (unsigned)__builtin_amdgcn_mov_dpp((int)s1, 0xB1, 0xF, 0xF, true);
            const v4u v = odd ? v4u{r0, r1, pk[1][0], pk[1][1]} : v4u{pk[0][0], pk[0][1], r0, r1};
            const int quad = 32 * s + 4 * (4 * nh + 2 * n2 + (odd ? 1 : 0)) + g;
            gst128(rs, WS_QKV + (quad * 2048 + P128 * 128 + mt * 16 + (tok & 14)) * 8, v);
          }
        }
      }
      if (w < 4) publish(0, 4u, WS_F1 + (P128 * 15 + s) * FL_STRIDE);
    } else {
      const int v = ((xcd - 4) * 4 + (t - 28)) * 8 + w;
#pragma unroll 1
      for (int i = 0; i < 3; ++i) {
        const char *ps0, *ps1; int ppitch;
        pack_src(p, v + 128 * i < 375 ? v + 128 * i : 0, lane, ps0, ps1, ppitch);
        pack_issue(ps0, ps1, ppitch, lds0 + (w * 3 + i) * 4096);
      }
      att_consts();
      tag = read_tag();
#pragma unroll 1
      for (int i = 0; i < 3; ++i) {
        const int u = v + 128 * i;
        if (u < 375) { int pd[4]; bool phalf; pack_dest(u, pd, phalf); pack_finish(rs, L + (w * 3 + i) * 4096, pd, phalf, lane); }
      }
      WAIT_BAR(0);
      if (tid == 0) set_flag(p.ws, WS_ARR + b * 8, tag);
      if (w == 6) poll_early();
    }
  }

  const int pg = xcd * 4 + (t & 3), jg = t >> 2;
  v4u wo[5][3];
  v2u xr[3];
  const int ntile = jg < 6 ? 14 : 12, tbase = jg < 7 ? 14 * jg : 96;
  const bool gemm = 2 * w < ntile;
  constexpr int W1E = 8;
  constexpr int W1M = 14;
  constexpr int W2L = 7;
  constexpr int W2LOFF = 8 * 11776 + 1024;
  v4u w1f[20][2];
  const int w1base = gemm ? WS_W1P + (tbase + 2 * w) * 20480 + lane * 16 : 0x7F000000;
  const int w2base = WS_W2P + (jg * 8 + w) * 35840;
  {
    const int pn = t, j = xcd;
    const int tok0 = 64 * pn - 256;
    const int wobase = WS_WOP + (jg * 8 + w) * 12800;
    auto wo_loads = [&](int i) {
      if (i < 5) {
        wo[i][0] = gld128(rs, wobase + i * 2560 + lane * 16);
        wo[i][1] = gld128(rs, wobase + i * 2560 + 1024 + lane * 16);
        wo[i][2] = gld128(rs, wobase + i * 2560 + 2048 + lane * 8);
      } else {
        const char* src = p.x + (size_t)((64 * pg + 16 * (w >> 1) + tok) * 1280 + (80 * jg + 32 * (w & 1) + 4 * g) * 2);
        xr[0] = *(const GAS v2u*)src;
        xr[1] = *(const GAS v2u*)(src + 32);
        xr[2] = *(const GAS v2u*)(p.x + (size_t)((64 * pg + 16 * (w & 3) + tok) * 1280 + (80 * jg + 64 + 4 * g) * 2));
      }
      SBAR();
    };
    auto pre = [&](int i) {
      const int q = t * 8 + w;
      if (q < 216 && lane < 16) touch4(p.ws + (size_t)(WS_W1P + (q >> 1) * 20480 + (32 * i + 16 * (q & 1) + lane) * 128), lds0 + LDS_BYTES - 256);
    };
    auto wo_all = [&]() {
#pragma unroll
      for (int i = 0; i < 6; ++i) wo_loads(i);
    };
    LBAR();
    if (w != 7) { wo_all(); code_warm((unsigned)tid); }
    if (w == 7) {
      const int R = lane / 6, rem = lane % 6, pi = rem >> 1;
      const int q0 = 160 * R + 20 * j, sl = (rem & 1) ? (q0 + 19) >> 5 : q0 >> 5;
      const int P = (pn >> 1) - pi;
      const bool want = lane < 18 && P >= 0 && (R > 0 || pi == 0);
      poll_flags(p.ws, WS_F1 + ((want ? P : 0) * 15 + (want ? sl : 0)) * FL_STRIDE, want, tag);
    }
    LBAR();
    {
      const char* src = p.ws + (size_t)kvsrc;
      const unsigned dst = lds0 + 5 * w * PL;
      if (pn >= 4) {
#pragma unroll
        for (int i = 0; i < 5; ++i) { glds16_sc1(src + i * 16384, dst + i * PL); glds16_sc1(src + i * 16384 + 1024, dst + i * PL + 1024); }
      } else {
#pragma unroll 1
        for (int i = 0; i < 10; ++i) if (tok0 + (i & 1) * 128 + lane * 2 >= 0) glds16_sc1(src + (i >> 1) * 16384 + (i & 1) * 1024, dst + (i >> 1) * PL + (i & 1) * 1024);
      }
      if (lane < 32) {
#pragma unroll
        for (int i = 0; i < 5; ++i) glds16_sc1(src + i * 16384 + 2048, dst + i * PL + 2048);
      }
    }
    if (w < 5) {
#pragma unroll
      for (int i = 0; i < 2; ++i) {
        const int pl = 4 * w + 2 * i + (lane >> 5);
        const char* src = p.ws + (size_t)(WS_QKV + ((20 * j + pl) * 2048 + 64 * pn) * 8 + (lane & 31) * 16);
        glds16_sc1(src, lds0 + QOFF + (4 * w + 2 * i) * 512);
      }
    }
    if (tid < 352) *(LAS v2u*)(smem + ONESOFF + tid * 8) = v2u{0x00003F80u, 0u};
    if (pn < 4) {
      const int n16 = -tok0 / 2;
#pragma unroll 1
      for (int i = tid; i < 40 * n16; i += 512) { const int pl = i / n16; *(LAS v4u*)(smem + pl * PL + (i - pl * n16) * 16) = v4u{0u, 0u, 0u, 0u}; }
    }
    if (w == 6) poll_arr(p.ws, tag, lane);
    WAIT_BAR(0);
    if (w == 7) wo_all();
    pre(0);
    v4u qf0, qf1;
    {
      const v2u a = lds64(L + qa), c = lds64(L + qa + 512);
      qf0 = v4u{a.x, a.y, c.x, c.y};
      const v2u d = lds64(L + qb);
      qf1 = h ? v4u{0u, 0u, 0u, 0u} : v4u{d.x, d.y, 0x00003F80u, 0u};
    }
    lptr kp0 = L + ka, kp1 = L + kb;
    auto kfrag = [&](int blk, v4u (&kf)[2]) {
      const v2u a = lds64(kp0 + blk * 256), d = lds64(kp0 + PL + blk * 256), e = lds64(kp1 + blk * 256);
      kf[0] = v4u{a.x, a.y, d.x, d.y};
      kf[1] = v4u{e.x, e.y, blk >= bmin ? 0u : 0x0000F149u  , 0u};
    };
    auto scores = [&](const v4u (&kf)[2], f32x16 c) -> f32x16 { return mfma32(kf[1], qf1, mfma32(kf[0], qf0, c)); };
    f32x16 zero16;
#pragma unroll
    for (int r = 0; r < 16; ++r) zero16[r] = 0.f;
    lptr vp = L + va;
    f32x16 o = zero16;
    auto vfrag = [&](int blk, v4u (&vf)[2]) {
#pragma unroll
      for (int s2 = 0; s2 < 2; ++s2) {
        const v2u a = ldstr(vp + blk * 256 + s2 * 128), c = ldstr(vp + blk * 256 + s2 * 128 + 64);
        vf[s2] = v4u{a.x, a.y, c.x, c.y};
      }
    };
    auto pv = [&](const v4u (&vf)[2], const unsigned (&pk)[8]) {
      o = mfma32(vf[0], v4u{pk[0], pk[1], pk[2], pk[3]}, o);
      o = mfma32(vf[1], v4u{pk[4], pk[5], pk[6], pk[7]}, o);
    };
    auto xmax = [&](float v) {
      const unsigned u = asu(v);
      const auto sw = __builtin_amdgcn_permlane32_swap(u, u, false, false);
      return fmaxf(asf((unsigned)sw[0]), asf((unsigned)sw[1]));
    };
    {
      constexpr float THR = ATT_THR;
      v4u k8[2], k0[2], kn[2][2], v8[2], v0[2];
      kfrag(8, k8); kfrag(0, k0); kfrag(1, kn[1]); kfrag(2, kn[0]);
      vfrag(8, v8); vfrag(0, v0);
      SBAR();
      const f32x16 sa = scores(k8, zero16), sb = scores(k0, zero16);
      SBAR();
      float sm[16];
#pragma unroll
      for (int r = 0; r < 16; ++r) sm[r] = __builtin_amdgcn_inverse_ballot_w64(le_mask(r)) ? sa[r] : sb[r];
      float m = fmaxf(sm[0], sm[1]);
#pragma unroll
      for (int r = 2; r < 16; r += 2) m = fmaxf(fmaxf(m, sm[r]), sm[r + 1]);
      m = xmax(m);
      f32x16 negm;
#pragma unroll
      for (int r = 0; r < 16; ++r) negm[r] = -m;
      SBAR();
      f32x16 sn = scores(kn[1], negm);
      SBAR();
      unsigned p8[8], p0[8];
#pragma unroll
      for (int r = 0; r < 16; r += 2) {
        const unsigned pkd = pk2(__builtin_amdgcn_exp2f(sm[r] - m), __builtin_amdgcn_exp2f(sm[r + 1] - m));
        p8[r >> 1] = pkd & msk[r >> 1]; p0[r >> 1] = pkd & ~msk[r >> 1];
        PIN(p8[r >> 1]); PIN(p0[r >> 1]);
      }
      SBAR();
      pv(v8, p8);
      pv(v0, p0);
#pragma unroll
      for (int blk = 1; blk < 8; ++blk) {
        f32x16 sc = sn;
        v4u vf[2];
        SBAR();
        if (blk < 7) sn = scores(kn[(blk + 1) & 1], negm);
        vfrag(blk, vf);
        if (blk < 6) kfrag(blk + 2, kn[blk & 1]);
        if (blk < 5) pre(blk);
        SBAR();
        float b0 = fmaxf(fmaxf(sc[0], sc[1]), sc[2]), b1 = fmaxf(fmaxf(sc[3], sc[4]), sc[5]);
#pragma unroll
        for (int r = 6; r < 16; r += 4) { b0 = fmaxf(fmaxf(b0, sc[r]), sc[r + 1]); if (r + 3 < 16) b1 = fmaxf(fmaxf(b1, sc[r + 2]), sc[r + 3]); }
        b0 = fmaxf(b0, b1);
        if (__builtin_expect(__any(b0 > THR), 0)) {
          const float d = fmaxf(xmax(b0), 0.f), f = __builtin_amdgcn_exp2f(-d);
#pragma unroll
          for (int r = 0; r < 16; ++r) { sc[r] -= d; sn[r] -= d; negm[r] -= d; o[r] *= f; }
        }
        SBAR();
        unsigned pk[8];
#pragma unroll
        for (int r = 0; r < 16; r += 2) { pk[r >> 1] = pk2(__builtin_amdgcn_exp2f(sc[r]), __builtin_amdgcn_exp2f(sc[r + 1])); PIN(pk[r >> 1]); }
        SBAR();
        pv(vf, pk);
      }
    }
    {
      const unsigned du = asu(o[8]);
      const auto dsw = __builtin_amdgcn_permlane32_swap(du, du, false, false);
      const float inv = __builtin_amdgcn_rcpf(asf((unsigned)dsw[1]));
#pragma unroll
      for (int c = 0; c < 3; ++c) gst64(rs, oadr[c], v2u{pk2(o[4 * c] * inv, o[4 * c + 1] * inv), pk2(o[4 * c + 2] * inv, o[4 * c + 3] * inv)});
    }
    publish(1, 8u, WS_F2 + (pn * 8 + j) * FL_STRIDE);
    LBAR();
    code_warm((unsigned)tid);
  }
  const int pn = pg, j = jg;

  auto red_write = [&](int lane, int hp, const f32x4 (&acc)[2][5]) {
#pragma unroll
    for (int ml = 0; ml < 2; ++ml) {
      LAS char* d = (LAS char*)smem + ((2 * hp + ml) * 8 + w) * 2560;
#pragma unroll
      for (int q = 0; q < 2; ++q) {
        const f32x4 a = acc[ml][2 * q], c = acc[ml][2 * q + 1];
        *(LAS v4u*)(d + q * 1024 + lane * 16) = v4u{pk2(a[0], a[1]), pk2(a[2], a[3]), pk2(c[0], c[1]), pk2(c[2], c[3])};
      }
      const f32x4 a = acc[ml][4];
      *(LAS v2u*)(d + 2048 + lane * 8) = v2u{pk2(a[0], a[1]), pk2(a[2], a[3])};
    }
  };
  auto red_sum = [&](int lane, f32x4& s0, f32x4& s1, f32x4& s2) {
    const int n = lane & 15, gg = lane >> 4;
    const unsigned one = (n >> 2) == gg ? ((n & 1) ? 0x3F800000u : 0x00003F80u) : 0u;
    const unsigned e01 = (n & 2) ? 0u : one, e23 = (n & 2) ? one : 0u;
    const v4u selA = {e01, e23, 0u, 0u}, selB = {0u, 0u, e01, e23};
    v4u r[8]; v2u r2[8];
#pragma unroll
    for (int ww = 0; ww < 8; ++ww) {
      r[ww] = lds128(L + ((w >> 1) * 8 + ww) * 2560 + (w & 1) * 1024 + lane * 16);
      r2[ww] = lds64(L + ((w & 3) * 8 + ww) * 2560 + 2048 + lane * 8);
    }
    SBAR();
#pragma unroll
    for (int ww = 0; ww < 8; ++ww) { s0 = mfma16(selA, r[ww], s0); s1 = mfma16(selB, r[ww], s1); }
    const v4u selC = {e01, e23, e01, e23};
#pragma unroll
    for (int ww = 0; ww < 8; ww += 2) s2 = mfma16(selC, v4u{r2[ww].x, r2[ww].y, r2[ww + 1].x, r2[ww + 1].y}, s2);
    SBAR();
  };
  {
#pragma unroll
    for (int ks = 0; ks < W1E; ++ks) { w1f[ks][0] = gld128(rs, w1base + ks * 1024); w1f[ks][1] = gld128(rs, w1base + (20 + ks) * 1024); }
    SBAR();
    poll_flags(p.ws, WS_F2 + (pn * 8 + w) * FL_STRIDE, lane == 0, tag);
    const int obase = WS_O + (pn * 8 + w) * 10240;
    v4u ba[2][2][3];
    auto oload = [&](int hp) {
#pragma unroll
      for (int ml = 0; ml < 2; ++ml) {
        ba[hp][ml][0] = gld128(rs, obase + (2 * hp + ml) * 2560 + lane * 16);
        ba[hp][ml][1] = gld128(rs, obase + (2 * hp + ml) * 2560 + 1024 + lane * 16);
        const v2u c = gld64(rs, obase + (2 * hp + ml) * 2560 + 2048 + lane * 8);
        ba[hp][ml][2] = v4u{c.x, c.y, 0u, 0u};
      }
      SBAR();
    };
    oload(0); oload(1);
#pragma unroll
    for (int hp = 0; hp < 2; ++hp) {
      f32x4 acc[2][5];
#pragma unroll
      for (int ml = 0; ml < 2; ++ml)
#pragma unroll
        for (int nt = 0; nt < 5; ++nt) acc[ml][nt] = mfma16(wo[nt][0], ba[hp][ml][0], f32x4{0, 0, 0, 0});
#pragma unroll
      for (int k = 1; k < 3; ++k)
#pragma unroll
        for (int ml = 0; ml < 2; ++ml)
#pragma unroll
          for (int nt = 0; nt < 5; ++nt) acc[ml][nt] = mfma16(wo[nt][k], ba[hp][ml][k], acc[ml][nt]);
      red_write(lane, hp, acc);
      SBAR();
    }
    LBAR();
    {
      f32x4 s0 = {bflo(xr[0].x), bfhi(xr[0].x), bflo(xr[0].y), bfhi(xr[0].y)};
      f32x4 s1 = {bflo(xr[1].x), bfhi(xr[1].x), bflo(xr[1].y), bfhi(xr[1].y)};
      f32x4 s2 = {bflo(xr[2].x), bfhi(xr[2].x), bflo(xr[2].y), bfhi(xr[2].y)};
      red_sum(lane, s0, s1, s2);
      const v4u hres = v4u{pk2(s0[0], s0[1]), pk2(s0[2], s0[3]), pk2(s1[0], s1[1]), pk2(s1[2], s1[3])};
      const v4u hres2 = v4u{pk2(s2[0], s2[1]), pk2(s2[2], s2[3]), 0u, 0u};
      const int base = WS_H + (pn * 8 + j) * HB;
      gst128(rs, base + (w >> 1) * 2560 + (w & 1) * 1024 + lane * 16, hres);
      gst64(rs, w < 4 ? base + w * 2560 + 2048 + lane * 8 : 0x7F000000, v2u{hres2.x, hres2.y});
      const f32x4 z4 = {0, 0, 0, 0};
      const f32x4 su = mfma16(ONES, hres, z4), sq = mfma16(hres, hres, z4), tu = mfma16(ONES, hres2, z4), tq = mfma16(hres2, hres2, z4);
      const int r = tok & 3;
      const float q1 = r == 0 ? sq[0] : r == 1 ? sq[1] : r == 2 ? sq[2] : sq[3], q2 = r == 0 ? tq[0] : r == 1 ? tq[1] : r == 2 ? tq[2] : tq[3];
      const bool diag = g == (tok >> 2);
      gst64(rs, diag ? base + 10240 + (((w >> 1) * 3 + (w & 1)) * 16 + tok) * 8 : 0x7F000000, v2u{asu(su[0]), asu(q1)});
      gst64(rs, diag && w < 4 ? base + 10240 + ((w * 3 + 2) * 16 + tok) * 8 : 0x7F000000, v2u{asu(tu[0]), asu(q2)});
    }
    publish(2, 8u, WS_F3 + (pn * 8 + j) * FL_STRIDE);
    LBAR();
#pragma unroll
    for (int ks = W1E; ks < W1M; ++ks) { w1f[ks][0] = gld128(rs, w1base + ks * 1024); w1f[ks][1] = gld128(rs, w1base + (20 + ks) * 1024); }
    SBAR();
  }

  v4u w2f[35];
  auto w2off = [&](int f) { return w2base + f * 1024; };
  {
    constexpr int HL = 11776;
    LAS f32x2* st = (LAS f32x2*)(smem + 8 * HL);
    f32x4 acc[4][2], accC[2];
    poll_flags(p.ws, WS_F3 + (pn * 8 + w) * FL_STRIDE, lane == 0, tag);
    {
      const char* src = p.ws + (size_t)(WS_H + (pn * 8 + w) * HB + lane * 16);
#pragma unroll
      for (int i = 0; i < 11; ++i) glds16_sc1(src + i * 1024, lds0 + w * HL + i * 1024);
      if (lane < 32) glds16_sc1(src + 11 * 1024, lds0 + w * HL + 11 * 1024);
    }
    accC[0] = accC[1] = f32x4{0, 0, 0, 0};
#pragma unroll
    for (int ks = 0; ks < W1M; ++ks) { accC[0] = mfma16(w1f[ks][0], ONES, accC[0]); accC[1] = mfma16(w1f[ks][1], ONES, accC[1]); }
    SBAR();
    WAIT_BAR(0);
    auto bfrag = [&](int ks, v4u (&bf)[4]) {
#pragma unroll
      for (int mt = 0; mt < 4; ++mt) {
        if (ks < 16) bf[mt] = lds128(L + (ks >> 1) * HL + mt * 2560 + (ks & 1) * 1024 + lane * 16);
        else {
          const v2u a = lds64(L + (2 * (ks - 16)) * HL + mt * 2560 + 2048 + lane * 8), c = lds64(L + (2 * (ks - 16) + 1) * HL + mt * 2560 + 2048 + lane * 8);
          bf[mt] = v4u{a.x, a.y, c.x, c.y};
        }
      }
      SBAR();
    };
    if (w == 7) {
      float su = 0.f, sq = 0.f;
#pragma unroll
      for (int q = 0; q < 8; ++q)
#pragma unroll
        for (int part = 0; part < 3; ++part) {
          const f32x2 v = *(const LAS f32x2*)(smem + q * HL + 10240 + ((g * 3 + part) * 16 + tok) * 8);
          su += v.x; sq += v.y;
        }
      const float mean = su * (1.0f / DM);
      const float var = fmaxf(sq * (1.0f / DM) - mean * mean, 0.f);
      st[lane] = f32x2{mean, 1.0f / sqrtf(var + LN_EPS)};
    }
#pragma unroll
    for (int i = 0; i < 4; ++i) { acc[i][0] = f32x4{0, 0, 0, 0}; acc[i][1] = f32x4{0, 0, 0, 0}; }
    v4u bf[2][4];
    bfrag(0, bf[0]);
#pragma unroll
    for (int ks = 0; ks < 20; ++ks) {
      if (ks + 1 < 20) bfrag(ks + 1, bf[(ks + 1) & 1]);
#pragma unroll
      for (int mt = 0; mt < 4; ++mt) {
        acc[mt][0] = mfma16(w1f[ks][0], bf[ks & 1][mt], acc[mt][0]);
        acc[mt][1] = mfma16(w1f[ks][1], bf[ks & 1][mt], acc[mt][1]);
      }
      if (ks >= W1M) { accC[0] = mfma16(w1f[ks][0], ONES, accC[0]); accC[1] = mfma16(w1f[ks][1], ONES, accC[1]); }
      SBAR();
      if (ks < 20 - W1M) { w1f[W1M + ks][0] = gld128(rs, w1base + (W1M + ks) * 1024); w1f[W1M + ks][1] = gld128(rs, w1base + (20 + W1M + ks) * 1024); SBAR(); }
      if (ks >= 6 && ks < 6 + W2L) glds16_sc1(p.ws + (size_t)(w2off(ks - 6) + lane * 16), lds0 + W2LOFF + (w * W2L + ks - 6) * 1024);
      if (ks >= 6 && ks < 19) {
#pragma unroll
        for (int e = 0; e < (ks >= 13 ? 2 : 1); ++e) { const int f = ks < 13 ? W2L + (ks - 6) : W2L + 7 + 2 * (ks - 13) + e; w2f[f] = gld128(rs, w2off(f) + lane * 16); }
        SBAR();
      }
    }
    LBAR();
    {
#pragma unroll
      for (int mt = 0; mt < 4; ++mt) {
        const f32x2 ms = st[mt * 16 + tok];
        float v[8];
#pragma unroll
        for (int e = 0; e < 2; ++e)
#pragma unroll
          for (int r = 0; r < 4; ++r) v[4 * e + r] = acc[mt][e][r] - ms.x * accC[e][r];
        gst128(rs, gemm ? WS_H1 + (((pn * 8 + j) * 7 + w) * 4 + mt) * 1024 + lane * 16 : 0x7F000000, v4u{relu2(pk2(v[0], v[1])), relu2(pk2(v[2], v[3])), relu2(pk2(v[4], v[5])), relu2(pk2(v[6], v[7]))});
      }
    }
    publish(3, 8u, WS_F4 + (pn * 8 + j) * FL_STRIDE);
    LBAR();
  }

  {
    const bool full = w < 6;
    int lz = lane; asm volatile("" : "+v"(lz));
    v4u hres; v2u hres2;
#pragma unroll
    for (int f = 26; f < 35; ++f) w2f[f] = gld128(rs, (f < 30 || full ? w2off(f) : 0x7F000000) + lane * 16);
    SBAR();
    poll_flags(p.ws, WS_F4 + (pn * 8 + w) * FL_STRIDE, lane == 0, tag);
    const int bbase = WS_H1 + (pn * 8 + w) * 28672 + lane * 16;
    v4u bf[2][7][2];
#pragma unroll
    for (int q = 0; q < 7; ++q) {
      const int ks = (q + 2) % 7;
#pragma unroll
      for (int ml = 0; ml < 2; ++ml) bf[0][ks][ml] = gld128(rs, bbase + ((ks < 6 || full ? ks : 0) * 4 + ml) * 1024);
    }
    SBAR();
#pragma unroll
    for (int hp = 0; hp < 2; ++hp) {
      f32x4 acc[2][5];
#pragma unroll
      for (int ml = 0; ml < 2; ++ml)
#pragma unroll
        for (int nt = 0; nt < 5; ++nt) acc[ml][nt] = f32x4{0, 0, 0, 0};
#pragma unroll
      for (int q = 0; q < 7; ++q) {
        const int ks = (q + 2) % 7;
        if (q == 4 || q == 5) {
#pragma unroll
          for (int f = 5 * (q - 4); f < 5 * (q - 3); ++f) if (f < W2L) w2f[f] = lds128(L + W2LOFF + (w * W2L + f) * 1024 + lane * 16);
          SBAR();
        }
#pragma unroll
        for (int ml = 0; ml < 2; ++ml)
#pragma unroll
          for (int nt = 0; nt < 5; ++nt) acc[ml][nt] = mfma16(w2f[ks * 5 + nt], bf[hp][ks][ml], acc[ml][nt]);
        SBAR();
        if (hp == 0) {
#pragma unroll
          for (int ml = 0; ml < 2; ++ml) bf[1][ks][ml] = gld128(rs, bbase + ((ks < 6 || full ? ks : 0) * 4 + 2 + ml) * 1024);
          SBAR();
        }
      }
      red_write(lz, hp, acc);
      SBAR();
      if (hp == 0) {
        asm volatile("" ::: "memory");
        const int base = WS_H + (pn * 8 + j) * HB;
        hres = gld128(rs, base + (w >> 1) * 2560 + (w & 1) * 1024 + lz * 16);
        hres2 = gld64(rs, base + (w & 3) * 2560 + 2048 + lz * 8);
        SBAR();
      }
    }
    LBAR();
    {
      f32x4 s0 = {0, 0, 0, 0}, s1 = s0, s2 = s0;
      red_sum(lz, s0, s1, s2);
      const LAS f32x2* st = (const LAS f32x2*)(smem + 8 * 11776);
      const float r1 = st[16 * (w >> 1) + (lz & 15)].y, r2 = st[16 * (w & 3) + (lz & 15)].y;
      char* dst = p.out + (size_t)((64 * pn + 16 * (w >> 1) + (lz & 15)) * 1280 + (80 * j + 32 * (w & 1) + 4 * (lz >> 4)) * 2);
      *(GAS v2u*)dst = v2u{pk2(fmaf(s0[0], r1, bflo(hres.x)), fmaf(s0[1], r1, bfhi(hres.x))), pk2(fmaf(s0[2], r1, bflo(hres.y)), fmaf(s0[3], r1, bfhi(hres.y)))};
      *(GAS v2u*)(dst + 32) = v2u{pk2(fmaf(s1[0], r1, bflo(hres.z)), fmaf(s1[1], r1, bfhi(hres.z))), pk2(fmaf(s1[2], r1, bflo(hres.w)), fmaf(s1[3], r1, bfhi(hres.w)))};
      if (w < 4) *(GAS v2u*)(p.out + (size_t)((64 * pn + 16 * w + (lz & 15)) * 1280 + (80 * j + 64 + 4 * (lz >> 4)) * 2)) =
          v2u{pk2(fmaf(s2[0], r2, bflo(hres2.x)), fmaf(s2[1], r2, bfhi(hres2.x))), pk2(fmaf(s2[2], r2, bflo(hres2.y)), fmaf(s2[3], r2, bfhi(hres2.y)))};
    }
  }
  if (b == 0 && tid == 0) set_flag(p.ws, WS_EPOCH, tag);
  asm volatile("mk_code_end:");
}
}

extern "C" void kernel_launch(void* const* d_in, const int* in_sizes, int n_in,
                              void* d_out, int out_size, void* d_ws, size_t ws_size,
                              hipStream_t stream) {
  (void)in_sizes; (void)n_in; (void)out_size; (void)ws_size;
  Params p;
  p.x = (const char*)d_in[0]; p.wqkv = (const char*)d_in[1]; p.wo = (const char*)d_in[2];
  p.w1 = (const char*)d_in[4]; p.w2 = (const char*)d_in[5];
  p.out = (char*)d_out; p.ws = (char*)d_ws;
  (void)hipFuncSetAttribute((const void*)block_kernel, hipFuncAttributeMaxDynamicSharedMemorySize, LDS_BYTES);
  block_kernel<<<256, 512, LDS_BYTES, stream>>>(p);
}
```

```cpp
#include <hip/hip_runtime.h>
#include <hip/hip_bf16.h>

#define GAS __attribute__((address_space(1)))
#define LAS __attribute__((address_space(3)))
#define DI __device__ __forceinline__
typedef unsigned v4u __attribute__((ext_vector_type(4)));
typedef unsigned v2u __attribute__((ext_vector_type(2)));
typedef float f32x2 __attribute__((ext_vector_type(2)));
typedef float f32x4 __attribute__((ext_vector_type(4)));
typedef float f32x16 __attribute__((ext_vector_type(16)));
typedef short bf16x8 __attribute__((ext_vector_type(8)));
typedef short bf16x4 __attribute__((ext_vector_type(4)));
typedef __bf16 bfx2 __attribute__((ext_vector_type(2)));
typedef unsigned long long u64;
typedef GAS u64 gu64;
#define RLX_AGENT __ATOMIC_RELAXED, __HIP_MEMORY_SCOPE_AGENT

namespace {
constexpr int SEQ = 2048, DM = 640, NQKV = 1920, DFF = 1728;
constexpr float LN_EPS = 1e-5f;
constexpr float QSCALE = 0.22360679774997896f * 1.4426950408889634f;

constexpr int FL_STRIDE = 128;
constexpr int WS_EPOCH = 0, WS_TMO = 256, WS_ARR = 1024;
constexpr int WS_F1 = 4096;
constexpr int WS_F2 = WS_F1 + 256 * FL_STRIDE;
constexpr int WS_F3 = WS_F2 + 256 * FL_STRIDE;
constexpr int WS_F4 = WS_F3 + 256 * FL_STRIDE;
constexpr int WS_F0 = WS_F4 + 256 * FL_STRIDE;
constexpr int WS_SP = 512 * 1024;
constexpr int WS_CP = WS_SP + 16 * 10 * 1024;
constexpr int WS_QKV = 1 << 20;
constexpr int WS_O = WS_QKV + 480 * 2048 * 8;
constexpr int HB = 12288;
constexpr int WS_H = WS_O + 32 * 81920;
constexpr int WS_H1 = WS_H + 256 * HB;
constexpr int WS_WOP = WS_H1 + 32 * 8 * 28672;
constexpr int WS_W1P = WS_WOP + 64 * 12800;
constexpr int WS_W2P = WS_W1P + 108 * 20 * 1024;
constexpr int WS_END = WS_W2P + 64 * 35 * 1024;

constexpr int LDS_BYTES = 160 * 1024;
constexpr unsigned SPIN_CAP = 1u << 18;
constexpr int AUX_SC1 = 16;
constexpr float ATT_THR = 8.0f;

constexpr u64 le_mask(int r) {
  u64 m = 0;
  for (int l = 0; l < 64; ++l) if ((r & 3) + 8 * (r >> 2) + 4 * (l >> 5) <= (l & 31)) m |= 1ull << l;
  return m;
}

struct Params {
  const char* x; const char* wqkv; const char* wo; const char* w1; const char* w2;
  char* out; char* ws;
};

DI f32x4 mfma16(v4u a, v4u b, f32x4 c) {
  return __builtin_amdgcn_mfma_f32_16x16x32_bf16(__builtin_bit_cast(bf16x8, a), __builtin_bit_cast(bf16x8, b), c, 0, 0, 0);
}
DI f32x16 mfma32(v4u a, v4u b, f32x16 c) {
  return __builtin_amdgcn_mfma_f32_32x32x16_bf16(__builtin_bit_cast(bf16x8, a), __builtin_bit_cast(bf16x8, b), c, 0, 0, 0);
}
DI unsigned pk2(float lo, float hi) {
  f32x2 v = {lo, hi};
  return __builtin_bit_cast(unsigned, __builtin_convertvector(v, bfx2));
}
DI float asf(unsigned u) { return __builtin_bit_cast(float, u); }
DI unsigned asu(float f) { return __builtin_bit_cast(unsigned, f); }
typedef short s16x2 __attribute__((ext_vector_type(2)));
DI unsigned relu2(unsigned u) {
  const s16x2 v = __builtin_bit_cast(s16x2, u);
  return __builtin_bit_cast(unsigned, __builtin_elementwise_max(v, s16x2{0, 0}));
}
DI u64 uni64(unsigned lo, unsigned hi) {
  const unsigned l = (unsigned)__builtin_amdgcn_readfirstlane(lo), h = (unsigned)__builtin_amdgcn_readfirstlane(hi);
  return ((u64)h << 32) | (u64)l;
}
DI float bflo(unsigned u) { return __builtin_bit_cast(float, u << 16); }
DI float bfhi(unsigned u) { return __builtin_bit_cast(float, u & 0xffff0000u); }

DI void glds16(const char* g, unsigned lds_base) {
  unsigned sv;
  asm volatile("s_mov_b32 %0, m0\n\ts_mov_b32 m0, %2\n\ts_nop 0\n\tglobal_load_lds_dwordx4 %1, off\n\ts_mov_b32 m0, %0"
               : "=&s"(sv) : "v"(g), "s"(lds_base) : "memory");
}
DI void glds16_sc1(const char* g, unsigned lds_base) {
  unsigned sv;
  asm volatile("s_mov_b32 %0, m0\n\ts_mov_b32 m0, %2\n\ts_nop 0\n\tglobal_load_lds_dwordx4 %1, off sc1\n\ts_mov_b32 m0, %0"
               : "=&s"(sv) : "v"(g), "s"(lds_base) : "memory");
}
DI void touch4(const char* g, unsigned lds_base) {
  unsigned sv;
  asm volatile("s_mov_b32 %0, m0\n\ts_mov_b32 m0, %2\n\ts_nop 0\n\tglobal_load_lds_dword %1, off sc1\n\ts_mov_b32 m0, %0"
               : "=&s"(sv) : "v"(g), "s"(lds_base) : "memory");
}
#define WAITV(N) asm volatile("s_waitcnt vmcnt(" #N ")" ::: "memory")
#define WAIT_BAR(N) asm volatile("s_waitcnt vmcnt(" #N ") lgkmcnt(0)\n\ts_barrier" ::: "memory")
#define LBAR() asm volatile("s_waitcnt lgkmcnt(0)\n\ts_barrier" ::: "memory")
#define SBAR() __builtin_amdgcn_sched_barrier(0)
#define PIN(x) asm volatile("" : "+v"(x))

typedef LAS const char* lptr;
DI v4u lds128(lptr p) { return *(const LAS v4u*)p; }
DI v2u lds64(lptr p) { return *(const volatile LAS v2u*)p; }
DI v2u ldstr(lptr p) { return __builtin_bit_cast(v2u, __builtin_amdgcn_ds_read_tr16_b64_v4i16((LAS bf16x4*)p)); }

DI v4u gld128(__amdgpu_buffer_rsrc_t rs, int off) { return __builtin_amdgcn_raw_buffer_load_b128(rs, off, 0, AUX_SC1); }
DI v2u gld64(__amdgpu_buffer_rsrc_t rs, int off) { return __builtin_amdgcn_raw_buffer_load_b64(rs, off, 0, AUX_SC1); }
DI void gst128(__amdgpu_buffer_rsrc_t rs, int off, v4u v) { __builtin_amdgcn_raw_buffer_store_b128(v, rs, off, 0, AUX_SC1); }
DI void gst64(__amdgpu_buffer_rsrc_t rs, int off, v2u v) { __builtin_amdgcn_raw_buffer_store_b64(v, rs, off, 0, AUX_SC1); }

DI void poll_flags(char* ws, int off, bool want, u64 tag) {
  gu64* f = (gu64*)(ws + off);
  for (unsigned spins = 0;;) {
    u64 v = tag;
    if (want) v = __hip_atomic_load(f, RLX_AGENT);
    if (__all(v == tag)) break;
    if (++spins > SPIN_CAP) { __hip_atomic_store((gu64*)(ws + WS_TMO), tag, RLX_AGENT); break; }
    if ((spins & 1023u) == 0u && __builtin_amdgcn_readfirstlane((unsigned)(__hip_atomic_load((gu64*)(ws + WS_TMO), RLX_AGENT) == tag))) break;
  }
  __builtin_amdgcn_fence(__ATOMIC_ACQUIRE, "wavefront");
}
DI void poll_arr(char* ws, u64 tag, int lane) {
  gu64* f = (gu64*)(ws + WS_ARR) + lane;
  for (unsigned spins = 0;;) {
    const u64 a = __hip_atomic_load(f, RLX_AGENT), b = __hip_atomic_load(f + 64, RLX_AGENT), c = __hip_atomic_load(f + 128, RLX_AGENT), d = __hip_atomic_load(f + 192, RLX_AGENT);
    if (__all(a == tag && b == tag && c == tag && d == tag)) break;
    if (++spins > SPIN_CAP) { __hip_atomic_store((gu64*)(ws + WS_TMO), tag, RLX_AGENT); break; }
    if ((spins & 1023u) == 0u && __builtin_amdgcn_readfirstlane((unsigned)(__hip_atomic_load((gu64*)(ws + WS_TMO), RLX_AGENT) == tag))) break;
  }
  __builtin_amdgcn_fence(__ATOMIC_ACQUIRE, "wavefront");
}
DI void set_flag(char* ws, int off, u64 tag) { __hip_atomic_store((gu64*)(ws + off), tag, RLX_AGENT); }


DI void pack_src(const Params& p, int u, int lane, const char*& s0, const char*& s1, int& pitch) {
  const char* m; int r0, r1, cg;
  if (u < 240) {
    const int ru = u / 10, w = ru / 3, part = ru - 3 * w;
    cg = u - 10 * ru; m = p.wo; pitch = 1280; r0 = 80 * w + 32 * part; r1 = part == 2 ? r0 : r0 + 16;
  } else if (u < 780) {
    const int v = u - 240, ks = v / 27;
    cg = v - 27 * ks; m = p.w1; pitch = 3456;
    if (ks < 16) { r0 = 80 * (ks >> 1) + 32 * (ks & 1); r1 = r0 + 16; } else { r0 = 160 * (ks - 16) + 64; r1 = r0 + 80; }
  } else {
    const int v = u - 780, ks = v / 10;
    cg = v - 10 * ks; m = p.w2; pitch = 1280; r0 = 32 * ks; r1 = r0 + 16;
  }
  const int lr = lane >> 3, c = (lane & 7) ^ (((lr >> 1) & 3) << 1);
  s0 = m + (size_t)((r0 + lr) * pitch + cg * 128 + c * 16);
  s1 = m + (size_t)((r1 + lr) * pitch + cg * 128 + c * 16);
}
DI void pack_issue(const char* s0, const char* s1, int pitch, unsigned lds) {
  glds16(s0, lds); glds16(s0 + 8 * pitch, lds + 1024); glds16(s1, lds + 2048); glds16(s1 + 8 * pitch, lds + 3072);
}

DI void pack_dest(int u, int (&d)[4], bool& half) {
  int T0, base, sj, sn;
  half = false;
  if (u < 240) {
    const int ru = u / 10, w = ru / 3, part = ru - 3 * w;
    T0 = 4 * (u - 10 * ru); base = WS_WOP + w * 12800 + part * 1024; sj = 8 * 12800; sn = 2560; half = part == 2;
  } else if (u < 780) {
    const int v = u - 240, ks = v / 27;
    T0 = 4 * (v - 27 * ks); base = WS_W1P + ks * 1024; sj = 5 * 20480; sn = 20480;
  } else {
    const int v = u - 780, ru = v / 10;
    const int w = ru < 42 ? ru / 7 : 6 + (ru - 42) / 6, ks = ru < 42 ? ru - 7 * w : ru - 42 - 6 * (w - 6);
    T0 = 4 * (v - 10 * ru); base = WS_W2P + w * 35840 + ks * 5120; sj = 8 * 35840; sn = 1024;
  }
#pragma unroll
  for (int t = 0; t < 4; ++t) { const int T = T0 + t, jj = T / 5; d[t] = base + jj * sj + (T - 5 * jj) * sn; }
}
DI void pack_finish(__amdgpu_buffer_rsrc_t rs, lptr L, const int (&d)[4], bool half, int lane) {
  const int row = 4 * (lane >> 4) + ((lane & 15) >> 2);
  const int a0 = row * 128 + (lane & 3) * 8, sw = (row >> 1) & 3;
  v2u lo[4], hi[4];
#pragma unroll
  for (int t = 0; t < 4; ++t) { lo[t] = ldstr(L + a0 + ((t ^ sw) * 32)); hi[t] = ldstr(L + a0 + ((t ^ sw) * 32) + 2048); }
  if (half) {
#pragma unroll
    for (int t = 0; t < 4; ++t) gst64(rs, d[t] + lane * 8, lo[t]);
  } else {
#pragma unroll
    for (int t = 0; t < 4; ++t) gst128(rs, d[t] + lane * 16, v4u{lo[t].x, lo[t].y, hi[t].x, hi[t].y});
  }
}

__global__ void __launch_bounds__(512, 2) block_kernel(Params p) {
  extern __shared__ __attribute__((aligned(16))) char smem[];
  const int tid = threadIdx.x, lane = tid & 63, w = __builtin_amdgcn_readfirstlane(tid >> 6);
  const int tok = lane & 15, g = lane >> 4;
  const int b = blockIdx.x, xcd = b & 7, t = b >> 3;
  const unsigned lds0 = (unsigned)(size_t)smem;
  lptr L = (lptr)smem;
  const __amdgpu_buffer_rsrc_t rs = __builtin_amdgcn_make_buffer_rsrc(p.ws, 0, WS_END, 0x00020000);
  {
    unsigned len;
    asm volatile("mk_code_begin:\n\ts_mov_b32 %0, mk_code_end-mk_code_begin" : "=s"(len));
    const char* pc = (const char*)__builtin_amdgcn_s_getpc();
    if (w < 4) for (unsigned off = (unsigned)tid * 128u; off < len; off += 256u * 128u) touch4(pc + off, lds0 + LDS_BYTES - 256);
  }
  auto read_tag = [&]() -> u64 {
    const u64 e0 = __hip_atomic_load((gu64*)(p.ws + WS_EPOCH), RLX_AGENT);
    return uni64((unsigned)e0, (unsigned)(e0 >> 32)) + 0x9E3779B97F4A7C15ull;
  };
  const v4u ONES = {0x3F803F80u, 0x3F803F80u, 0x3F803F80u, 0x3F803F80u};
  u64 tag;
  LAS unsigned* cnt = (LAS unsigned*)(smem + LDS_BYTES - 1792);
  if (tid < 8) cnt[tid] = 0u;
  auto publish = [&](int which, unsigned n, int flag_off) {
    WAITV(0);
    unsigned old = 0;
    if (lane == 0) old = __hip_atomic_fetch_add(cnt + which, 1u, __ATOMIC_RELAXED, __HIP_MEMORY_SCOPE_WORKGROUP);
    if (lane == 0 && old == n - 1u) set_flag(p.ws, flag_off, tag);
  };

  auto poll_early = [&]() { poll_flags(p.ws, WS_ARR + ((28 + (lane & 3)) * 8 + 4 + ((lane >> 2) & 3)) * 8, lane < 16, tag); };

  constexpr int PL = 2592, QOFF = 40 * PL, ONESOFF = QOFF + 20 * 512;
  const int hl = w >> 1, qh = w & 1, r32 = lane & 31, h = lane >> 5;
  const int bmin = 8 - 2 * t - qh;
  int qa, qb, ka, kb, va, kvsrc, oadr[3];
  unsigned msk[8];
  auto att_consts = [&]() {
    qa = QOFF + (5 * hl + 2 * h) * 512 + (32 * qh + r32) * 8; qb = QOFF + (5 * hl + 4) * 512 + (32 * qh + r32) * 8;
    ka = (5 * hl + 2 * h) * PL + (32 * qh + r32) * 8; kb = (5 * hl + 4) * PL + (32 * qh + r32) * 8;
    const int blk16 = (lane >> 4) & 1, q4 = (lane & 15) >> 2, pp = lane & 3;
    va = ((blk16 == 0) ? (20 + 5 * hl + pp) * PL : (pp == 0 ? (24 + 5 * hl) * PL : ONESOFF + (((20 + 5 * hl) * 32 + 160) & 255))) + (32 * qh + 4 * h + q4) * 8;
#pragma unroll
    for (int c = 0; c < 3; ++c) {
      const int Qd = 5 * hl + (c < 2 ? 2 * c + h : 4), nt = Qd >> 2, gg = Qd & 3, tk = r32 & 15;
      const int off = nt < 4 ? (nt >> 1) * 1024 + (tk + 16 * gg) * 16 + (nt & 1) * 8 : 2048 + (tk + 16 * gg) * 8;
      oadr[c] = (c < 2 || h == 0) ? WS_O + ((t * 8 + xcd) * 4 + 2 * qh + (r32 >> 4)) * 2560 + off : 0x7F000000;
      PIN(oadr[c]);
    }
#pragma unroll
    for (int r = 0; r < 16; r += 2) {
      const int kk = (r & 3) + 8 * (r >> 2) + 4 * h;
      msk[r >> 1] = kk + 1 <= r32 ? 0xffffffffu : (kk <= r32 ? 0x0000ffffu : 0u);
      PIN(msk[r >> 1]);
    }
    kvsrc = WS_QKV + (((w < 4 ? 160 : 300) + 5 * w + 20 * xcd) * 2048 + 64 * t - 256) * 8 + lane * 16;
    PIN(qa); PIN(qb); PIN(ka); PIN(kb); PIN(va); PIN(kvsrc);
  };

  {
    const int P128 = 4 * (xcd & 3) + (t & 3), s = (xcd >> 2) * 8 + (t >> 2);
    if (s < 15) {
      LAS f32x2* st = (LAS f32x2*)(smem + 131072);
      LAS float* cs = (LAS float*)(smem + 132096);
      const int q4 = (lane & 15) >> 2, pp = lane & 3;
      const int fsw = q4 | ((g & 1) << 2);
      const int xlane = tok * 128 + ((g ^ (tok >> 1)) * 16);
      const int wlane = 16384 + (8 * g + q4) * 256 + pp * 8;
      const int cres = (3 * (s + P128)) % 10;
      const int rot = cres >= 5 ? 5 : 0, jres = cres - rot;
      if (w >= 4) {
        const int a = w - 4, lt = tid - 256;
        const char* xs; const char* wsrc;
        { const int row = lt >> 3, c = (lt & 7) ^ ((row >> 1) & 7); xs = p.x + (size_t)((P128 * 128 + row) * 1280 + c * 16); }
        { const int row = lt >> 4, f = (row & 3) | (((row >> 3) & 1) << 2), c = (lt & 15) ^ (f << 1); wsrc = p.wqkv + (size_t)(row * 3840 + s * 256 + c * 16); }
        auto stage = [&](int i) {
          int kc = i + rot; if (kc >= 10) kc -= 10;
          const unsigned d = lds0 + (i & 3) * 32768 + a * 1024;
          const char* xk = xs + kc * 128; const char* wk = wsrc + (size_t)(kc * (64 * 3840));
#pragma unroll
          for (int q = 0; q < 4; ++q) { glds16(wk + q * (16 * 3840), d + 16384 + q * 4096); glds16(xk + q * (32 * 1280), d + q * 4096); }
        };
        stage(0);
        att_consts();
        stage(1); stage(2);
        WAIT_BAR(16);
        {
          const v2u tv = *(const LAS v2u*)(smem + 132616);
          tag = uni64(tv.x, tv.y);
        }
        stage(3);
        v4u fx[3][2], fw[2][2];
#pragma unroll
        for (int k2 = 0; k2 < 2; ++k2) fx[0][k2] = fx[1][k2] = fx[2][k2] = fw[0][k2] = fw[1][k2] = v4u{0u, 0u, 0u, 0u};
#pragma unroll 1
        for (int i = 0; i < 7; ++i) {
          if (i == jres + 1) {
            const int r = tok & 3;
            f32x4 aC[2], aS[2], aG[2];
#pragma unroll
            for (int e = 0; e < 2; ++e) {
              aC[e] = mfma16(fw[e][1], ONES, mfma16(fw[e][0], ONES, f32x4{0, 0, 0, 0}));
              aS[e] = mfma16(ONES, fx[e][1], mfma16(ONES, fx[e][0], f32x4{0, 0, 0, 0}));
              aG[e] = mfma16(fx[2 * e][1], fx[e][1], mfma16(fx[2 * e][0], fx[e][0], f32x4{0, 0, 0, 0}));
            }
            const bool d1 = g == ((tok >> 2) ^ 1);
            const f32x4 G4 = d1 ? aG[1] : aG[0];
            const float G = r == 0 ? G4[0] : r == 1 ? G4[1] : r == 2 ? G4[2] : G4[3];
            gst64(rs, (s < 10 && (d1 || g == (tok >> 2))) ? WS_SP + ((P128 * 10 + cres) * 128 + (2 * a + (d1 ? 1 : 0)) * 16 + tok) * 8 : 0x7F000000, v2u{asu(d1 ? aS[1][0] : aS[0][0]), asu(G)});
            gst128(rs, (P128 < 10 && tok < 2) ? WS_CP + ((s * 10 + cres) * 128 + (2 * a + tok) * 16 + 4 * g) * 4 : 0x7F000000, __builtin_bit_cast(v4u, tok ? aC[1] : aC[0]));
          }
          if (i == jres) {
            lptr S = L + (i & 3) * 32768;
            const int xl2 = (tok ^ 4) * 128 + ((g ^ ((tok ^ 4) >> 1)) * 16);
#pragma unroll
            for (int k2 = 0; k2 < 2; ++k2) {
#pragma unroll
              for (int e = 0; e < 2; ++e) {
                fx[e][k2] = lds128(S + (2 * a + e) * 2048 + (xlane ^ (k2 * 64)));
                const int wo0 = wlane + (((2 * a + e) ^ fsw) * 32) + k2 * 8192;
                const v2u lo = ldstr(S + wo0), hi = ldstr(S + wo0 + 1024);
                fw[e][k2] = v4u{lo.x, lo.y, hi.x, hi.y};
              }
              fx[2][k2] = lds128(S + (2 * a + 1) * 2048 + (xl2 ^ (k2 * 64)));
            }
          }
          if (i < 6) WAITV(16); else WAITV(0);
          LBAR();
          if (i == (jres < 4 ? jres + 3 : 6) && tid == 256) set_flag(p.ws, WS_F0 + (P128 * 15 + s) * FL_STRIDE, tag);
          if (i + 4 < 10) stage(i + 4);
        }
        if (a < 2) {
          poll_flags(p.ws, WS_F0 + (a == 0 ? P128 * 15 + (lane < 10 ? lane : 0) : (lane < 10 ? lane : 0) * 15 + s) * FL_STRIDE, lane < 10, tag);
          if (a == 0) {
            v4u v[10];
#pragma unroll
            for (int c = 0; c < 10; ++c) v[c] = gld128(rs, WS_SP + ((P128 * 10 + c) * 128 + 2 * lane) * 8);
            float su = 0.f, sq = 0.f, tu = 0.f, tq = 0.f;
#pragma unroll
            for (int c = 0; c < 10; ++c) { su += asf(v[c].x); sq += asf(v[c].y); tu += asf(v[c].z); tq += asf(v[c].w); }
            const float m0 = su * (1.0f / DM), m1 = tu * (1.0f / DM);
            st[2 * lane] = f32x2{m0, 1.0f / sqrtf(fmaxf(sq * (1.0f / DM) - m0 * m0, 0.f) + LN_EPS)};
            st[2 * lane + 1] = f32x2{m1, 1.0f / sqrtf(fmaxf(tq * (1.0f / DM) - m1 * m1, 0.f) + LN_EPS)};
          } else {
            v2u v[10];
#pragma unroll
            for (int c = 0; c < 10; ++c) v[c] = gld64(rs, WS_CP + ((s * 10 + c) * 128 + 2 * lane) * 4);
            float su = 0.f, tu = 0.f;
#pragma unroll
            for (int c = 0; c < 10; ++c) { su += asf(v[c].x); tu += asf(v[c].y); }
            cs[2 * lane] = su; cs[2 * lane + 1] = tu;
          }
          if (a == 1) poll_early();
          LBAR();
        } else {
#pragma unroll 1
          for (int i = 0; i < 2; ++i) {
            const int pu = 375 + P128 * 15 + s + 240 * (2 * (a - 2) + i);
            const char *ps0, *ps1; int ppitch;
            pack_src(p, pu < 1320 ? pu : 0, lane, ps0, ps1, ppitch);
            pack_issue(ps0, ps1, ppitch, lds0 + 133120 + (2 * (a - 2) + i) * 4096);
          }
          LBAR();
          WAITV(0);
#pragma unroll 1
          for (int i = 0; i < 2; ++i) {
            const int pu = 375 + P128 * 15 + s + 240 * (2 * (a - 2) + i);
            if (pu < 1320) { int pd[4]; bool phalf; pack_dest(pu, pd, phalf); pack_finish(rs, L + 133120 + (2 * (a - 2) + i) * 4096, pd, phalf, lane); }
          }
          publish(4, 2u, WS_ARR + b * 8);
        }

      } else {
        const int mh = w >> 1, nh = w & 1;
        int xa[2], wq[4];
        xa[0] = mh * 8192 + xlane; xa[1] = xa[0] ^ 64;
#pragma unroll
        for (int n = 0; n < 4; ++n) wq[n] = wlane + (((4 * nh + n) ^ fsw) * 32);
        f32x4 acc[4][4];
#pragma unroll
        for (int m = 0; m < 4; ++m)
#pragma unroll
          for (int n = 0; n < 4; ++n) acc[m][n] = f32x4{0, 0, 0, 0};
        v4u xb[2][4], wa[2][4];
        auto rdfrag = [&](int k2) {
#pragma unroll
          for (int n = 0; n < 4; ++n) {
            const v2u lo = ldstr(L + wq[n] + k2 * 8192), hi = ldstr(L + wq[n] + k2 * 8192 + 1024);
            wa[k2][n] = v4u{lo.x, lo.y, hi.x, hi.y};
            xb[k2][n] = lds128(L + xa[k2] + n * 2048);
          }
        };
        auto mm = [&](int k2) {
#pragma unroll
          for (int m = 0; m < 4; ++m)
#pragma unroll
            for (int n = 0; n < 4; ++n) acc[m][n] = mfma16(wa[k2][n], xb[k2][m], acc[m][n]);
#pragma unroll
          for (int q = 0; q < 12; ++q) { __builtin_amdgcn_sched_group_barrier(0x008, 1, 0); __builtin_amdgcn_sched_group_barrier(0x100, 1, 0); }
          __builtin_amdgcn_sched_group_barrier(0x008, 4, 0);
          SBAR();
        };
        att_consts();
        if (w == 0) { const u64 tg = read_tag(); if (lane == 0) *(LAS u64*)(smem + 132616) = tg; }
        LBAR();
        {
          const v2u tv = *(const LAS v2u*)(smem + 132616);
          tag = uni64(tv.x, tv.y);
        }
        rdfrag(0);
        SBAR();
#pragma unroll 1
        for (int i = 0; i < 10; ++i) {
          rdfrag(1);
          mm(0);
#pragma unroll
          for (int n = 0; n < 4; ++n) wq[n] = (wq[n] + 32768) & 0x1FFFF;
          xa[0] = (xa[0] + 32768) & 0x1FFFF; xa[1] = (xa[1] + 32768) & 0x1FFFF;
          if (i < 7) LBAR();
          SBAR();
          rdfrag(0);
          mm(1);
        }
        LBAR();
        const float qs = s < 5 ? QSCALE : 1.0f;
        f32x4 c4[4];
#pragma unroll
        for (int n = 0; n < 4; ++n) c4[n] = *(const LAS f32x4*)(cs + (4 * nh + n) * 16 + 4 * g);
        const bool odd = tok & 1;
#pragma unroll
        for (int m = 0; m < 4; ++m) {
          const int mt = 4 * mh + m;
          const f32x2 ms = st[mt * 16 + tok];
          const float sc = ms.y * qs;
#pragma unroll
          for (int n2 = 0; n2 < 2; ++n2) {
            unsigned pk[2][2];
#pragma unroll
            for (int e = 0; e < 2; ++e) {
              const f32x4 a4 = acc[m][2 * n2 + e], cc = c4[2 * n2 + e];
              pk[e][0] = pk2((a4[0] - ms.x * cc[0]) * sc, (a4[1] - ms.x * cc[1]) * sc);
              pk[e][1] = pk2((a4[2] - ms.x * cc[2]) * sc, (a4[3] - ms.x * cc[3]) * sc);
            }
            const unsigned s0 = odd ? pk[0][0] : pk[1][0], s1 = odd ? pk[0][1] : pk[1][1];
            const unsigned r0 = (unsigned)__builtin_amdgcn_mov_dpp((int)s0, 0xB1, 0xF, 0xF, true);
            const unsigned r1 = (unsigned)__builtin_amdgcn_mov_dpp((int)s1, 0xB1, 0xF, 0xF, true);
            const v4u v = odd ? v4u{r0, r1, pk[1][0], pk[1][1]} : v4u{pk[0][0], pk[0][1], r0, r1};
            const int quad = 32 * s + 4 * (4 * nh + 2 * n2 + (odd ? 1 : 0)) + g;
            gst128(rs, WS_QKV + (quad * 2048 + P128 * 128 + mt * 16 + (tok & 14)) * 8, v);
          }
        }
      }
      if (w < 4) publish(0, 4u, WS_F1 + (P128 * 15 + s) * FL_STRIDE);
    } else {
      const int v = ((xcd - 4) * 4 + (t - 28)) * 8 + w;
#pragma unroll 1
      for (int i = 0; i < 3; ++i) {
        const char *ps0, *ps1; int ppitch;
        pack_src(p, v + 128 * i < 375 ? v + 128 * i : 0, lane, ps0, ps1, ppitch);
        pack_issue(ps0, ps1, ppitch, lds0 + (w * 3 + i) * 4096);
      }
      att_consts();
      tag = read_tag();
#pragma unroll 1
      for (int i = 0; i < 3; ++i) {
        const int u = v + 128 * i;
        if (u < 375) { int pd[4]; bool phalf; pack_dest(u, pd, phalf); pack_finish(rs, L + (w * 3 + i) * 4096, pd, phalf, lane); }
      }
      WAIT_BAR(0);
      if (tid == 0) set_flag(p.ws, WS_ARR + b * 8, tag);
      if (w == 6) poll_early();
    }
  }

  const int pg = xcd * 4 + (t & 3), jg = t >> 2;
  v4u wo[5][3];
  v2u xr[3];
  const int ntile = jg < 6 ? 14 : 12, tbase = jg < 7 ? 14 * jg : 96;
  const bool gemm = 2 * w < ntile;
  constexpr int W1E = 8;
  constexpr int W1M = 14;
  constexpr int W2L = 7;
  constexpr int W2LOFF = 8 * 11776 + 1024;
  v4u w1f[20][2];
  const int w1base = gemm ? WS_W1P + (tbase + 2 * w) * 20480 + lane * 16 : 0x7F000000;
  const int w2base = WS_W2P + (jg * 8 + w) * 35840;
  {
    const int pn = t, j = xcd;
    const int tok0 = 64 * pn - 256;
    const int wobase = WS_WOP + (jg * 8 + w) * 12800;
    auto wo_loads = [&](int i) {
      if (i < 5) {
        wo[i][0] = gld128(rs, wobase + i * 2560 + lane * 16);
        wo[i][1] = gld128(rs, wobase + i * 2560 + 1024 + lane * 16);
        wo[i][2] = gld128(rs, wobase + i * 2560 + 2048 + lane * 8);
      } else {
        const char* src = p.x + (size_t)((64 * pg + 16 * (w >> 1) + tok) * 1280 + (80 * jg + 32 * (w & 1) + 4 * g) * 2);
        xr[0] = *(const GAS v2u*)src;
        xr[1] = *(const GAS v2u*)(src + 32);
        xr[2] = *(const GAS v2u*)(p.x + (size_t)((64 * pg + 16 * (w & 3) + tok) * 1280 + (80 * jg + 64 + 4 * g) * 2));
      }
      SBAR();
    };
    auto pre = [&](int i) {
      const int q = t * 8 + w;
      if (q < 216 && lane < 16) touch4(p.ws + (size_t)(WS_W1P + (q >> 1) * 20480 + (32 * i + 16 * (q & 1) + lane) * 128), lds0 + LDS_BYTES - 256);
    };
    auto wo_all = [&]() {
#pragma unroll
      for (int i = 0; i < 6; ++i) wo_loads(i);
    };
    LBAR();
    if (w != 7) wo_all();
    if (w == 7) {
      const int R = lane / 6, rem = lane % 6, pi = rem >> 1;
      const int q0 = 160 * R + 20 * j, sl = (rem & 1) ? (q0 + 19) >> 5 : q0 >> 5;
      const int P = (pn >> 1) - pi;
      const bool want = lane < 18 && P >= 0 && (R > 0 || pi == 0);
      poll_flags(p.ws, WS_F1 + ((want ? P : 0) * 15 + (want ? sl : 0)) * FL_STRIDE, want, tag);
    }
    LBAR();
    {
      const char* src = p.ws + (size_t)kvsrc;
      const unsigned dst = lds0 + 5 * w * PL;
      if (pn >= 4) {
#pragma unroll
        for (int i = 0; i < 5; ++i) { glds16_sc1(src + i * 16384, dst + i * PL); glds16_sc1(src + i * 16384 + 1024, dst + i * PL + 1024); }
      } else {
#pragma unroll 1
        for (int i = 0; i < 10; ++i) if (tok0 + (i & 1) * 128 + lane * 2 >= 0) glds16_sc1(src + (i >> 1) * 16384 + (i & 1) * 1024, dst + (i >> 1) * PL + (i & 1) * 1024);
      }
      if (lane < 32) {
#pragma unroll
        for (int i = 0; i < 5; ++i) glds16_sc1(src + i * 16384 + 2048, dst + i * PL + 2048);
      }
    }
    if (w < 5) {
#pragma unroll
      for (int i = 0; i < 2; ++i) {
        const int pl = 4 * w + 2 * i + (lane >> 5);
        const char* src = p.ws + (size_t)(WS_QKV + ((20 * j + pl) * 2048 + 64 * pn) * 8 + (lane & 31) * 16);
        glds16_sc1(src, lds0 + QOFF + (4 * w + 2 * i) * 512);
      }
    }
    if (tid < 352) *(LAS v2u*)(smem + ONESOFF + tid * 8) = v2u{0x00003F80u, 0u};
    if (pn < 4) {
      const int n16 = -tok0 / 2;
#pragma unroll 1
      for (int i = tid; i < 40 * n16; i += 512) { const int pl = i / n16; *(LAS v4u*)(smem + pl * PL + (i - pl * n16) * 16) = v4u{0u, 0u, 0u, 0u}; }
    }
    if (w == 6) poll_arr(p.ws, tag, lane);
    WAIT_BAR(0);
    if (w == 7) wo_all();
    pre(0);
    v4u qf0, qf1;
    {
      const v2u a = lds64(L + qa), c = lds64(L + qa + 512);
      qf0 = v4u{a.x, a.y, c.x, c.y};
      const v2u d = lds64(L + qb);
      qf1 = h ? v4u{0u, 0u, 0u, 0u} : v4u{d.x, d.y, 0x00003F80u, 0u};
    }
    lptr kp0 = L + ka, kp1 = L + kb;
    auto kfrag = [&](int blk, v4u (&kf)[2]) {
      const v2u a = lds64(kp0 + blk * 256), d = lds64(kp0 + PL + blk * 256), e = lds64(kp1 + blk * 256);
      kf[0] = v4u{a.x, a.y, d.x, d.y};
      kf[1] = v4u{e.x, e.y, blk >= bmin ? 0u : 0x0000F149u  , 0u};
    };
    auto scores = [&](const v4u (&kf)[2], f32x16 c) -> f32x16 { return mfma32(kf[1], qf1, mfma32(kf[0], qf0, c)); };
    f32x16 zero16;
#pragma unroll
    for (int r = 0; r < 16; ++r) zero16[r] = 0.f;
    lptr vp = L + va;
    f32x16 o = zero16;
    auto vfrag = [&](int blk, v4u (&vf)[2]) {
#pragma unroll
      for (int s2 = 0; s2 < 2; ++s2) {
        const v2u a = ldstr(vp + blk * 256 + s2 * 128), c = ldstr(vp + blk * 256 + s2 * 128 + 64);
        vf[s2] = v4u{a.x, a.y, c.x, c.y};
      }
    };
    auto pv = [&](const v4u (&vf)[2], const unsigned (&pk)[8]) {
      o = mfma32(vf[0], v4u{pk[0], pk[1], pk[2], pk[3]}, o);
      o = mfma32(vf[1], v4u{pk[4], pk[5], pk[6], pk[7]}, o);
    };
    auto xmax = [&](float v) {
      const unsigned u = asu(v);
      const auto sw = __builtin_amdgcn_permlane32_swap(u, u, false, false);
      return fmaxf(asf((unsigned)sw[0]), asf((unsigned)sw[1]));
    };
    {
      constexpr float THR = ATT_THR;
      v4u k8[2], k0[2], kn[2][2], v8[2], v0[2];
      kfrag(8, k8); kfrag(0, k0); kfrag(1, kn[1]); kfrag(2, kn[0]);
      vfrag(8, v8); vfrag(0, v0);
      SBAR();
      const f32x16 sa = scores(k8, zero16), sb = scores(k0, zero16);
      SBAR();
      float sm[16];
#pragma unroll
      for (int r = 0; r < 16; ++r) sm[r] = __builtin_amdgcn_inverse_ballot_w64(le_mask(r)) ? sa[r] : sb[r];
      float m = fmaxf(sm[0], sm[1]);
#pragma unroll
      for (int r = 2; r < 16; r += 2) m = fmaxf(fmaxf(m, sm[r]), sm[r + 1]);
      m = xmax(m);
      f32x16 negm;
#pragma unroll
      for (int r = 0; r < 16; ++r) negm[r] = -m;
      SBAR();
      f32x16 sn = scores(kn[1], negm);
      SBAR();
      unsigned p8[8], p0[8];
#pragma unroll
      for (int r = 0; r < 16; r += 2) {
        const unsigned pkd = pk2(__builtin_amdgcn_exp2f(sm[r] - m), __builtin_amdgcn_exp2f(sm[r + 1] - m));
        p8[r >> 1] = pkd & msk[r >> 1]; p0[r >> 1] = pkd & ~msk[r >> 1];
        PIN(p8[r >> 1]); PIN(p0[r >> 1]);
      }
      SBAR();
      pv(v8, p8);
      pv(v0, p0);
#pragma unroll
      for (int blk = 1; blk < 8; ++blk) {
        f32x16 sc = sn;
        v4u vf[2];
        SBAR();
        if (blk < 7) sn = scores(kn[(blk + 1) & 1], negm);
        vfrag(blk, vf);
        if (blk < 6) kfrag(blk + 2, kn[blk & 1]);
        if (blk < 5) pre(blk);
        SBAR();
        float b0 = fmaxf(fmaxf(sc[0], sc[1]), sc[2]), b1 = fmaxf(fmaxf(sc[3], sc[4]), sc[5]);
#pragma unroll
        for (int r = 6; r < 16; r += 4) { b0 = fmaxf(fmaxf(b0, sc[r]), sc[r + 1]); if (r + 3 < 16) b1 = fmaxf(fmaxf(b1, sc[r + 2]), sc[r + 3]); }
        b0 = fmaxf(b0, b1);
        if (__builtin_expect(__any(b0 > THR), 0)) {
          const float d = fmaxf(xmax(b0), 0.f), f = __builtin_amdgcn_exp2f(-d);
#pragma unroll
          for (int r = 0; r < 16; ++r) { sc[r] -= d; sn[r] -= d; negm[r] -= d; o[r] *= f; }
        }
        SBAR();
        unsigned pk[8];
#pragma unroll
        for (int r = 0; r < 16; r += 2) { pk[r >> 1] = pk2(__builtin_amdgcn_exp2f(sc[r]), __builtin_amdgcn_exp2f(sc[r + 1])); PIN(pk[r >> 1]); }
        SBAR();
        pv(vf, pk);
      }
    }
    {
      const unsigned du = asu(o[8]);
      const auto dsw = __builtin_amdgcn_permlane32_swap(du, du, false, false);
      const float inv = __builtin_amdgcn_rcpf(asf((unsigned)dsw[1]));
#pragma unroll
      for (int c = 0; c < 3; ++c) gst64(rs, oadr[c], v2u{pk2(o[4 * c] * inv, o[4 * c + 1] * inv), pk2(o[4 * c + 2] * inv, o[4 * c + 3] * inv)});
    }
    publish(1, 8u, WS_F2 + (pn * 8 + j) * FL_STRIDE);
    LBAR();
  }
  const int pn = pg, j = jg;

  auto red_write = [&](int lane, int hp, const f32x4 (&acc)[2][5]) {
#pragma unroll
    for (int ml = 0; ml < 2; ++ml) {
      LAS char* d = (LAS char*)smem + ((2 * hp + ml) * 8 + w) * 2560;
#pragma unroll
      for (int q = 0; q < 2; ++q) {
        const f32x4 a = acc[ml][2 * q], c = acc[ml][2 * q + 1];
        *(LAS v4u*)(d + q * 1024 + lane * 16) = v4u{pk2(a[0], a[1]), pk2(a[2], a[3]), pk2(c[0], c[1]), pk2(c[2], c[3])};
      }
      const f32x4 a = acc[ml][4];
      *(LAS v2u*)(d + 2048 + lane * 8) = v2u{pk2(a[0], a[1]), pk2(a[2], a[3])};
    }
  };
  auto red_sum = [&](int lane, f32x4& s0, f32x4& s1, f32x4& s2) {
    const int n = lane & 15, gg = lane >> 4;
    const unsigned one = (n >> 2) == gg ? ((n & 1) ? 0x3F800000u : 0x00003F80u) : 0u;
    const unsigned e01 = (n & 2) ? 0u : one, e23 = (n & 2) ? one : 0u;
    const v4u selA = {e01, e23, 0u, 0u}, selB = {0u, 0u, e01, e23};
    v4u r[8]; v2u r2[8];
#pragma unroll
    for (int ww = 0; ww < 8; ++ww) {
      r[ww] = lds128(L + ((w >> 1) * 8 + ww) * 2560 + (w & 1) * 1024 + lane * 16);
      r2[ww] = lds64(L + ((w & 3) * 8 + ww) * 2560 + 2048 + lane * 8);
    }
    SBAR();
#pragma unroll
    for (int ww = 0; ww < 8; ++ww) { s0 = mfma16(selA, r[ww], s0); s1 = mfma16(selB, r[ww], s1); }
    const v4u selC = {e01, e23, e01, e23};
#pragma unroll
    for (int ww = 0; ww < 8; ww += 2) s2 = mfma16(selC, v4u{r2[ww].x, r2[ww].y, r2[ww + 1].x, r2[ww + 1].y}, s2);
    SBAR();
  };
  {
#pragma unroll
    for (int ks = 0; ks < W1E; ++ks) { w1f[ks][0] = gld128(rs, w1base + ks * 1024); w1f[ks][1] = gld128(rs, w1base + (20 + ks) * 1024); }
    SBAR();
    poll_flags(p.ws, WS_F2 + (pn * 8 + w) * FL_STRIDE, lane == 0, tag);
    const int obase = WS_O + (pn * 8 + w) * 10240;
    v4u ba[2][2][3];
    auto oload = [&](int hp) {
#pragma unroll
      for (int ml = 0; ml < 2; ++ml) {
        ba[hp][ml][0] = gld128(rs, obase + (2 * hp + ml) * 2560 + lane * 16);
        ba[hp][ml][1] = gld128(rs, obase + (2 * hp + ml) * 2560 + 1024 + lane * 16);
        const v2u c = gld64(rs, obase + (2 * hp + ml) * 2560 + 2048 + lane * 8);
        ba[hp][ml][2] = v4u{c.x, c.y, 0u, 0u};
      }
      SBAR();
    };
    oload(0); oload(1);
#pragma unroll
    for (int hp = 0; hp < 2; ++hp) {
      f32x4 acc[2][5];
#pragma unroll
      for (int ml = 0; ml < 2; ++ml)
#pragma unroll
        for (int nt = 0; nt < 5; ++nt) acc[ml][nt] = mfma16(wo[nt][0], ba[hp][ml][0], f32x4{0, 0, 0, 0});
#pragma unroll
      for (int k = 1; k < 3; ++k)
#pragma unroll
        for (int ml = 0; ml < 2; ++ml)
#pragma unroll
          for (int nt = 0; nt < 5; ++nt) acc[ml][nt] = mfma16(wo[nt][k], ba[hp][ml][k], acc[ml][nt]);
      red_write(lane, hp, acc);
      SBAR();
    }
    LBAR();
    {
      f32x4 s0 = {bflo(xr[0].x), bfhi(xr[0].x), bflo(xr[0].y), bfhi(xr[0].y)};
      f32x4 s1 = {bflo(xr[1].x), bfhi(xr[1].x), bflo(xr[1].y), bfhi(xr[1].y)};
      f32x4 s2 = {bflo(xr[2].x), bfhi(xr[2].x), bflo(xr[2].y), bfhi(xr[2].y)};
      red_sum(lane, s0, s1, s2);
      const v4u hres = v4u{pk2(s0[0], s0[1]), pk2(s0[2], s0[3]), pk2(s1[0], s1[1]), pk2(s1[2], s1[3])};
      const v4u hres2 = v4u{pk2(s2[0], s2[1]), pk2(s2[2], s2[3]), 0u, 0u};
      const int base = WS_H + (pn * 8 + j) * HB;
      gst128(rs, base + (w >> 1) * 2560 + (w & 1) * 1024 + lane * 16, hres);
      gst64(rs, w < 4 ? base + w * 2560 + 2048 + lane * 8 : 0x7F000000, v2u{hres2.x, hres2.y});
      const f32x4 z4 = {0, 0, 0, 0};
      const f32x4 su = mfma16(ONES, hres, z4), sq = mfma16(hres, hres, z4), tu = mfma16(ONES, hres2, z4), tq = mfma16(hres2, hres2, z4);
      const int r = tok & 3;
      const float q1 = r == 0 ? sq[0] : r == 1 ? sq[1] : r == 2 ? sq[2] : sq[3], q2 = r == 0 ? tq[0] : r == 1 ? tq[1] : r == 2 ? tq[2] : tq[3];
      const bool diag = g == (tok >> 2);
      gst64(rs, diag ? base + 10240 + (((w >> 1) * 3 + (w & 1)) * 16 + tok) * 8 : 0x7F000000, v2u{asu(su[0]), asu(q1)});
      gst64(rs, diag && w < 4 ? base + 10240 + ((w * 3 + 2) * 16 + tok) * 8 : 0x7F000000, v2u{asu(tu[0]), asu(q2)});
    }
    publish(2, 8u, WS_F3 + (pn * 8 + j) * FL_STRIDE);
    LBAR();
#pragma unroll
    for (int ks = W1E; ks < W1M; ++ks) { w1f[ks][0] = gld128(rs, w1base + ks * 1024); w1f[ks][1] = gld128(rs, w1base + (20 + ks) * 1024); }
    SBAR();
  }

  v4u w2f[35];
  auto w2off = [&](int f) { return w2base + f * 1024; };
  {
    constexpr int HL = 11776;
    LAS f32x2* st = (LAS f32x2*)(smem + 8 * HL);
    f32x4 acc[4][2], accC[2];
    poll_flags(p.ws, WS_F3 + (pn * 8 + w) * FL_STRIDE, lane == 0, tag);
    {
      const char* src = p.ws + (size_t)(WS_H + (pn * 8 + w) * HB + lane * 16);
#pragma unroll
      for (int i = 0; i < 11; ++i) glds16_sc1(src + i * 1024, lds0 + w * HL + i * 1024);
      if (lane < 32) glds16_sc1(src + 11 * 1024, lds0 + w * HL + 11 * 1024);
    }
    accC[0] = accC[1] = f32x4{0, 0, 0, 0};
#pragma unroll
    for (int ks = 0; ks < W1M; ++ks) { accC[0] = mfma16(w1f[ks][0], ONES, accC[0]); accC[1] = mfma16(w1f[ks][1], ONES, accC[1]); }
    SBAR();
    WAIT_BAR(0);
    auto bfrag = [&](int ks, v4u (&bf)[4]) {
#pragma unroll
      for (int mt = 0; mt < 4; ++mt) {
        if (ks < 16) bf[mt] = lds128(L + (ks >> 1) * HL + mt * 2560 + (ks & 1) * 1024 + lane * 16);
        else {
          const v2u a = lds64(L + (2 * (ks - 16)) * HL + mt * 2560 + 2048 + lane * 8), c = lds64(L + (2 * (ks - 16) + 1) * HL + mt * 2560 + 2048 + lane * 8);
          bf[mt] = v4u{a.x, a.y, c.x, c.y};
        }
      }
      SBAR();
    };
    if (w == 7) {
      float su = 0.f, sq = 0.f;
#pragma unroll
      for (int q = 0; q < 8; ++q)
#pragma unroll
        for (int part = 0; part < 3; ++part) {
          const f32x2 v = *(const LAS f32x2*)(smem + q * HL + 10240 + ((g * 3 + part) * 16 + tok) * 8);
          su += v.x; sq += v.y;
        }
      const float mean = su * (1.0f / DM);
      const float var = fmaxf(sq * (1.0f / DM) - mean * mean, 0.f);
      st[lane] = f32x2{mean, 1.0f / sqrtf(var + LN_EPS)};
      if (lane == 0) *(volatile LAS unsigned*)(cnt + 6) = 1u;
    }
#pragma unroll
    for (int i = 0; i < 4; ++i) { acc[i][0] = f32x4{0, 0, 0, 0}; acc[i][1] = f32x4{0, 0, 0, 0}; }
    v4u bf[2][4];
    bfrag(0, bf[0]);
#pragma unroll
    for (int ks = 0; ks < 20; ++ks) {
      if (ks + 1 < 20) bfrag(ks + 1, bf[(ks + 1) & 1]);
#pragma unroll
      for (int mt = 0; mt < 4; ++mt) {
        acc[mt][0] = mfma16(w1f[ks][0], bf[ks & 1][mt], acc[mt][0]);
        acc[mt][1] = mfma16(w1f[ks][1], bf[ks & 1][mt], acc[mt][1]);
      }
      if (ks >= W1M) { accC[0] = mfma16(w1f[ks][0], ONES, accC[0]); accC[1] = mfma16(w1f[ks][1], ONES, accC[1]); }
      SBAR();
      if (ks < 20 - W1M) { w1f[W1M + ks][0] = gld128(rs, w1base + (W1M + ks) * 1024); w1f[W1M + ks][1] = gld128(rs, w1base + (20 + W1M + ks) * 1024); SBAR(); }
      if (ks >= 6 && ks < 6 + W2L) glds16_sc1(p.ws + (size_t)(w2off(ks - 6) + lane * 16), lds0 + W2LOFF + (w * W2L + ks - 6) * 1024);
      if (ks >= 6 && ks < 19) {
#pragma unroll
        for (int e = 0; e < (ks >= 13 ? 2 : 1); ++e) { const int f = ks < 13 ? W2L + (ks - 6) : W2L + 7 + 2 * (ks - 13) + e; w2f[f] = gld128(rs, w2off(f) + lane * 16); }
        SBAR();
      }
    }
    while (*(volatile LAS unsigned*)(cnt + 6) == 0u) __builtin_amdgcn_s_sleep(1);
    asm volatile("" ::: "memory");
    {
#pragma unroll
      for (int mt = 0; mt < 4; ++mt) {
        const f32x2 ms = st[mt * 16 + tok];
        float v[8];
#pragma unroll
        for (int e = 0; e < 2; ++e)
#pragma unroll
          for (int r = 0; r < 4; ++r) v[4 * e + r] = acc[mt][e][r] - ms.x * accC[e][r];
        gst128(rs, gemm ? WS_H1 + (((pn * 8 + j) * 7 + w) * 4 + mt) * 1024 + lane * 16 : 0x7F000000, v4u{relu2(pk2(v[0], v[1])), relu2(pk2(v[2], v[3])), relu2(pk2(v[4], v[5])), relu2(pk2(v[6], v[7]))});
      }
    }
    publish(3, 8u, WS_F4 + (pn * 8 + j) * FL_STRIDE);
    LBAR();
  }

  {
    const bool full = w < 6;
    int lz = lane; asm volatile("" : "+v"(lz));
    v4u hres; v2u hres2;
#pragma unroll
    for (int f = 26; f < 35; ++f) w2f[f] = gld128(rs, (f < 30 || full ? w2off(f) : 0x7F000000) + lane * 16);
    SBAR();
    poll_flags(p.ws, WS_F4 + (pn * 8 + w) * FL_STRIDE, lane == 0, tag);
    const int bbase = WS_H1 + (pn * 8 + w) * 28672 + lane * 16;
    v4u bf[2][7][2];
#pragma unroll
    for (int q = 0; q < 7; ++q) {
      const int ks = (q + 2) % 7;
#pragma unroll
      for (int ml = 0; ml < 2; ++ml) bf[0][ks][ml] = gld128(rs, bbase + ((ks < 6 || full ? ks : 0) * 4 + ml) * 1024);
    }
    SBAR();
#pragma unroll
    for (int hp = 0; hp < 2; ++hp) {
      f32x4 acc[2][5];
#pragma unroll
      for (int ml = 0; ml < 2; ++ml)
#pragma unroll
        for (int nt = 0; nt < 5; ++nt) acc[ml][nt] = f32x4{0, 0, 0, 0};
#pragma unroll
      for (int q = 0; q < 7; ++q) {
        const int ks = (q + 2) % 7;
        if (q == 4 || q == 5) {
#pragma unroll
          for (int f = 5 * (q - 4); f < 5 * (q - 3); ++f) if (f < W2L) w2f[f] = lds128(L + W2LOFF + (w * W2L + f) * 1024 + lane * 16);
          SBAR();
        }
#pragma unroll
        for (int ml = 0; ml < 2; ++ml)
#pragma unroll
          for (int nt = 0; nt < 5; ++nt) acc[ml][nt] = mfma16(w2f[ks * 5 + nt], bf[hp][ks][ml], acc[ml][nt]);
        SBAR();
        if (hp == 0) {
#pragma unroll
          for (int ml = 0; ml < 2; ++ml) bf[1][ks][ml] = gld128(rs, bbase + ((ks < 6 || full ? ks : 0) * 4 + 2 + ml) * 1024);
          SBAR();
        }
      }
      red_write(lz, hp, acc);
      SBAR();
      if (hp == 0) {
        asm volatile("" ::: "memory");
        const int base = WS_H + (pn * 8 + j) * HB;
        hres = gld128(rs, base + (w >> 1) * 2560 + (w & 1) * 1024 + lz * 16);
        hres2 = gld64(rs, base + (w & 3) * 2560 + 2048 + lz * 8);
        SBAR();
      }
    }
    LBAR();
    {
      f32x4 s0 = {0, 0, 0, 0}, s1 = s0, s2 = s0;
      red_sum(lz, s0, s1, s2);
      const LAS f32x2* st = (const LAS f32x2*)(smem + 8 * 11776);
      const float r1 = st[16 * (w >> 1) + (lz & 15)].y, r2 = st[16 * (w & 3) + (lz & 15)].y;
      char* dst = p.out + (size_t)((64 * pn + 16 * (w >> 1) + (lz & 15)) * 1280 + (80 * j + 32 * (w & 1) + 4 * (lz >> 4)) * 2);
      *(GAS v2u*)dst = v2u{pk2(fmaf(s0[0], r1, bflo(hres.x)), fmaf(s0[1], r1, bfhi(hres.x))), pk2(fmaf(s0[2], r1, bflo(hres.y)), fmaf(s0[3], r1, bfhi(hres.y)))};
      *(GAS v2u*)(dst + 32) = v2u{pk2(fmaf(s1[0], r1, bflo(hres.z)), fmaf(s1[1], r1, bfhi(hres.z))), pk2(fmaf(s1[2], r1, bflo(hres.w)), fmaf(s1[3], r1, bfhi(hres.w)))};
      if (w < 4) *(GAS v2u*)(p.out + (size_t)((64 * pn + 16 * w + (lz & 15)) * 1280 + (80 * j + 64 + 4 * (lz >> 4)) * 2)) =
          v2u{pk2(fmaf(s2[0], r2, bflo(hres2.x)), fmaf(s2[1], r2, bfhi(hres2.x))), pk2(fmaf(s2[2], r2, bflo(hres2.y)), fmaf(s2[3], r2, bfhi(hres2.y)))};
    }
  }
  if (b == 0 && tid == 0) set_flag(p.ws, WS_EPOCH, tag);
  asm volatile("mk_code_end:");
}
}

extern "C" void kernel_launch(void* const* d_in, const int* in_sizes, int n_in,
                              void* d_out, int out_size, void* d_ws, size_t ws_size,
                              hipStream_t stream) {
  (void)in_sizes; (void)n_in; (void)out_size; (void)ws_size;
  Params p;
  p.x = (const char*)d_in[0]; p.wqkv = (const char*)d_in[1]; p.wo = (const char*)d_in[2];
  p.w1 = (const char*)d_in[4]; p.w2 = (const char*)d_in[5];
  p.out = (char*)d_out; p.ws = (char*)d_ws;
  (void)hipFuncSetAttribute((const void*)block_kernel, hipFuncAttributeMaxDynamicSharedMemorySize, LDS_BYTES);
  block_kernel<<<256, 512, LDS_BYTES, stream>>>(p);
}
```
